```python
import torch

def _softmax(x, axis=-1):
    return torch.softmax(x, dim=axis)
def _gelu(x):
    return torch.nn.functional.gelu(x, approximate="tanh")
def _silu(x):
    return torch.nn.functional.silu(x)
def _sigmoid(x):
    return torch.sigmoid(x)
def _layernorm(x, axis=-1, eps=1e-5):
    m = x.mean(dim=axis, keepdim=True)
    v = x.var(dim=axis, keepdim=True, unbiased=False)
    return (x - m) / torch.sqrt(v + eps)
def _scatter_add(tgt, idx, src, axis=0):
    return tgt.clone().index_add_(axis, idx, src)
def _segment_sum(src, seg, n):
    out = torch.zeros((n,) + src.shape[1:], dtype=src.dtype)
    return out.index_add_(0, seg, src)
def _pad(x, pw):
    flat = tuple(v for pair in reversed(pw) for v in pair)
    return torch.nn.functional.pad(x, flat)

kernel_name = 'gated_mix__ssm_conv_s5__glu_gated_glu__prompt'

def setup_inputs():

    _w = (lambda a: (lambda t: t.to(torch.bfloat16) if t.is_floating_point() else t)(torch.as_tensor(a)))
    def _A_setup():
        B, S, D = 1, 2048, 512
        P = D
        Lambda_re = -0.5 * (lambda _x: torch.exp(torch.as_tensor(_x)))((lambda *s: torch.randn(*s, dtype=torch.bfloat16) * 0.4)(P))
        Lambda_im = (lambda *s: torch.randn(*s, dtype=torch.bfloat16) * 0.8)(P) * 1.0
        B_re = (lambda *s: torch.randn(*s, dtype=torch.bfloat16) * 0.8)(P, D) * (D ** -0.5)
        B_im = (lambda *s: torch.randn(*s, dtype=torch.bfloat16) * 0.8)(P, D) * (D ** -0.5)
        C_re = (lambda *s: torch.randn(*s, dtype=torch.bfloat16) * 0.8)(D, P) * (P ** -0.5)
        C_im = (lambda *s: torch.randn(*s, dtype=torch.bfloat16) * 0.8)(D, P) * (P ** -0.5)
        log_dt = (lambda *s: torch.randn(*s, dtype=torch.bfloat16) * 0.4)(P) * 0.3 - 2.0
        Dskip = (lambda *s: torch.randn(*s, dtype=torch.bfloat16) * 0.1)(D)
        return {
            "x": _w((lambda *s: torch.randn(*s, dtype=torch.bfloat16) * 0.05)(B, S, D)),
            "Lambda_re": _w(Lambda_re),
            "Lambda_im": _w(Lambda_im),
            "B_re": _w(B_re),
            "B_im": _w(B_im),
            "C_re": _w(C_re),
            "C_im": _w(C_im),
            "log_dt": _w(log_dt),
            "Dskip": _w(Dskip),
        }
    _A_in = _A_setup()

    def _B_setup():
        B, S, D, F = 1, 2048, 512, 1792
        return {
            "x": _w((lambda *s: torch.randn(*s, dtype=torch.bfloat16) * 0.05)(B, S, D)),
            "w_gate": _w((lambda *s: torch.randn(*s, dtype=torch.bfloat16) * 0.4)(D, F)),
            "w_up": _w((lambda *s: torch.randn(*s, dtype=torch.bfloat16) * 0.05)(D, F)),
            "w_down": _w((lambda *s: torch.randn(*s, dtype=torch.bfloat16) * 0.05)(F, D)),
        }
    _B_in = _B_setup()

    return {
        "A_x": _A_in["x"],
        "A_Lambda_re": _A_in["Lambda_re"],
        "A_Lambda_im": _A_in["Lambda_im"],
        "A_B_re": _A_in["B_re"],
        "A_B_im": _A_in["B_im"],
        "A_C_re": _A_in["C_re"],
        "A_C_im": _A_in["C_im"],
        "A_log_dt": _A_in["log_dt"],
        "A_Dskip": _A_in["Dskip"],
        "B_x": _B_in["x"],
        "B_w_gate": _B_in["w_gate"],
        "B_w_up": _B_in["w_up"],
        "B_w_down": _B_in["w_down"],
        "gate_w": _w((lambda *s: torch.randn(*s, dtype=torch.bfloat16) * 0.05)(512) * 16.0),
    }

def reference(A_x, A_Lambda_re, A_Lambda_im, A_B_re, A_B_im, A_C_re, A_C_im, A_log_dt, A_Dskip, B_x, B_w_gate, B_w_up, B_w_down, gate_w):
    def _A_ref(x, Lambda_re, Lambda_im, B_re, B_im, C_re, C_im, log_dt, Dskip):
        Bsz, L, H = x.shape
        Lam = Lambda_re + 1j * Lambda_im
        Bmat = B_re + 1j * B_im
        Cmat = C_re + 1j * C_im
        dt = torch.exp(log_dt)
        Lam_bar = torch.exp(dt * Lam)
        B_bar = ((Lam_bar - 1.0) / Lam)[:, None] * Bmat
        Bu = torch.einsum("bld,pd->blp", x + 0j, B_bar)
        h = torch.zeros_like(Bu[:, 0, :])
        hs = []
        for t in range(L):
            h = Lam_bar[None, :] * h + Bu[:, t, :]
            hs.append(h)
        hseq = torch.stack(hs, 1)
        y = 2.0 * torch.einsum("blp,dp->bld", hseq, Cmat).real + x * Dskip[None, None, :]
        return y

    def _B_ref(x, w_gate, w_up, w_down):
        g = torch.einsum("bsd,df->bsf", x, w_gate)
        u = torch.einsum("bsd,df->bsf", x, w_up)
        h = _sigmoid(g) * u
        return torch.einsum("bsf,fd->bsd", h, w_down)

    x = A_x
    g = _sigmoid((x * gate_w).sum(-1, keepdims=True))
    a = _A_ref(x, A_Lambda_re, A_Lambda_im, A_B_re, A_B_im, A_C_re, A_C_im, A_log_dt, A_Dskip)
    b = _B_ref(x, B_w_gate, B_w_up, B_w_down)
    return g * a + (1 - g) * b
```

```cpp
#include <hip/hip_runtime.h>
#include <hip/hip_bf16.h>
typedef unsigned short u16;
__device__ __forceinline__ float bf2f(u16 v){ return __uint_as_float(((unsigned)v)<<16); }
__device__ __forceinline__ float rbf(float f){ return __bfloat162float(__float2bfloat16(f)); }
constexpr int L=2048, D=512, P=512, F=1792;

__global__ void k_gemm1(const u16* x, const u16* wg, const u16* wu, const u16* bre, const u16* bim, float* hglu, float* vre, float* vim){
  int n = blockIdx.x*256+threadIdx.x;
  int t = blockIdx.y;
  __shared__ float xs[D];
  for(int i=threadIdx.x;i<D;i+=256) xs[i]=bf2f(x[t*D+i]);
  __syncthreads();
  if(n<F){
    float g=0,u=0;
    for(int k=0;k<D;k++){ g+=xs[k]*bf2f(wg[k*F+n]); u+=xs[k]*bf2f(wu[k*F+n]); }
    g=rbf(g); u=rbf(u);
    float s=rbf(1.f/(1.f+__expf(-g)));
    hglu[t*F+n]=rbf(s*u);
  } else if(n<F+P){
    int p=n-F; float r=0,i=0;
    for(int k=0;k<D;k++){ r+=xs[k]*bf2f(bre[p*D+k]); i+=xs[k]*bf2f(bim[p*D+k]); }
    vre[t*P+p]=r; vim[t*P+p]=i;
  }
}
__global__ void k_scan(const u16* lre, const u16* lim, const u16* ldt, float* vre, float* vim){
  int p = blockIdx.x*64+threadIdx.x;
  float dt=rbf(expf(bf2f(ldt[p])));
  float a=bf2f(lre[p]), b=bf2f(lim[p]);
  float m=expf(dt*a); float lr=m*cosf(dt*b), li=m*sinf(dt*b);
  float nr=lr-1.f, ni=li; float den=a*a+b*b;
  float cr=(nr*a+ni*b)/den, ci=(ni*a-nr*b)/den;
  float hr=0,hi=0;
  for(int t=0;t<L;t++){
    float r=vre[t*P+p], i=vim[t*P+p];
    float ur=cr*r-ci*i, ui=cr*i+ci*r;
    float nhr=lr*hr-li*hi+ur, nhi=lr*hi+li*hr+ui;
    hr=nhr; hi=nhi;
    vre[t*P+p]=rbf(hr); vim[t*P+p]=rbf(hi);
  }
}
__global__ void k_gemm2(const u16* x, const u16* gw, const u16* dsk, const u16* cre, const u16* cim, const u16* wd, const float* hglu, const float* hre, const float* him, float* out){
  int d = blockIdx.x*256+threadIdx.x;
  int t = blockIdx.y;
  float s=0;
  for(int k=0;k<D;k++) s+=rbf(bf2f(x[t*D+k])*bf2f(gw[k]));
  s=rbf(s);
  float g=rbf(1.f/(1.f+__expf(-s)));
  float b=0;
  for(int k=0;k<F;k++) b+=hglu[t*F+k]*bf2f(wd[k*D+d]);
  b=rbf(b);
  float a=0;
  for(int k=0;k<P;k++) a+=hre[t*P+k]*bf2f(cre[d*P+k])-him[t*P+k]*bf2f(cim[d*P+k]);
  a=2.f*a+rbf(bf2f(x[t*D+d])*bf2f(dsk[d]));
  out[t*D+d]=g*a+rbf(rbf(1.f-g)*b);
}
extern "C" void kernel_launch(void* const* d_in, const int* in_sizes, int n_in, void* d_out, int out_size, void* d_ws, size_t ws_size, hipStream_t stream) {
  const u16* x=(const u16*)d_in[0]; const u16* lre=(const u16*)d_in[1]; const u16* lim=(const u16*)d_in[2];
  const u16* bre=(const u16*)d_in[3]; const u16* bim=(const u16*)d_in[4]; const u16* cre=(const u16*)d_in[5]; const u16* cim=(const u16*)d_in[6];
  const u16* ldt=(const u16*)d_in[7]; const u16* dsk=(const u16*)d_in[8];
  const u16* wg=(const u16*)d_in[10]; const u16* wu=(const u16*)d_in[11]; const u16* wd=(const u16*)d_in[12]; const u16* gw=(const u16*)d_in[13];
  float* hglu=(float*)d_ws; float* vre=hglu+L*F; float* vim=vre+L*P;
  k_gemm1<<<dim3((F+P)/256,L),256,0,stream>>>(x,wg,wu,bre,bim,hglu,vre,vim);
  k_scan<<<P/64,64,0,stream>>>(lre,lim,ldt,vre,vim);
  k_gemm2<<<dim3(D/256,L),256,0,stream>>>(x,gw,dsk,cre,cim,wd,hglu,vre,vim,(float*)d_out);
}
```

```cpp
#include <hip/hip_runtime.h>
#include <stdint.h>

typedef unsigned short u16;
typedef __bf16 bf16x8 __attribute__((ext_vector_type(8)));
typedef __bf16 bf16x4 __attribute__((ext_vector_type(4)));
typedef __bf16 bf16x2 __attribute__((ext_vector_type(2)));
typedef float f32x4 __attribute__((ext_vector_type(4)));
typedef float f32x2 __attribute__((ext_vector_type(2)));
typedef unsigned u32x4 __attribute__((ext_vector_type(4)));
typedef unsigned u32x2 __attribute__((ext_vector_type(2)));
#define LAS __attribute__((address_space(3)))
#define GAS __attribute__((address_space(1)))
#define RLX_AGENT __ATOMIC_RELAXED, __HIP_MEMORY_SCOPE_AGENT
typedef GAS unsigned gu32;
typedef GAS unsigned long long gu64;

constexpr int L = 2048, D = 512, P = 512, F = 1792;
constexpr int KS2 = 88;
constexpr int SLOT = 53248;
constexpr int LDS_BYTES = 163840;
constexpr size_t H_OFF = 0, H_BYTES = (size_t)32 * KS2 * 4096;
constexpr size_t E_OFF = H_OFF + H_BYTES, E_BYTES = (size_t)8 * 512 * 2 * 8;
constexpr size_t G_OFF = E_OFF + E_BYTES;

#define BARRIER() asm volatile("s_waitcnt lgkmcnt(0)\n\ts_barrier" ::: "memory")
#define WAITV(n) asm volatile("s_waitcnt vmcnt(" #n ")" ::: "memory")
#define SCHED() __builtin_amdgcn_sched_barrier(0)

__device__ __forceinline__ float bf2f(u16 v) { return __uint_as_float(((unsigned)v) << 16); }
__device__ __forceinline__ float rbf(float f) { return (float)(__bf16)f; }
__device__ __forceinline__ unsigned pack2(float a, float b) {
  bf16x2 v = {(__bf16)a, (__bf16)b};
  return __builtin_bit_cast(unsigned, v);
}
__device__ __forceinline__ __amdgpu_buffer_rsrc_t mkrsrc(const void* p, unsigned bytes) {
  return __builtin_amdgcn_make_buffer_rsrc((void*)p, 0, bytes, 0x00020000);
}
__device__ __forceinline__ void dma16(__amdgpu_buffer_rsrc_t r, LAS char* dst, unsigned voff, unsigned soff) {
  __builtin_amdgcn_raw_ptr_buffer_load_lds(r, (LAS void*)dst, 16, voff, soff, 0, 0);
}
__device__ __forceinline__ bf16x8 ld128(LAS char* p) { return *(LAS bf16x8*)p; }
__device__ __forceinline__ bf16x8 ldtr(LAS char* p0, LAS char* p1) {
  bf16x4 a = __builtin_amdgcn_ds_read_tr16_b64_v4bf16((LAS bf16x4*)p0);
  bf16x4 b = __builtin_amdgcn_ds_read_tr16_b64_v4bf16((LAS bf16x4*)p1);
  return __builtin_shufflevector(a, b, 0, 1, 2, 3, 4, 5, 6, 7);
}
__device__ __forceinline__ float sigmoidf_(float g) {
  return __builtin_amdgcn_rcpf(1.0f + __builtin_amdgcn_exp2f(-1.4426950408889634f * g));
}

struct Params {
  const u16 *x, *lre, *lim, *bre, *bim, *cre, *cim, *ldt, *dsk, *wg, *wu, *wd, *gw;
  char* ws;
  float* out;
};

__device__ __forceinline__ void ssm_consts(const Params& p, int ch, float& lr, float& li, float& cr, float& ci) {
  const float dt = rbf(expf(bf2f(p.ldt[ch])));
  const float a = bf2f(p.lre[ch]), b = bf2f(p.lim[ch]);
  const float m = expf(dt * a);
  float s, c;
  sincosf(dt * b, &s, &c);
  lr = m * c;
  li = m * s;
  const float nr = lr - 1.0f, ni = li, den = a * a + b * b;
  cr = (nr * a + ni * b) / den;
  ci = (ni * a - nr * b) / den;
}

template <bool GLU, int NR>
__device__ __forceinline__ void main_loop(LAS char* lds, int wr, int wc, int lane, f32x4 (&acc)[8][NR]) {
  const int i = lane & 15, g = lane >> 4;
  const int sw = (i >> 1) & 7;
  unsigned resA[2], rolA[4];
  if constexpr (GLU) {
    resA[0] = (wr * 80 + i) * 128 + ((g ^ sw) << 4);
    resA[1] = resA[0] ^ 64;
    const int q = (lane & 15) >> 2, pp = lane & 3;
    const int swz = q | ((g & 1) << 2);
#pragma unroll
    for (int c = 0; c < 4; c++) rolA[c] = 20480 + (8 * g + q) * 256 + (((wc * 4 + c) ^ swz) << 5) + pp * 8;
  } else {
    resA[0] = 32768 + (wc * 32 + i) * 128 + ((g ^ sw) << 4);
    resA[1] = resA[0] ^ 64;
    rolA[0] = (wr * 128 + i) * 128 + ((g ^ sw) << 4);
    rolA[1] = rolA[0] ^ 64;
    rolA[2] = rolA[3] = 0;
  }
  auto ld_res = [&](int ks, int r) -> bf16x8 {
    if constexpr (GLU) return ld128(lds + resA[ks] + r * 2048);
    else return ld128(lds + resA[ks] + (r & 1) * 2048 + (r >> 1) * 8192);
  };
  auto ld_rol = [&](int j) -> bf16x8 {
    const int ks = j >> 3, c = j & 7;
    if constexpr (GLU) {
      LAS char* b = lds + rolA[c & 3] + (c >> 2) * 16384 + ks * 8192;
      return ldtr(b, b + 1024);
    } else {
      return ld128(lds + rolA[ks] + c * 2048);
    }
  };
  bf16x8 res[2][NR], rol[4];
  BARRIER();
#pragma unroll
  for (int r = 0; r < NR; r++) res[0][r] = ld_res(0, r);
#pragma unroll
  for (int j = 0; j < 3; j++) rol[j] = ld_rol(j);
  SCHED();
  int slot = 0;
#pragma unroll 1
  for (int s = 0; s < 8; s++) {
#pragma unroll
    for (int j = 0; j < 16; j++) {
      if (j == 13) {
        BARRIER();
        const int delta = (slot == 2) ? -2 * SLOT : SLOT;
        slot = (slot == 2) ? 0 : slot + 1;
        resA[0] += delta; resA[1] += delta;
        rolA[0] += delta; rolA[1] += delta;
        if constexpr (GLU) { rolA[2] += delta; rolA[3] += delta; }
      }
      rol[(j + 3) & 3] = ld_rol((j + 3) & 15);
      if (j >= 2 && j < 2 + NR) res[1][j - 2] = ld_res(1, j - 2);
      if (j == 13) { res[0][0] = ld_res(0, 0); res[0][1] = ld_res(0, 1); }
      if (j == 14) { res[0][2] = ld_res(0, 2); res[0][3] = ld_res(0, 3); }
      if (j == 15 && NR > 4) res[0][NR - 1] = ld_res(0, NR - 1);
#pragma unroll
      for (int r = 0; r < NR; r++)
        acc[j & 7][r] = __builtin_amdgcn_mfma_f32_16x16x32_bf16(rol[j & 3], res[j >> 3][r], acc[j & 7][r], 0, 0, 0);
      SCHED();
    }
  }
}

__global__ __launch_bounds__(512, 2) void k_phase1(Params p) {
  extern __shared__ __attribute__((aligned(16))) char smem[];
  LAS char* lds = (LAS char*)smem;
  const int tid = threadIdx.x, lane = tid & 63;
  const int wid = __builtin_amdgcn_readfirstlane(tid >> 6);
  const int xg = blockIdx.x & 7, idx = blockIdx.x >> 3;
  int role, rt = 0, ct = 0;
  if (xg < 2) {
    role = 1; rt = xg * 4 + (idx >> 3); ct = idx & 7;
  } else {
    const int k = xg - 2, cg = k / 3, k3 = k - cg * 3;
    const int start = k3 == 0 ? 0 : (k3 == 1 ? 31 : 61), cnt = k3 == 0 ? 31 : 30;
    if (idx < cnt) {
      role = 0; const int lin = start + idx; rt = lin / 7; ct = cg * 7 + (lin - rt * 7);
    } else {
      const int e = cg * 5 + (k3 == 0 ? 0 : (k3 == 1 ? 1 : 3)) + (idx - cnt);
      role = e < 8 ? 2 : 3; rt = e;
    }
  }
  const __amdgpu_buffer_rsrc_t hr = mkrsrc(p.ws + H_OFF, (unsigned)H_BYTES);

  if (role == 0) {
    const int r0 = min(rt * 160, L - 160);
    if (wid >= 4) {
      const int lw = wid - 4;
      const __amdgpu_buffer_rsrc_t xr = mkrsrc(p.x, L * D * 2), gr = mkrsrc(p.wg, D * F * 2), ur = mkrsrc(p.wu, D * F * 2);
      const int xrow = 8 * lw + (lane >> 3);
      const unsigned vx = (r0 + xrow) * 1024 + (((lane & 7) ^ ((xrow >> 1) & 7)) << 4);
      const int krow = 4 * lw + (lane >> 4);
      const int swz = (krow & 3) | (((krow >> 3) & 1) << 2);
      const unsigned vw = krow * (F * 2) + ct * 256 + (((lane & 15) ^ (swz << 1)) << 4);
      auto stage = [&](int s, int slot) {
        LAS char* b = lds + slot * SLOT + lw * 1024;
#pragma unroll
        for (int i = 0; i < 5; i++) dma16(xr, b + i * 4096, vx, s * 128 + i * 32768);
#pragma unroll
        for (int i = 0; i < 4; i++) {
          dma16(gr, b + 20480 + i * 4096, vw, (s * 64 + i * 16) * (F * 2));
          dma16(ur, b + 36864 + i * 4096, vw, (s * 64 + i * 16) * (F * 2));
        }
      };
      stage(0, 0); stage(1, 1);
      WAITV(13); BARRIER();
      int slot = 2;
#pragma unroll 1
      for (int s = 1; s < 7; s++) {
        stage(s + 1, slot); slot = slot == 2 ? 0 : slot + 1;
        WAITV(13); BARRIER();
      }
      WAITV(0); BARRIER();
      BARRIER();
    } else {
      const int wr = wid >> 1, wc = wid & 1;
      f32x4 acc[8][5];
#pragma unroll
      for (int c = 0; c < 8; c++)
#pragma unroll
        for (int m = 0; m < 5; m++) acc[c][m] = (f32x4){0.f, 0.f, 0.f, 0.f};
      main_loop<true, 5>(lds, wr, wc, lane, acc);
      const int mt0 = (r0 >> 4) + wr * 5;
#pragma unroll
      for (int m = 0; m < 5; m++) {
        const int mt = mt0 + m;
#pragma unroll
        for (int h = 0; h < 2; h++) {
          float v[8];
#pragma unroll
          for (int e = 0; e < 8; e++) {
            const int c = 2 * h + (e >> 2);
            v[e] = sigmoidf_(acc[c][m][e & 3]) * acc[4 + c][m][e & 3];
          }
          u32x4 o = {pack2(v[0], v[1]), pack2(v[2], v[3]), pack2(v[4], v[5]), pack2(v[6], v[7])};
          const int ks = ct * 4 + wc * 2 + h;
          __builtin_amdgcn_raw_buffer_store_b128(o, hr, lane * 16, (((mt >> 2) * KS2 + ks) * 4 + (mt & 3)) * 1024, 0);
        }
      }
    }
  } else if (role == 1) {
    const int r0 = rt * 256;
    LAS f32x2* eloc = (LAS f32x2*)(lds + 3 * SLOT);
    if (wid >= 4) {
      const int lw = wid - 4;
      const __amdgpu_buffer_rsrc_t xr = mkrsrc(p.x, L * D * 2), br = mkrsrc(p.bre, P * D * 2), bi = mkrsrc(p.bim, P * D * 2);
      const int ii = 8 * (lw & 1) + (lane >> 3);
      const int csw = ((lane & 7) ^ ((ii >> 1) & 7)) << 4;
      const unsigned vx = (r0 + 32 * (ii >> 2) + (ii & 3) + 4 * (lw >> 1)) * 1024 + csw;
      const unsigned vb = (ct * 64 + 8 * lw + (lane >> 3)) * 1024 + csw;
      auto stage = [&](int s, int slot) {
        LAS char* b = lds + slot * SLOT + lw * 1024;
#pragma unroll
        for (int i = 0; i < 2; i++) {
          dma16(br, b + 32768 + i * 4096, vb, s * 128 + i * 32768);
          dma16(bi, b + 40960 + i * 4096, vb, s * 128 + i * 32768);
        }
#pragma unroll
        for (int i = 0; i < 8; i++) dma16(xr, b + i * 4096, vx, s * 128 + (128 * (i >> 2) + 8 * (i & 3)) * 1024);
      };
      stage(0, 0); stage(1, 1);
      float lr = 0.f, li = 0.f, cr, ci;
      if (lw == 0) ssm_consts(p, ct * 64 + lane, lr, li, cr, ci);
      WAITV(12); BARRIER();
      int slot = 2;
#pragma unroll 1
      for (int s = 1; s < 7; s++) {
        stage(s + 1, slot); slot = slot == 2 ? 0 : slot + 1;
        WAITV(12); BARRIER();
      }
      WAITV(0); BARRIER();
      BARRIER();
#pragma unroll
      for (int k = 0; k < 5; k++) { const float t = lr * lr - li * li; li = 2.f * lr * li; lr = t; }
      float mr = lr, mi = li;
#pragma unroll
      for (int k = 0; k < 3; k++) { const float t = mr * mr - mi * mi; mi = 2.f * mr * mi; mr = t; }
      BARRIER();
      if (lw == 0) {
        f32x2 e[8];
#pragma unroll
        for (int s = 0; s < 8; s++) e[s] = eloc[s * 64 + lane];
        float ar = e[0].x, ai = e[0].y;
#pragma unroll
        for (int s = 1; s < 8; s++) { const float t = lr * ar - li * ai + e[s].x; ai = lr * ai + li * ar + e[s].y; ar = t; }
        gu64* eg = (gu64*)(p.ws + E_OFF);
        const unsigned long long tag = 1ull << 32;
        const int ch = ct * 64 + lane;
        __hip_atomic_store(eg + (rt * 2 + 0) * 512 + ch, tag | __float_as_uint(ar), RLX_AGENT);
        __hip_atomic_store(eg + (rt * 2 + 1) * 512 + ch, tag | __float_as_uint(ai), RLX_AGENT);
        float vr[7], vi[7];
        for (unsigned spins = 0; spins < (1u << 20); spins++) {
          bool ok = true;
#pragma unroll
          for (int c = 0; c < 7; c++) {
            const int cc = c < rt ? c : rt;
            const unsigned long long a = __hip_atomic_load(eg + (cc * 2 + 0) * 512 + ch, RLX_AGENT);
            const unsigned long long b = __hip_atomic_load(eg + (cc * 2 + 1) * 512 + ch, RLX_AGENT);
            ok &= (a >> 32) == (tag >> 32) && (b >> 32) == (tag >> 32);
            vr[c] = __uint_as_float((unsigned)a); vi[c] = __uint_as_float((unsigned)b);
          }
          if (__all(ok)) break;
          __builtin_amdgcn_s_sleep(1);
        }
        float hr_ = 0.f, hi_ = 0.f;
#pragma unroll
        for (int c = 0; c < 7; c++)
          if (c < rt) { const float t = mr * hr_ - mi * hi_ + vr[c]; hi_ = mr * hi_ + mi * hr_ + vi[c]; hr_ = t; }
#pragma unroll
        for (int s = 0; s < 8; s++) {
          eloc[s * 64 + lane] = (f32x2){hr_, hi_};
          const float t = lr * hr_ - li * hi_ + e[s].x; hi_ = lr * hi_ + li * hr_ + e[s].y; hr_ = t;
        }
      }
      BARRIER();
    } else {
      const int wr = wid >> 1, wc = wid & 1;
      const int n = lane & 15, q = lane >> 4;
      float lr[2], li[2], cr[2], ci[2];
#pragma unroll
      for (int u = 0; u < 2; u++) ssm_consts(p, ct * 64 + wc * 32 + u * 16 + n, lr[u], li[u], cr[u], ci[u]);
      f32x4 acc[8][4];
#pragma unroll
      for (int m = 0; m < 8; m++)
#pragma unroll
        for (int c = 0; c < 4; c++) acc[m][c] = (f32x4){0.f, 0.f, 0.f, 0.f};
      main_loop<false, 4>(lds, wr, wc, lane, acc);
#pragma unroll
      for (int u = 0; u < 2; u++) {
        float er = 0.f, ei = 0.f;
#pragma unroll
        for (int m = 0; m < 8; m++)
#pragma unroll
          for (int r = 0; r < 4; r++) {
            const float t = lr[u] * er - li[u] * ei + acc[m][u][r];
            ei = lr[u] * ei + li[u] * er + acc[m][2 + u][r];
            er = t;
          }
        eloc[(wr * 4 + q) * 64 + wc * 32 + u * 16 + n] = (f32x2){er, ei};
      }
      BARRIER();
#pragma unroll
      for (int u = 0; u < 2; u++) {
        cr[u] *= 2.f; ci[u] *= 2.f;
#pragma unroll
        for (int m = 0; m < 8; m++)
#pragma unroll
          for (int r = 0; r < 4; r++) {
            const float a = acc[m][u][r], b = acc[m][2 + u][r];
            acc[m][u][r] = cr[u] * a - ci[u] * b;
            acc[m][2 + u][r] = cr[u] * b + ci[u] * a;
          }
      }
      BARRIER();
      LAS char* tb = lds + wid * 17408;
#pragma unroll
      for (int u = 0; u < 2; u++) {
        const f32x2 c0 = eloc[(wr * 4 + q) * 64 + wc * 32 + u * 16 + n];
        float hr_ = cr[u] * c0.x - ci[u] * c0.y, hi_ = cr[u] * c0.y + ci[u] * c0.x;
#pragma unroll
        for (int mm = 0; mm < 4; mm++) {
          float a[8], b[8];
#pragma unroll
          for (int e = 0; e < 8; e++) {
            const int m = mm * 2 + (e >> 2), r = e & 3;
            const float t = lr[u] * hr_ - li[u] * hi_ + acc[m][u][r];
            hi_ = lr[u] * hi_ + li[u] * hr_ + acc[m][2 + u][r];
            hr_ = t;
            a[e] = hr_; b[e] = -hi_;
          }
          LAS char* w = tb + (u * 16 + n) * 272 + q * 64 + mm * 16;
          *(LAS u32x4*)w = (u32x4){pack2(a[0], a[1]), pack2(a[2], a[3]), pack2(a[4], a[5]), pack2(a[6], a[7])};
          *(LAS u32x4*)(w + 32 * 272) = (u32x4){pack2(b[0], b[1]), pack2(b[2], b[3]), pack2(b[4], b[5]), pack2(b[6], b[7])};
        }
      }
      asm volatile("s_waitcnt lgkmcnt(0)" ::: "memory");
      LAS char* rb = tb + (8 * q + (n >> 2)) * 272 + (n & 3) * 8;
      const int G = ct * 2 + wc;
#pragma unroll
      for (int part = 0; part < 2; part++)
#pragma unroll
        for (int m = 0; m < 8; m++) {
          LAS char* a = rb + part * 32 * 272 + m * 32;
          const bf16x8 v = ldtr(a, a + 4 * 272);
          const int mt = (r0 >> 4) + wr * 8 + m, ks = 56 + G * 2 + part;
          __builtin_amdgcn_raw_buffer_store_b128(__builtin_bit_cast(u32x4, v), hr, lane * 16,
                                                 (((mt >> 2) * KS2 + ks) * 4 + (mt & 3)) * 1024, 0);
        }
    }
  } else if (role == 2) {
    const int r0 = rt * 256 + wid * 32;
    const u32x4 gwv = *(const u32x4*)(p.gw + lane * 8);
    float gwf[8];
#pragma unroll
    for (int e = 0; e < 4; e++) { gwf[2 * e] = __uint_as_float(gwv[e] << 16); gwf[2 * e + 1] = __uint_as_float(gwv[e] & 0xffff0000u); }
    u32x4 xv[32];
#pragma unroll
    for (int r = 0; r < 32; r++) xv[r] = *(const u32x4*)(p.x + (size_t)(r0 + r) * D + lane * 8);
    float res = 0.f;
#pragma unroll
    for (int r = 0; r < 32; r++) {
      float s = 0.f;
#pragma unroll
      for (int e = 0; e < 4; e++) {
        s += rbf(__uint_as_float(xv[r][e] << 16) * gwf[2 * e]);
        s += rbf(__uint_as_float(xv[r][e] & 0xffff0000u) * gwf[2 * e + 1]);
      }
#pragma unroll
      for (int o = 32; o > 0; o >>= 1) s += __shfl_xor(s, o);
      if (lane == r) res = s;
    }
    if (lane < 32) ((float*)(p.ws + G_OFF))[r0 + lane] = rbf(sigmoidf_(rbf(res)));
  }
}

__global__ __launch_bounds__(512, 2) void k_phase2(Params p) {
  extern __shared__ __attribute__((aligned(16))) char smem[];
  LAS char* lds = (LAS char*)smem;
  const int tid = threadIdx.x, lane = tid & 63;
  const int wid = __builtin_amdgcn_readfirstlane(tid >> 6);
  const int xg = blockIdx.x & 7, idx = blockIdx.x >> 3;
  const int rb = (xg >> 1) * 8 + (idx >> 2), cb = (xg & 1) * 4 + (idx & 3);
  const __amdgpu_buffer_rsrc_t hr = mkrsrc(p.ws + H_OFF, (unsigned)H_BYTES);
  const int n = lane & 15, g = lane >> 4;
  f32x4 acc[4][4];
#pragma unroll
  for (int c = 0; c < 4; c++)
#pragma unroll
    for (int m = 0; m < 4; m++) acc[c][m] = (f32x4){0.f, 0.f, 0.f, 0.f};
  auto ldh = [&](int ks, int m) -> bf16x8 {
    return __builtin_bit_cast(bf16x8, __builtin_amdgcn_raw_buffer_load_b128(hr, lane * 16, ((rb * KS2 + ks) * 4 + m) * 1024, 0));
  };
  if (wid < 4) {
    const __amdgpu_buffer_rsrc_t wr_ = mkrsrc(p.wd, F * D * 2);
    LAS char* wb = lds + wid * 40960;
    const unsigned vw = (lane >> 3) * 1024 + cb * 128 + (((lane & 7) ^ (((lane >> 4) & 3) << 1)) << 4);
    const int ks0 = wid * 14;
    auto stagew = [&](int k, int slot) {
#pragma unroll
      for (int i = 0; i < 4; i++) dma16(wr_, wb + slot * 4096 + i * 1024, vw, ((ks0 + k) * 32 + i * 8) * 1024);
    };
    const int q = n >> 2, pp = n & 3, swz = (2 * g + (q >> 1)) & 3;
    unsigned ra[4];
#pragma unroll
    for (int c = 0; c < 4; c++) ra[c] = (4 * g + q) * 128 + ((c ^ swz) << 5) + pp * 8;
    auto stagea = [&](int k, int slot) {
#pragma unroll
      for (int m = 0; m < 4; m++) dma16(hr, wb + 20480 + slot * 4096 + m * 1024, lane * 16, ((rb * KS2 + ks0 + k) * 4 + m) * 1024);
    };
#pragma unroll
    for (int k = 0; k < 4; k++) { stagew(k, k); stagea(k, k); }
#pragma unroll
    for (int k = 0; k < 14; k++) {
      if (k + 4 < 14) { stagew(k + 4, (k + 4) % 5); stagea(k + 4, (k + 4) % 5); WAITV(32); }
      else if (k == 10) WAITV(24);
      else if (k == 11) WAITV(16);
      else if (k == 12) WAITV(8);
      else WAITV(0);
      const int slot = k % 5;
      bf16x8 hf[4], wf[4];
#pragma unroll
      for (int m = 0; m < 4; m++) hf[m] = ld128(wb + 20480 + slot * 4096 + m * 1024 + lane * 16);
#pragma unroll
      for (int c = 0; c < 4; c++) { LAS char* a = wb + slot * 4096 + ra[c]; wf[c] = ldtr(a, a + 2048); }
#pragma unroll
      for (int c = 0; c < 4; c++)
#pragma unroll
        for (int m = 0; m < 4; m++) acc[c][m] = __builtin_amdgcn_mfma_f32_16x16x32_bf16(wf[c], hf[m], acc[c][m], 0, 0, 0);
      asm volatile("s_waitcnt lgkmcnt(0)" ::: "memory");
    }
  } else {
    const int hw = wid - 4;
    const __amdgpu_buffer_rsrc_t cr_ = mkrsrc(p.cre, D * P * 2), ci_ = mkrsrc(p.cim, D * P * 2);
    const unsigned vc = (cb * 64 + n) * 1024 + hw * 256 + g * 16;
    bf16x8 cf[8][4], hf[3][4];
#pragma unroll
    for (int c = 0; c < 4; c++)
#pragma unroll
      for (int k = 0; k < 8; k++)
        cf[k][c] = __builtin_bit_cast(bf16x8, __builtin_amdgcn_raw_buffer_load_b128((k & 1) ? ci_ : cr_, vc, c * 16384 + (k >> 1) * 64, 0));
    const int ks0 = 56 + hw * 8;
#pragma unroll
    for (int k = 0; k < 3; k++)
#pragma unroll
      for (int m = 0; m < 4; m++) hf[k][m] = ldh(ks0 + k, m);
#pragma unroll
    for (int k = 0; k < 8; k++) {
#pragma unroll
      for (int c = 0; c < 4; c++)
#pragma unroll
        for (int m = 0; m < 4; m++) acc[c][m] = __builtin_amdgcn_mfma_f32_16x16x32_bf16(cf[k][c], hf[k % 3][m], acc[c][m], 0, 0, 0);
      if (k + 3 < 8) {
#pragma unroll
        for (int m = 0; m < 4; m++) hf[k % 3][m] = ldh(ks0 + k + 3, m);
      }
      SCHED();
    }
  }
  const int em = wid >> 1, ec = (wid & 1) * 2;
  const int t = rb * 64 + em * 16 + n;
  const float gv = ((const float*)(p.ws + G_OFF))[t];
  u32x2 xv[2], dv[2];
#pragma unroll
  for (int u = 0; u < 2; u++) {
    const int d = cb * 64 + (ec + u) * 16 + 4 * g;
    xv[u] = *(const u32x2*)(p.x + (size_t)t * D + d);
    dv[u] = *(const u32x2*)(p.dsk + d);
  }
  BARRIER();
#pragma unroll
  for (int c = 0; c < 4; c++)
#pragma unroll
    for (int m = 0; m < 4; m++) *(LAS f32x4*)(lds + ((wid * 16 + m * 4 + c) * 64 + lane) * 16) = acc[c][m];
  BARRIER();
  const float omg = rbf(1.0f - gv);
#pragma unroll
  for (int u = 0; u < 2; u++) {
    f32x4 b = {0.f, 0.f, 0.f, 0.f}, a = {0.f, 0.f, 0.f, 0.f};
#pragma unroll
    for (int w = 0; w < 4; w++) {
      b += *(LAS f32x4*)(lds + ((w * 16 + em * 4 + ec + u) * 64 + lane) * 16);
      a += *(LAS f32x4*)(lds + (((w + 4) * 16 + em * 4 + ec + u) * 64 + lane) * 16);
    }
    f32x4 o;
#pragma unroll
    for (int e = 0; e < 4; e++) {
      const unsigned xw = xv[u][e >> 1], dw = dv[u][e >> 1];
      const float xf = __uint_as_float((e & 1) ? (xw & 0xffff0000u) : (xw << 16));
      const float df = __uint_as_float((e & 1) ? (dw & 0xffff0000u) : (dw << 16));
      o[e] = gv * (a[e] + rbf(xf * df)) + rbf(omg * rbf(b[e]));
    }
    *(f32x4*)(p.out + (size_t)t * D + cb * 64 + (ec + u) * 16 + 4 * g) = o;
  }
}

extern "C" void kernel_launch(void* const* d_in, const int* in_sizes, int n_in, void* d_out, int out_size, void* d_ws,
                              size_t ws_size, hipStream_t stream) {
  Params p;
  p.x = (const u16*)d_in[0]; p.lre = (const u16*)d_in[1]; p.lim = (const u16*)d_in[2];
  p.bre = (const u16*)d_in[3]; p.bim = (const u16*)d_in[4]; p.cre = (const u16*)d_in[5]; p.cim = (const u16*)d_in[6];
  p.ldt = (const u16*)d_in[7]; p.dsk = (const u16*)d_in[8];
  p.wg = (const u16*)d_in[10]; p.wu = (const u16*)d_in[11]; p.wd = (const u16*)d_in[12]; p.gw = (const u16*)d_in[13];
  p.ws = (char*)d_ws; p.out = (float*)d_out;
  (void)hipFuncSetAttribute((const void*)k_phase1, hipFuncAttributeMaxDynamicSharedMemorySize, LDS_BYTES);
  (void)hipFuncSetAttribute((const void*)k_phase2, hipFuncAttributeMaxDynamicSharedMemorySize, LDS_BYTES);
  (void)hipMemsetAsync((char*)d_ws + E_OFF, 0, E_BYTES, stream);
  k_phase1<<<256, 512, LDS_BYTES, stream>>>(p);
  k_phase2<<<256, 512, LDS_BYTES, stream>>>(p);
}
```

```cpp
#include <hip/hip_runtime.h>
#include <stdint.h>

typedef unsigned short u16;
typedef unsigned long long u64;
typedef __bf16 bf16x8 __attribute__((ext_vector_type(8)));
typedef __bf16 bf16x4 __attribute__((ext_vector_type(4)));
typedef __bf16 bf16x2 __attribute__((ext_vector_type(2)));
typedef float f32x4 __attribute__((ext_vector_type(4)));
typedef float f32x2 __attribute__((ext_vector_type(2)));
typedef unsigned u32x4 __attribute__((ext_vector_type(4)));
typedef unsigned u32x2 __attribute__((ext_vector_type(2)));
#define LAS __attribute__((address_space(3)))
#define GAS __attribute__((address_space(1)))
#define RLX_AGENT __ATOMIC_RELAXED, __HIP_MEMORY_SCOPE_AGENT
typedef GAS unsigned gu32;
typedef GAS u64 gu64;
typedef __amdgpu_buffer_rsrc_t rsrc_t;

constexpr int L = 2048, D = 512, P = 512, F = 1792;
constexpr int KS2 = 88;
constexpr int SLOT = 53248;
constexpr int LDS_CTL = 3 * SLOT;
constexpr int LDS_BYTES = 163840;
constexpr int SC1 = 16;
constexpr size_t EPOCH_OFF = 0;
constexpr size_t FLAG_OFF = 4096;
constexpr size_t E_OFF = 65536;
constexpr size_t G_OFF = 131072;
constexpr size_t H_OFF = 262144, H_BYTES = (size_t)32 * KS2 * 4096;
constexpr u64 TAG_STEP = 0x9E3779B97F4A7C15ull;
constexpr unsigned SPIN_CAP = 1u << 18;

#define BARRIER() asm volatile("s_waitcnt lgkmcnt(0)\n\ts_barrier" ::: "memory")
#define WAITV(n) asm volatile("s_waitcnt vmcnt(%0)" ::"i"(n) : "memory")
#define WAITL0() asm volatile("s_waitcnt lgkmcnt(0)" ::: "memory")
#define SCHED() __builtin_amdgcn_sched_barrier(0)
#define PIN(x) asm volatile("" : "+v"(x))

#define STAMP(id) do {} while (0)

__device__ __forceinline__ float bf2f(u16 v) { return __uint_as_float(((unsigned)v) << 16); }
__device__ __forceinline__ float rbf(float f) { return (float)(__bf16)f; }
__device__ __forceinline__ unsigned pack2(float a, float b) {
  bf16x2 v = {(__bf16)a, (__bf16)b};
  return __builtin_bit_cast(unsigned, v);
}
__device__ __forceinline__ rsrc_t mkrsrc(const void* p, unsigned bytes) {
  return __builtin_amdgcn_make_buffer_rsrc((void*)p, 0, bytes, 0x00020000);
}
__device__ __forceinline__ void dma16(rsrc_t r, LAS char* dst, unsigned voff, unsigned soff) {
  __builtin_amdgcn_raw_ptr_buffer_load_lds(r, (LAS void*)dst, 16, voff, soff, 0, 0);
}
__device__ __forceinline__ void dma16_asm(const char* gsrc, unsigned lds_dst) {
  unsigned keep;
  asm volatile("s_mov_b32 %0, m0\n\ts_mov_b32 m0, %2\n\ts_nop 0\n\tglobal_load_lds_dwordx4 %1, off\n\ts_mov_b32 m0, %0"
               : "=&s"(keep) : "v"(gsrc), "s"(lds_dst) : "memory");
}
__device__ __forceinline__ bf16x8 ld128(LAS char* p) { return *(LAS bf16x8*)p; }
__device__ __forceinline__ bf16x8 ldtr(LAS char* p0, LAS char* p1) {
  bf16x4 a = __builtin_amdgcn_ds_read_tr16_b64_v4bf16((LAS bf16x4*)p0);
  bf16x4 b = __builtin_amdgcn_ds_read_tr16_b64_v4bf16((LAS bf16x4*)p1);
  return __builtin_shufflevector(a, b, 0, 1, 2, 3, 4, 5, 6, 7);
}
__device__ __forceinline__ float sigmoidf_(float g) {
  return __builtin_amdgcn_rcpf(1.0f + __builtin_amdgcn_exp2f(-1.4426950408889634f * g));
}
__device__ __forceinline__ f32x2 fma2(f32x2 a, f32x2 b, f32x2 c) { return __builtin_elementwise_fma(a, b, c); }
__device__ __forceinline__ f32x2 lo2(f32x4 v) { return __builtin_shufflevector(v, v, 0, 1); }
__device__ __forceinline__ f32x2 hi2(f32x4 v) { return __builtin_shufflevector(v, v, 2, 3); }
__device__ __forceinline__ void csq(float& r, float& i) { const float t = r * r - i * i; i = 2.f * r * i; r = t; }

struct Params {
  const u16 *x, *lre, *lim, *bre, *bim, *cre, *cim, *ldt, *dsk, *wg, *wu, *wd, *gw;
  char* ws;
  float* out;
};

__device__ __forceinline__ void ssm_consts(u16 ldt, u16 lre, u16 lim, float& lr, float& li, float& cr, float& ci) {
  const float dt = rbf(expf(bf2f(ldt)));
  const float a = bf2f(lre), b = bf2f(lim);
  const float m = expf(dt * a);
  float s, c;
  sincosf(dt * b, &s, &c);
  lr = m * c;
  li = m * s;
  const float nr = lr - 1.0f, ni = li, rden = 1.0f / (a * a + b * b);
  cr = (nr * a + ni * b) * rden;
  ci = (ni * a - nr * b) * rden;
}
__device__ __forceinline__ void share2(float v, float (&o)[2]) {
  const auto t = __builtin_amdgcn_permlane16_swap(__float_as_uint(v), __float_as_uint(v), false, false);
  o[0] = __uint_as_float(t[0]); o[1] = __uint_as_float(t[1]);
}

__device__ __forceinline__ void poll_flags(const Params& p, int id, u64 tag) {
  gu64* f = (gu64*)(p.ws + FLAG_OFF) + id * 16;
  for (unsigned spins = 0; spins < SPIN_CAP; spins++) {
    const u64 v = __hip_atomic_load(f, RLX_AGENT);
    if (__all(v == tag)) break;
    __builtin_amdgcn_s_sleep(2);
  }
  asm volatile("" ::: "memory");
}

template <bool GLU, int NR>
__device__ __forceinline__ void main_loop(LAS char* lds, int wr, int wc, int lane, f32x4 (&acc)[8][NR]) {
  const int i = lane & 15, g = lane >> 4;
  const int sw = (i >> 1) & 7;
  unsigned resA[2], rolA[4];
  if constexpr (GLU) {
    resA[0] = (wr * 80 + i) * 128 + ((g ^ sw) << 4);
    resA[1] = resA[0] ^ 64;
    const int q = (lane & 15) >> 2, pp = lane & 3;
    const int swz = q | ((g & 1) << 2);
#pragma unroll
    for (int c = 0; c < 4; c++) rolA[c] = 20480 + (8 * g + q) * 256 + (((wc * 4 + c) ^ swz) << 5) + pp * 8;
  } else {
    resA[0] = 32768 + (wc * 32 + i) * 128 + ((g ^ sw) << 4);
    resA[1] = resA[0] ^ 64;
    rolA[0] = (wr * 128 + i) * 128 + ((g ^ sw) << 4);
    rolA[1] = rolA[0] ^ 64;
    rolA[2] = rolA[3] = 0;
  }
  auto ld_res = [&](int ks, int r) -> bf16x8 {
    if constexpr (GLU) return ld128(lds + resA[ks] + r * 2048);
    else return ld128(lds + resA[ks] + (r & 1) * 2048 + (r >> 1) * 8192);
  };
  auto ld_rol = [&](int j) -> bf16x8 {
    const int ks = j >> 3, c = j & 7;
    if constexpr (GLU) {
      LAS char* b = lds + rolA[c & 3] + (c >> 2) * 16384 + ks * 8192;
      return ldtr(b, b + 1024);
    } else {
      return ld128(lds + rolA[ks] + c * 2048);
    }
  };
#pragma unroll
  for (int c = 0; c < 8; c++)
#pragma unroll
    for (int r = 0; r < NR; r++) { acc[c][r] = (f32x4){0.f, 0.f, 0.f, 0.f}; PIN(acc[c][r]); }
  bf16x8 res[2][NR], rol[5];
  BARRIER();
  rol[0] = ld_rol(0); SCHED(); res[0][0] = ld_res(0, 0); SCHED(); res[0][1] = ld_res(0, 1); SCHED();
  rol[1] = ld_rol(1); SCHED(); res[0][2] = ld_res(0, 2); SCHED(); res[0][3] = ld_res(0, 3); SCHED();
  rol[2] = ld_rol(2); SCHED();
  if constexpr (NR > 4) res[0][NR - 1] = ld_res(0, NR - 1);
  SCHED();
  int slot = 0;
#pragma unroll 1
  for (int s = 0; s < 8; s++) {
#pragma unroll
    for (int j = 0; j < 16; j++) {
      if (j == 13) {
        BARRIER();
        const int delta = (slot == 2) ? -2 * SLOT : SLOT;
        slot = (slot == 2) ? 0 : slot + 1;
        resA[0] += delta; resA[1] += delta;
        rolA[0] += delta; rolA[1] += delta;
        if constexpr (GLU) { rolA[2] += delta; rolA[3] += delta; }
      }
      int nds = 0;
      constexpr int RD = GLU ? 2 : 1;
      if (j == 13) { res[0][0] = ld_res(0, 0); nds++; }
      if (j != 12) { rol[(j + 3) & 3] = ld_rol((j + 3) & 15); nds += RD; }
      if (j == 10) { rol[4] = ld_rol(15); nds += RD; }
      if (j >= 2 && j < 2 + NR) { res[1][j - 2] = ld_res(1, j - 2); nds++; }
      if (j == 13) { res[0][1] = ld_res(0, 1); nds++; }
      if (j == 14) { res[0][2] = ld_res(0, 2); res[0][3] = ld_res(0, 3); nds += 2; }
      if (j == 15 && NR > 4) { res[0][NR - 1] = ld_res(0, NR - 1); nds++; }
#pragma unroll
      for (int r = 0; r < NR; r++)
        acc[j & 7][r] = __builtin_amdgcn_mfma_f32_16x16x32_bf16(rol[j == 15 ? 4 : (j & 3)], res[j >> 3][r], acc[j & 7][r], 0, 0, 0);
#pragma unroll
      for (int r = 0; r < NR; r++) {
        __builtin_amdgcn_sched_group_barrier(0x8, 1, 0);
        if (j == 13) __builtin_amdgcn_sched_group_barrier(0x2, 2, 0);
        if (r < nds) __builtin_amdgcn_sched_group_barrier(0x100, 1, 0);
      }
      SCHED();
    }
  }
}

__device__ __forceinline__ constexpr int wslot(int i) { return (i / 3 == 0 ? 2 : (i / 3 == 1 ? 0 : 1)) * SLOT + (i % 3) * 4096; }

__global__ __launch_bounds__(512, 2) void k_fused(Params p) {
  extern __shared__ __attribute__((aligned(16))) char smem[];
  LAS char* lds = (LAS char*)smem;
  const int tid = threadIdx.x, lane = tid & 63;
  const int wid = __builtin_amdgcn_readfirstlane(tid >> 6);
  const int xg = blockIdx.x & 7, idx = blockIdx.x >> 3;
  int role, rt, ct = 0, myid;
  {
    const int cg = xg & 1, rq = xg >> 1;
    if (idx < 8) {
      role = 1; rt = rq * 2 + (idx >> 2); ct = cg * 4 + (idx & 3); myid = 182 + rt * 8 + ct;
    } else if (idx == 8) {
      role = 2; rt = xg; myid = 246 + xg;
    } else {
      const int lin = rq * 23 + (idx - 9);
      if (lin < 91) { role = 0; rt = lin / 7; ct = cg * 7 + (lin - rt * 7); myid = rt * 14 + ct; }
      else { role = 3; rt = 0; myid = 254 + cg; }
    }
  }
  const int rb = xg * 4 + (idx >> 3), cb = idx & 7;
  const rsrc_t hr = mkrsrc(p.ws + H_OFF, (unsigned)H_BYTES);
  const int n = lane & 15, g = lane >> 4;
  STAMP(0);
  f32x4 acc2[4][4];
  u64 tag;
  auto ldh = [&](int ks, int m) -> bf16x8 {
    return __builtin_bit_cast(bf16x8, __builtin_amdgcn_raw_buffer_load_b128(hr, lane * 16, ((rb * KS2 + ks) * 4 + m) * 1024, SC1));
  };
  const unsigned vwd = (lane >> 3) * 1024 + cb * 128 + (((lane & 7) ^ (((lane >> 4) & 3) << 1)) << 4);

  if (wid >= 4) {
    const int lw = wid - 4;
    const rsrc_t cr_ = mkrsrc(p.cre, D * P * 2), ci_ = mkrsrc(p.cim, D * P * 2), wdr = mkrsrc(p.wd, F * D * 2);
    const unsigned vc = (cb * 64 + (lane >> 2)) * 1024 + lw * 256 + (lane & 3) * 16;
    bf16x8 cf[8][4];
    auto ldc = [&](int a, int b) {
#pragma unroll
      for (int f = a; f < b; f++) {
        const int c = f >> 3, part = (f >> 2) & 1, G = f & 3;
        cf[G * 2 + part][c] = __builtin_bit_cast(bf16x8, __builtin_amdgcn_raw_buffer_load_b128(part ? ci_ : cr_, vc, c * 16384 + G * 64, 0));
      }
    };
    auto wround = [&](int r) {
#pragma unroll
      for (int k = 0; k < 3; k++)
#pragma unroll
        for (int i = 0; i < 4; i++)
          dma16(wdr, lds + wslot(r * 3 + k) + lw * 12288 + i * 1024, vwd, ((lw * 14 + r * 3 + k) * 32 + i * 8) * 1024);
    };
    auto loader = [&](auto piece, auto np_) {
      constexpr int NP = decltype(np_)::value;
      auto pieces = [&](int s, int a, int b) {
#pragma unroll
        for (int k = a; k < b; k++) piece(s, k);
      };
      pieces(0, 0, NP); pieces(1, 0, 5); WAITV(5); BARRIER();
      pieces(1, 5, NP); pieces(2, 0, 8); WAITV(8); BARRIER();
      pieces(2, 8, NP); pieces(3, 0, NP); WAITV(NP); BARRIER();
      pieces(4, 0, NP); ldc(0, 4); WAITV(NP + 4); BARRIER();
      pieces(5, 0, NP); ldc(4, 8); WAITV(NP + 8); BARRIER();
      pieces(6, 0, NP); ldc(8, 12); WAITV(NP + 8); BARRIER();
      pieces(7, 0, NP); ldc(12, 16); WAITV(NP + 8); BARRIER();
      wround(0); ldc(16, 20); WAITV(4 + 12 + 4); BARRIER();
      wround(1); ldc(20, 24); BARRIER();
    };
    if (role == 0) {
      const int r0 = min(rt * 160, L - 160);
      const rsrc_t xr = mkrsrc(p.x, L * D * 2), gr = mkrsrc(p.wg, D * F * 2), ur = mkrsrc(p.wu, D * F * 2);
      const int xrow = 8 * lw + (lane >> 3);
      const unsigned vx = (r0 + xrow) * 1024 + (((lane & 7) ^ ((xrow >> 1) & 7)) << 4);
      const int krow = 4 * lw + (lane >> 4);
      const int swz = (krow & 3) | (((krow >> 3) & 1) << 2);
      const unsigned vw = krow * (F * 2) + ct * 256 + (((lane & 15) ^ (swz << 1)) << 4);
      loader([&](int s, int k) {
        LAS char* b = lds + (s % 3) * SLOT + lw * 1024;
        if (k < 5) dma16(xr, b + k * 4096, vx, s * 128 + k * 32768);
        else { const int i = (k - 5) >> 1; dma16(((k - 5) & 1) ? ur : gr, b + 20480 + ((k - 5) & 1) * 16384 + i * 4096, vw, (s * 64 + i * 16) * (F * 2)); }
      }, std::integral_constant<int, 13>{});
      wround(2); ldc(24, 32);
    } else if (role == 1) {
      const int r0 = rt * 256;
      const rsrc_t xr = mkrsrc(p.x, L * D * 2), br = mkrsrc(p.bre, P * D * 2), bi = mkrsrc(p.bim, P * D * 2);
      const int ii = 8 * (lw & 1) + (lane >> 3);
      const int csw = ((lane & 7) ^ ((ii >> 1) & 7)) << 4;
      const unsigned vx = (r0 + 32 * (ii >> 2) + 16 * (ii & 1) + ((ii >> 1) & 1) + 2 * (lw >> 1)) * 1024 + csw;
      const unsigned vb = (ct * 64 + 8 * lw + (lane >> 3)) * 1024 + csw;
      loader([&](int s, int k) {
        LAS char* b = lds + (s % 3) * SLOT + lw * 1024;
        if (k < 4) dma16((k & 1) ? bi : br, b + 32768 + (k & 1) * 8192 + (k >> 1) * 4096, vb, s * 128 + (k >> 1) * 32768);
        else { const int i = k - 4; dma16(xr, b + i * 4096, vx, s * 128 + (128 * (i >> 2) + 4 * (i & 3)) * 1024); }
      }, std::integral_constant<int, 12>{});
      LAS f32x2* eloc = (LAS f32x2*)(lds + SLOT + 49152);
      if (lw == 0) {
        tag = *(LAS u64*)(lds + LDS_CTL);
        const f32x2 l16 = *(LAS f32x2*)(lds + LDS_CTL + 64 + lane * 8);
        float lr = l16.x, li = l16.y;
        csq(lr, li);
        float mr = lr, mi = li;
        csq(mr, mi); csq(mr, mi); csq(mr, mi);
        BARRIER();
        f32x2 e[8];
#pragma unroll
        for (int s = 0; s < 8; s++) e[s] = eloc[s * 64 + lane];
        float ar = e[0].x, ai = e[0].y;
#pragma unroll
        for (int s = 1; s < 8; s++) { const float t = lr * ar - li * ai + e[s].x; ai = lr * ai + li * ar + e[s].y; ar = t; }
        gu64* eg = (gu64*)(p.ws + E_OFF);
        const u64 gt = tag << 32;
        const int ch = ct * 64 + lane;
        __hip_atomic_store(eg + (rt * 2 + 0) * 512 + ch, gt | __float_as_uint(ar), RLX_AGENT);
        __hip_atomic_store(eg + (rt * 2 + 1) * 512 + ch, gt | __float_as_uint(ai), RLX_AGENT);
        STAMP(3);
        float hr_ = 0.f, hi_ = 0.f;
        if (rt > 0) {
          float vr[7], vi[7];
          for (unsigned spins = 0; spins < SPIN_CAP; spins++) {
            bool ok = true;
#pragma unroll
            for (int c = 0; c < 7; c++) {
              const int cc = c < rt ? c : 0;
              const u64 a = __hip_atomic_load(eg + (cc * 2 + 0) * 512 + ch, RLX_AGENT);
              const u64 b = __hip_atomic_load(eg + (cc * 2 + 1) * 512 + ch, RLX_AGENT);
              ok &= (unsigned)(a >> 32) == (unsigned)tag && (unsigned)(b >> 32) == (unsigned)tag;
              vr[c] = __uint_as_float((unsigned)a); vi[c] = __uint_as_float((unsigned)b);
            }
            if (__all(ok)) break;
            __builtin_amdgcn_s_sleep(1);
          }
#pragma unroll
          for (int c = 0; c < 7; c++)
            if (c < rt) { const float t = mr * hr_ - mi * hi_ + vr[c]; hi_ = mr * hi_ + mi * hr_ + vi[c]; hr_ = t; }
        }
#pragma unroll
        for (int s = 0; s < 8; s++) {
          eloc[s * 64 + lane] = (f32x2){hr_, hi_};
          const float t = lr * hr_ - li * hi_ + e[s].x; hi_ = lr * hi_ + li * hr_ + e[s].y; hr_ = t;
        }
        STAMP(4);
        BARRIER();
        ldc(24, 32);
      } else {
        ldc(24, 32);
        BARRIER();
        BARRIER();
      }
    } else {
      wround(0); wround(1); ldc(0, 32);
      WAITV(32);
      wround(2);
    }
    WAITV(0);
    STAMP(5);
    BARRIER();
    tag = *(LAS u64*)(lds + LDS_CTL);
    {
      const int src = (n * 4 + g) * 4;
#pragma unroll
      for (int k = 0; k < 8; k++)
#pragma unroll
        for (int c = 0; c < 4; c++) {
          u32x4 v = __builtin_bit_cast(u32x4, cf[k][c]);
#pragma unroll
          for (int e = 0; e < 4; e++) v[e] = __builtin_amdgcn_ds_bpermute(src, v[e]);
          cf[k][c] = __builtin_bit_cast(bf16x8, v);
          PIN(cf[k][c]);
        }
    }
    STAMP(6);
    poll_flags(p, lane == 2 ? 246 + (rb >> 2) : 182 + (rb >> 2) * 8 + lw * 2 + (lane & 1), tag);
    STAMP(7);
#pragma unroll
    for (int c = 0; c < 4; c++)
#pragma unroll
      for (int m = 0; m < 4; m++) acc2[c][m] = (f32x4){0.f, 0.f, 0.f, 0.f};
    bf16x8 hf[3][4];
    const int ks0 = 56 + lw * 8;
#pragma unroll
    for (int k = 0; k < 3; k++)
#pragma unroll
      for (int m = 0; m < 4; m++) hf[k][m] = ldh(ks0 + k, m);
    SCHED();
#pragma unroll
    for (int k = 0; k < 8; k++) {
#pragma unroll
      for (int m = 0; m < 4; m++) {
#pragma unroll
        for (int c = 0; c < 4; c++) acc2[c][m] = __builtin_amdgcn_mfma_f32_16x16x32_bf16(cf[k][c], hf[k % 3][m], acc2[c][m], 0, 0, 0);
        if (k + 3 < 8) hf[k % 3][m] = ldh(ks0 + k + 3, m);
        SCHED();
      }
    }
  } else {
    const int wr = wid >> 1, wc = wid & 1;
    const int chl = wc * 32 + (lane & 31), chg = (ct * 64 + chl) & (P - 1);
    const u16 c_ldt = p.ldt[chg], c_lre = p.lre[chg], c_lim = p.lim[chg];
    {
      u64 w0;
      asm volatile("s_load_dwordx2 %0, %1, 0x0\n\ts_waitcnt lgkmcnt(0)" : "=s"(w0) : "s"(p.ws + EPOCH_OFF) : "memory");
      tag = w0 + TAG_STEP;
      if (tid == 0) *(LAS u64*)(lds + LDS_CTL) = tag;
    }
    if (role == 0) {
      const int r0 = min(rt * 160, L - 160);
      f32x4 acc[8][5];
      STAMP(1);
      main_loop<true, 5>(lds, wr, wc, lane, acc);
      STAMP(2);
      const int mt0 = (r0 >> 4) + wr * 5;
#pragma unroll
      for (int m = 0; m < 5; m++) {
        const int mt = mt0 + m;
        if (mt < rt * 10) continue;
#pragma unroll
        for (int h = 0; h < 2; h++) {
          u32x4 o;
#pragma unroll
          for (int e = 0; e < 4; e++) {
            const int c = 2 * h + (e >> 1);
            const f32x2 gv = (e & 1) ? hi2(acc[c][m]) : lo2(acc[c][m]);
            const f32x2 uv = (e & 1) ? hi2(acc[4 + c][m]) : lo2(acc[4 + c][m]);
            f32x2 t = gv * -1.4426950408889634f;
            t = (f32x2){__builtin_amdgcn_exp2f(t.x), __builtin_amdgcn_exp2f(t.y)} + 1.0f;
            t = (f32x2){__builtin_amdgcn_rcpf(t.x), __builtin_amdgcn_rcpf(t.y)} * uv;
            o[e] = pack2(t.x, t.y);
          }
          const int ks = ct * 4 + wc * 2 + h;
          __builtin_amdgcn_raw_buffer_store_b128(o, hr, lane * 16 + (((mt >> 2) * KS2 + ks) * 4 + (mt & 3)) * 1024, 0, SC1);
        }
      }
    } else if (role == 1) {
      const int r0 = rt * 256;
      LAS f32x2* eloc = (LAS f32x2*)(lds + SLOT + 49152);
      float lr[2], li[2], cr[2], ci[2], sr[2], si[2];
      {
        float a0, a1, a2, a3;
        ssm_consts(c_ldt, c_lre, c_lim, a0, a1, a2, a3);
        share2(a0, lr); share2(a1, li); share2(2.f * a2, cr); share2(2.f * a3, ci);
        csq(a0, a1); csq(a0, a1); csq(a0, a1); csq(a0, a1);
        *(LAS f32x2*)(lds + LDS_CTL + 64 + chl * 8) = (f32x2){a0, a1};
        share2(a0, sr); share2(a1, si);
#pragma unroll
        for (int u = 0; u < 2; u++) { PIN(lr[u]); PIN(li[u]); PIN(cr[u]); PIN(ci[u]); PIN(sr[u]); PIN(si[u]); }
      }
      f32x4 acc[8][4];
      STAMP(1);
      main_loop<false, 4>(lds, wr, wc, lane, acc);
      STAMP(2);
      float e0r[2], e0i[2];
#pragma unroll
      for (int u = 0; u < 2; u++) {
        const f32x2 l2 = {lr[u], lr[u]}, i2 = {li[u], li[u]};
        f32x2 er = lo2(acc[0][u]), ei = lo2(acc[0][2 + u]);
#pragma unroll
        for (int st = 1; st < 16; st++) {
          const f32x2 vr = (st & 1) ? hi2(acc[st >> 1][u]) : lo2(acc[st >> 1][u]);
          const f32x2 vi = (st & 1) ? hi2(acc[st >> 1][2 + u]) : lo2(acc[st >> 1][2 + u]);
          const f32x2 t = fma2(-i2, ei, fma2(l2, er, vr));
          ei = fma2(i2, er, fma2(l2, ei, vi));
          er = t;
        }
        e0r[u] = er.x; e0i[u] = ei.x;
        eloc[(wr * 4 + g) * 64 + wc * 32 + u * 16 + n] =
            (f32x2){sr[u] * er.x - si[u] * ei.x + er.y, sr[u] * ei.x + si[u] * er.x + ei.y};
      }
      STAMP(3);
      BARRIER();
#pragma unroll
      for (int u = 0; u < 2; u++) {
        PIN(cr[u]);
#pragma unroll
        for (int m = 0; m < 8; m++) {
          const f32x4 a = acc[m][u], b = acc[m][2 + u];
          acc[m][u] = cr[u] * a - ci[u] * b;
          acc[m][2 + u] = cr[u] * b + ci[u] * a;
          PIN(acc[m][u]); PIN(acc[m][2 + u]);
        }
      }
      BARRIER();
      STAMP(4);
#pragma unroll
      for (int u = 0; u < 2; u++) {
        const f32x2 c0 = eloc[(wr * 4 + g) * 64 + wc * 32 + u * 16 + n];
        const float c1r = sr[u] * c0.x - si[u] * c0.y + e0r[u], c1i = sr[u] * c0.y + si[u] * c0.x + e0i[u];
        const f32x2 a = {c0.x, c1r}, b = {c0.y, c1i};
        f32x2 hr_ = cr[u] * a - ci[u] * b, hi_ = cr[u] * b + ci[u] * a;
        const f32x2 l2 = {lr[u], lr[u]}, i2 = {li[u], li[u]};
#pragma unroll
        for (int m = 0; m < 8; m++) {
          const f32x2 r0_ = fma2(-i2, hi_, fma2(l2, hr_, lo2(acc[m][u])));
          const f32x2 i0_ = fma2(i2, hr_, fma2(l2, hi_, lo2(acc[m][2 + u])));
          hr_ = fma2(-i2, i0_, fma2(l2, r0_, hi2(acc[m][u])));
          hi_ = fma2(i2, r0_, fma2(l2, i0_, hi2(acc[m][2 + u])));
          acc[m][u][0] = __uint_as_float(pack2(r0_.x, hr_.x)); acc[m][u][1] = __uint_as_float(pack2(r0_.y, hr_.y));
          acc[m][2 + u][0] = __uint_as_float(pack2(-i0_.x, -hi_.x)); acc[m][2 + u][1] = __uint_as_float(pack2(-i0_.y, -hi_.y));
        }
      }
      LAS char* tb = lds + SLOT + wid * 12288;
      LAS char* rbp = tb + (8 * g + (n >> 2)) * 272 + (n & 3) * 8;
      const int G = ct * 2 + wc;
#pragma unroll
      for (int part = 0; part < 2; part++) {
#pragma unroll
        for (int u = 0; u < 2; u++)
#pragma unroll
          for (int c = 0; c < 2; c++)
#pragma unroll
            for (int hh = 0; hh < 2; hh++) {
              const int a = part * 2 + u;
              *(LAS f32x4*)(tb + (u * 16 + n) * 272 + g * 64 + c * 32 + hh * 16) =
                  (f32x4){acc[hh * 4][a][c], acc[hh * 4 + 1][a][c], acc[hh * 4 + 2][a][c], acc[hh * 4 + 3][a][c]};
            }
        WAITL0();
#pragma unroll
        for (int m = 0; m < 8; m++) {
          const bf16x8 v = ldtr(rbp + m * 32, rbp + m * 32 + 4 * 272);
          const int mt = (r0 >> 4) + wr * 8 + m, ks = 56 + G * 2 + part;
          __builtin_amdgcn_raw_buffer_store_b128(__builtin_bit_cast(u32x4, v), hr,
                                                 lane * 16 + (((mt >> 2) * KS2 + ks) * 4 + (mt & 3)) * 1024, 0, SC1);
        }
        WAITL0();
      }
    } else if (role == 2) {
      const u32x4 gwv = *(const u32x4*)(p.gw + lane * 8);
      f32x2 gwf[4];
#pragma unroll
      for (int e = 0; e < 4; e++) gwf[e] = (f32x2){__uint_as_float(gwv[e] << 16), __uint_as_float(gwv[e] & 0xffff0000u)};
      const bf16x2 ones = {(__bf16)1.0f, (__bf16)1.0f};
#pragma unroll 1
      for (int it = 0; it < 2; it++) {
        const int r0 = rt * 256 + wid * 64 + it * 32;
        u32x4 xv[32];
#pragma unroll
        for (int r = 0; r < 32; r++) xv[r] = *(const u32x4*)(p.x + (size_t)(r0 + r) * D + lane * 8);
#pragma unroll
        for (int hb = 0; hb < 2; hb++) {
          float s[16];
#pragma unroll
          for (int r = 0; r < 16; r++) {
            s[r] = 0.f;
#pragma unroll
            for (int e = 0; e < 4; e++) {
              const unsigned w = xv[hb * 16 + r][e];
              const f32x2 pr = (f32x2){__uint_as_float(w << 16), __uint_as_float(w & 0xffff0000u)} * gwf[e];
              const bf16x2 pb = {(__bf16)pr.x, (__bf16)pr.y};
              s[r] = __builtin_amdgcn_fdot2_f32_bf16(pb, ones, s[r], false);
            }
          }
#pragma unroll
          for (int r = 0; r < 8; r++) {
            const auto t = __builtin_amdgcn_permlane32_swap(__float_as_uint(s[r]), __float_as_uint(s[r + 8]), false, false);
            s[r] = __uint_as_float(t[0]) + __uint_as_float(t[1]);
          }
#pragma unroll
          for (int r = 0; r < 4; r++) {
            const auto t = __builtin_amdgcn_permlane16_swap(__float_as_uint(s[r]), __float_as_uint(s[r + 4]), false, false);
            s[r] = __uint_as_float(t[0]) + __uint_as_float(t[1]);
          }
#pragma unroll
          for (int r = 0; r < 4; r++) {
            s[r] += __uint_as_float(__builtin_amdgcn_update_dpp(0, __float_as_uint(s[r]), 0x128, 0xf, 0xf, false));
            s[r] += __uint_as_float(__builtin_amdgcn_update_dpp(0, __float_as_uint(s[r]), 0x124, 0xf, 0xf, false));
            s[r] += __uint_as_float(__builtin_amdgcn_update_dpp(0, __float_as_uint(s[r]), 0x122, 0xf, 0xf, false));
            s[r] += __uint_as_float(__builtin_amdgcn_update_dpp(0, __float_as_uint(s[r]), 0x121, 0xf, 0xf, false));
          }
          const float sv = (n & 2) ? ((n & 1) ? s[3] : s[2]) : ((n & 1) ? s[1] : s[0]);
          if (n < 4)
            __hip_atomic_store((gu32*)(p.ws + G_OFF) + r0 + hb * 16 + n + 4 * (g & 1) + 8 * (g >> 1),
                               __float_as_uint(rbf(sigmoidf_(rbf(sv)))), RLX_AGENT);
        }
      }
    }
    STAMP(5);
    WAITV(0);
    STAMP(6);
    BARRIER();
    if (tid == 0) __hip_atomic_store((gu64*)(p.ws + FLAG_OFF) + myid * 16, tag, RLX_AGENT);
    const int q = n >> 2, pp = n & 3, swz = (2 * g + (q >> 1)) & 3;
    unsigned ra[4];
#pragma unroll
    for (int c = 0; c < 4; c++) ra[c] = wid * 12288 + (4 * g + q) * 128 + ((c ^ swz) << 5) + pp * 8;
    auto ldw = [&](int i, int c) -> bf16x8 { LAS char* a = lds + ra[c] + wslot(i < 9 ? i : i - 9); return ldtr(a, a + 2048); };
    const char* wsrc = (const char*)p.wd + vwd + wid * 14 * 32768;
    auto late = [&](int i) {
#pragma unroll
      for (int k = 0; k < 4; k++) dma16_asm(wsrc + (i * 32 + k * 8) * 1024, wslot(i < 9 ? i : i - 9) + wid * 12288 + k * 1024);
    };
    if (role == 1) { late(6); late(7); late(8); }
    bf16x8 wf[6][4];
#pragma unroll
    for (int i = 0; i < 5; i++)
#pragma unroll
      for (int c = 0; c < 4; c++) wf[i][c] = ldw(i, c);
    WAITL0();
#pragma unroll
    for (int i = 9; i < 14; i++) late(i);
    {
      const int ct0 = (wid * 14) >> 2;
      const int rta = (rb * 4) / 10, rtb = (rb * 4 + 3) / 10;
      poll_flags(p, lane == 8 ? 246 + (rb >> 2) : ((lane & 4) ? rtb : rta) * 14 + ct0 + (lane & 3), tag);
    }
    STAMP(7);
#pragma unroll
    for (int c = 0; c < 4; c++)
#pragma unroll
      for (int m = 0; m < 4; m++) acc2[c][m] = (f32x4){0.f, 0.f, 0.f, 0.f};
    bf16x8 hf[4][4];
    const int ks0 = wid * 14;
#pragma unroll
    for (int k = 0; k < 4; k++)
#pragma unroll
      for (int m = 0; m < 4; m++) hf[k][m] = ldh(ks0 + k, m);
    SCHED();
#pragma unroll
    for (int k = 0; k < 14; k++) {
#pragma unroll
      for (int m = 0; m < 4; m++) {
        if (k + 1 >= 5 && k + 1 < 14) wf[(k + 1) % 6][m] = ldw(k + 1, m);
#pragma unroll
        for (int c = 0; c < 4; c++) acc2[c][m] = __builtin_amdgcn_mfma_f32_16x16x32_bf16(wf[k % 6][c], hf[k & 3][m], acc2[c][m], 0, 0, 0);
        if (k + 4 < 14) hf[k & 3][m] = ldh(ks0 + k + 4, m);
        SCHED();
      }
    }
  }
  STAMP(8);
  float gv[2];
  u32x2 xv[2];
  const int dcol = cb * 64 + n * 4;
#pragma unroll
  for (int u = 0; u < 2; u++) {
    const int t = rb * 64 + wid * 8 + u * 4 + g;
    gv[u] = __uint_as_float(__hip_atomic_load((gu32*)(p.ws + G_OFF) + t, RLX_AGENT));
    xv[u] = *(const u32x2*)(p.x + (size_t)t * D + dcol);
  }
  const u32x2 dv = *(const u32x2*)(p.dsk + dcol);
  BARRIER();
#pragma unroll
  for (int c = 0; c < 4; c++)
#pragma unroll
    for (int m = 0; m < 4; m++) *(LAS f32x4*)(lds + (wid * 16 + m * 4 + c) * 1024 + g * 256 + ((n ^ (4 * c + g)) << 4)) = acc2[c][m];
  BARRIER();
  STAMP(9);
#pragma unroll
  for (int u = 0; u < 2; u++) {
    const int row = wid * 8 + u * 4 + g;
    LAS char* src = lds + (row >> 4) * 4096 + n * 256 + (((row & 15) ^ n) << 4);
    f32x4 b = {0.f, 0.f, 0.f, 0.f}, a = {0.f, 0.f, 0.f, 0.f};
#pragma unroll
    for (int w = 0; w < 4; w++) {
      b += *(LAS f32x4*)(src + w * 16384);
      a += *(LAS f32x4*)(src + (w + 4) * 16384);
    }
    const float omg = rbf(1.0f - gv[u]);
    f32x4 o;
#pragma unroll
    for (int e = 0; e < 4; e++) {
      const unsigned xw = xv[u][e >> 1], dw = dv[e >> 1];
      const float xf = __uint_as_float((e & 1) ? (xw & 0xffff0000u) : (xw << 16));
      const float df = __uint_as_float((e & 1) ? (dw & 0xffff0000u) : (dw << 16));
      o[e] = gv[u] * (a[e] + rbf(xf * df)) + omg * b[e];
    }
    *(f32x4*)(p.out + (size_t)(rb * 64 + row) * D + dcol) = o;
  }
  STAMP(10);
  if (myid == 255 && wid == 7) {
#pragma unroll 1
    for (int k = 0; k < 4; k++) poll_flags(p, k * 64 + lane, tag);
    if (lane == 0) __hip_atomic_store((gu64*)(p.ws + EPOCH_OFF), tag, RLX_AGENT);
  }
}

extern "C" void kernel_launch(void* const* d_in, const int* in_sizes, int n_in, void* d_out, int out_size, void* d_ws,
                              size_t ws_size, hipStream_t stream) {
  Params p;
  p.x = (const u16*)d_in[0]; p.lre = (const u16*)d_in[1]; p.lim = (const u16*)d_in[2];
  p.bre = (const u16*)d_in[3]; p.bim = (const u16*)d_in[4]; p.cre = (const u16*)d_in[5]; p.cim = (const u16*)d_in[6];
  p.ldt = (const u16*)d_in[7]; p.dsk = (const u16*)d_in[8];
  p.wg = (const u16*)d_in[10]; p.wu = (const u16*)d_in[11]; p.wd = (const u16*)d_in[12]; p.gw = (const u16*)d_in[13];
  p.ws = (char*)d_ws; p.out = (float*)d_out;
  (void)hipFuncSetAttribute((const void*)k_fused, hipFuncAttributeMaxDynamicSharedMemorySize, LDS_BYTES);
  k_fused<<<256, 512, LDS_BYTES, stream>>>(p);
}
```

```cpp
#define PROBE 99
#define PROBE_ROLE 0
#define PROBE_HALF 0
#include <hip/hip_runtime.h>
#include <stdint.h>

typedef unsigned short u16;
typedef unsigned long long u64;
typedef __bf16 bf16x8 __attribute__((ext_vector_type(8)));
typedef __bf16 bf16x4 __attribute__((ext_vector_type(4)));
typedef __bf16 bf16x2 __attribute__((ext_vector_type(2)));
typedef float f32x4 __attribute__((ext_vector_type(4)));
typedef float f32x2 __attribute__((ext_vector_type(2)));
typedef unsigned u32x4 __attribute__((ext_vector_type(4)));
typedef unsigned u32x2 __attribute__((ext_vector_type(2)));
#define LAS __attribute__((address_space(3)))
#define GAS __attribute__((address_space(1)))
#define RLX_AGENT __ATOMIC_RELAXED, __HIP_MEMORY_SCOPE_AGENT
typedef GAS unsigned gu32;
typedef GAS u64 gu64;
typedef __amdgpu_buffer_rsrc_t rsrc_t;

constexpr int L = 2048, D = 512, P = 512, F = 1792;
constexpr int KS2 = 88;
constexpr int SLOT = 53248;
constexpr int LDS_CTL = 3 * SLOT;
constexpr int LDS_BYTES = 163840;
constexpr int SC1 = 16;
constexpr size_t EPOCH_OFF = 0;
constexpr size_t FLAG_OFF = 4096;
constexpr size_t E_OFF = 65536;
constexpr size_t G_OFF = 131072;
constexpr size_t H_OFF = 262144, H_BYTES = (size_t)32 * KS2 * 4096;
constexpr u64 TAG_STEP = 0x9E3779B97F4A7C15ull;
constexpr unsigned SPIN_CAP = 1u << 18;

#define BARRIER() asm volatile("s_waitcnt lgkmcnt(0)\n\ts_barrier" ::: "memory")
#define WAITV(n) asm volatile("s_waitcnt vmcnt(%0)" ::"i"(n) : "memory")
#define WAITL0() asm volatile("s_waitcnt lgkmcnt(0)" ::: "memory")
#define SCHED() __builtin_amdgcn_sched_barrier(0)
#define PIN(x) asm volatile("" : "+v"(x))

#define STAMP(id) do { if ((id) == 0) { pt0 = pm = __builtin_amdgcn_s_memrealtime(); } else if ((id) == PROBE) { SCHED(); if (role == PROBE_ROLE && (wid >> 2) == PROBE_HALF) pm = __builtin_amdgcn_s_memrealtime(); SCHED(); } } while (0)

__device__ __forceinline__ float bf2f(u16 v) { return __uint_as_float(((unsigned)v) << 16); }
__device__ __forceinline__ float rbf(float f) { return (float)(__bf16)f; }
__device__ __forceinline__ unsigned pack2(float a, float b) {
  bf16x2 v = {(__bf16)a, (__bf16)b};
  return __builtin_bit_cast(unsigned, v);
}
__device__ __forceinline__ rsrc_t mkrsrc(const void* p, unsigned bytes) {
  return __builtin_amdgcn_make_buffer_rsrc((void*)p, 0, bytes, 0x00020000);
}
__device__ __forceinline__ void dma16(rsrc_t r, LAS char* dst, unsigned voff, unsigned soff) {
  __builtin_amdgcn_raw_ptr_buffer_load_lds(r, (LAS void*)dst, 16, voff, soff, 0, 0);
}
__device__ __forceinline__ void dma16_asm(const char* gsrc, unsigned lds_dst) {
  unsigned keep;
  asm volatile("s_mov_b32 %0, m0\n\ts_mov_b32 m0, %2\n\ts_nop 0\n\tglobal_load_lds_dwordx4 %1, off\n\ts_mov_b32 m0, %0"
               : "=&s"(keep) : "v"(gsrc), "s"(lds_dst) : "memory");
}
__device__ __forceinline__ bf16x8 ld128(LAS char* p) { return *(LAS bf16x8*)p; }
__device__ __forceinline__ bf16x8 ldtr(LAS char* p0, LAS char* p1) {
  bf16x4 a = __builtin_amdgcn_ds_read_tr16_b64_v4bf16((LAS bf16x4*)p0);
  bf16x4 b = __builtin_amdgcn_ds_read_tr16_b64_v4bf16((LAS bf16x4*)p1);
  return __builtin_shufflevector(a, b, 0, 1, 2, 3, 4, 5, 6, 7);
}
__device__ __forceinline__ float sigmoidf_(float g) {
  return __builtin_amdgcn_rcpf(1.0f + __builtin_amdgcn_exp2f(-1.4426950408889634f * g));
}
__device__ __forceinline__ f32x2 fma2(f32x2 a, f32x2 b, f32x2 c) { return __builtin_elementwise_fma(a, b, c); }
__device__ __forceinline__ f32x2 lo2(f32x4 v) { return __builtin_shufflevector(v, v, 0, 1); }
__device__ __forceinline__ f32x2 hi2(f32x4 v) { return __builtin_shufflevector(v, v, 2, 3); }
__device__ __forceinline__ void csq(float& r, float& i) { const float t = r * r - i * i; i = 2.f * r * i; r = t; }

struct Params {
  const u16 *x, *lre, *lim, *bre, *bim, *cre, *cim, *ldt, *dsk, *wg, *wu, *wd, *gw;
  char* ws;
  float* out;
};

__device__ __forceinline__ void ssm_consts(u16 ldt, u16 lre, u16 lim, float& lr, float& li, float& cr, float& ci) {
  const float dt = rbf(expf(bf2f(ldt)));
  const float a = bf2f(lre), b = bf2f(lim);
  const float m = expf(dt * a);
  float s, c;
  sincosf(dt * b, &s, &c);
  lr = m * c;
  li = m * s;
  const float nr = lr - 1.0f, ni = li, rden = 1.0f / (a * a + b * b);
  cr = (nr * a + ni * b) * rden;
  ci = (ni * a - nr * b) * rden;
}
__device__ __forceinline__ void share2(float v, float (&o)[2]) {
  const auto t = __builtin_amdgcn_permlane16_swap(__float_as_uint(v), __float_as_uint(v), false, false);
  o[0] = __uint_as_float(t[0]); o[1] = __uint_as_float(t[1]);
}

__device__ __forceinline__ void poll_flags(const Params& p, int id, u64 tag) {
  gu64* f = (gu64*)(p.ws + FLAG_OFF) + id * 16;
  for (unsigned spins = 0; spins < SPIN_CAP; spins++) {
    const u64 v = __hip_atomic_load(f, RLX_AGENT);
    if (__all(v == tag)) break;
    __builtin_amdgcn_s_sleep(2);
  }
  asm volatile("" ::: "memory");
}

template <bool GLU, int NR>
__device__ __forceinline__ void main_loop(LAS char* lds, int wr, int wc, int lane, f32x4 (&acc)[8][NR]) {
  const int i = lane & 15, g = lane >> 4;
  const int sw = (i >> 1) & 7;
  unsigned resA[2], rolA[4];
  if constexpr (GLU) {
    resA[0] = (wr * 80 + i) * 128 + ((g ^ sw) << 4);
    resA[1] = resA[0] ^ 64;
    const int q = (lane & 15) >> 2, pp = lane & 3;
    const int swz = q | ((g & 1) << 2);
#pragma unroll
    for (int c = 0; c < 4; c++) rolA[c] = 20480 + (8 * g + q) * 256 + (((wc * 4 + c) ^ swz) << 5) + pp * 8;
  } else {
    resA[0] = 32768 + (wc * 32 + i) * 128 + ((g ^ sw) << 4);
    resA[1] = resA[0] ^ 64;
    rolA[0] = (wr * 128 + i) * 128 + ((g ^ sw) << 4);
    rolA[1] = rolA[0] ^ 64;
    rolA[2] = rolA[3] = 0;
  }
  auto ld_res = [&](int ks, int r) -> bf16x8 {
    if constexpr (GLU) return ld128(lds + resA[ks] + r * 2048);
    else return ld128(lds + resA[ks] + (r & 1) * 2048 + (r >> 1) * 8192);
  };
  auto ld_rol = [&](int j) -> bf16x8 {
    const int ks = j >> 3, c = j & 7;
    if constexpr (GLU) {
      LAS char* b = lds + rolA[c & 3] + (c >> 2) * 16384 + ks * 8192;
      return ldtr(b, b + 1024);
    } else {
      return ld128(lds + rolA[ks] + c * 2048);
    }
  };
#pragma unroll
  for (int c = 0; c < 8; c++)
#pragma unroll
    for (int r = 0; r < NR; r++) { acc[c][r] = (f32x4){0.f, 0.f, 0.f, 0.f}; PIN(acc[c][r]); }
  bf16x8 res[2][NR], rol[5];
  BARRIER();
  rol[0] = ld_rol(0); SCHED(); res[0][0] = ld_res(0, 0); SCHED(); res[0][1] = ld_res(0, 1); SCHED();
  rol[1] = ld_rol(1); SCHED(); res[0][2] = ld_res(0, 2); SCHED(); res[0][3] = ld_res(0, 3); SCHED();
  rol[2] = ld_rol(2); SCHED();
  if constexpr (NR > 4) res[0][NR - 1] = ld_res(0, NR - 1);
  SCHED();
  int slot = 0;
#pragma unroll 1
  for (int s = 0; s < 8; s++) {
#pragma unroll
    for (int j = 0; j < 16; j++) {
      if (j == 13) {
        BARRIER();
        const int delta = (slot == 2) ? -2 * SLOT : SLOT;
        slot = (slot == 2) ? 0 : slot + 1;
        resA[0] += delta; resA[1] += delta;
        rolA[0] += delta; rolA[1] += delta;
        if constexpr (GLU) { rolA[2] += delta; rolA[3] += delta; }
      }
      int nds = 0;
      constexpr int RD = GLU ? 2 : 1;
      if (j == 13) { res[0][0] = ld_res(0, 0); nds++; }
      if (j != 12) { rol[(j + 3) & 3] = ld_rol((j + 3) & 15); nds += RD; }
      if (j == 10) { rol[4] = ld_rol(15); nds += RD; }
      if (j >= 2 && j < 2 + NR) { res[1][j - 2] = ld_res(1, j - 2); nds++; }
      if (j == 13) { res[0][1] = ld_res(0, 1); nds++; }
      if (j == 14) { res[0][2] = ld_res(0, 2); res[0][3] = ld_res(0, 3); nds += 2; }
      if (j == 15 && NR > 4) { res[0][NR - 1] = ld_res(0, NR - 1); nds++; }
#pragma unroll
      for (int r = 0; r < NR; r++)
        acc[j & 7][r] = __builtin_amdgcn_mfma_f32_16x16x32_bf16(rol[j == 15 ? 4 : (j & 3)], res[j >> 3][r], acc[j & 7][r], 0, 0, 0);
#pragma unroll
      for (int r = 0; r < NR; r++) {
        __builtin_amdgcn_sched_group_barrier(0x8, 1, 0);
        if (j == 13) __builtin_amdgcn_sched_group_barrier(0x2, 2, 0);
        if (r < nds) __builtin_amdgcn_sched_group_barrier(0x100, 1, 0);
      }
      SCHED();
    }
  }
}

__device__ __forceinline__ constexpr int wslot(int i) { return (i / 3 == 0 ? 2 : (i / 3 == 1 ? 0 : 1)) * SLOT + (i % 3) * 4096; }

__global__ __launch_bounds__(512, 2) void k_fused(Params p) {
  extern __shared__ __attribute__((aligned(16))) char smem[];
  LAS char* lds = (LAS char*)smem;
  const int tid = threadIdx.x, lane = tid & 63;
  const int wid = __builtin_amdgcn_readfirstlane(tid >> 6);
  const int xg = blockIdx.x & 7, idx = blockIdx.x >> 3;
  int role, rt, ct = 0, myid;
  {
    const int cg = xg & 1, rq = xg >> 1;
    if (idx < 8) {
      role = 1; rt = rq * 2 + (idx >> 2); ct = cg * 4 + (idx & 3); myid = 182 + rt * 8 + ct;
    } else if (idx == 8) {
      role = 2; rt = xg; myid = 246 + xg;
    } else {
      const int lin = rq * 23 + (idx - 9);
      if (lin < 91) { role = 0; rt = lin / 7; ct = cg * 7 + (lin - rt * 7); myid = rt * 14 + ct; }
      else { role = 3; rt = 0; myid = 254 + cg; }
    }
  }
  const int rb = xg * 4 + (idx >> 3), cb = idx & 7;
  const rsrc_t hr = mkrsrc(p.ws + H_OFF, (unsigned)H_BYTES);
  const int n = lane & 15, g = lane >> 4;
  u64 pt0, pm;
  STAMP(0);
  f32x4 acc2[4][4];
  u64 tag;
  auto ldh = [&](int ks, int m) -> bf16x8 {
    return __builtin_bit_cast(bf16x8, __builtin_amdgcn_raw_buffer_load_b128(hr, lane * 16, ((rb * KS2 + ks) * 4 + m) * 1024, SC1));
  };
  const unsigned vwd = (lane >> 3) * 1024 + cb * 128 + (((lane & 7) ^ (((lane >> 4) & 3) << 1)) << 4);

  if (wid >= 4) {
    const int lw = wid - 4;
    const rsrc_t cr_ = mkrsrc(p.cre, D * P * 2), ci_ = mkrsrc(p.cim, D * P * 2), wdr = mkrsrc(p.wd, F * D * 2);
    const unsigned vc = (cb * 64 + (lane >> 2)) * 1024 + lw * 256 + (lane & 3) * 16;
    bf16x8 cf[8][4];
    auto ldc = [&](int a, int b) {
#pragma unroll
      for (int f = a; f < b; f++) {
        const int c = f >> 3, part = (f >> 2) & 1, G = f & 3;
        cf[G * 2 + part][c] = __builtin_bit_cast(bf16x8, __builtin_amdgcn_raw_buffer_load_b128(part ? ci_ : cr_, vc, c * 16384 + G * 64, 0));
      }
    };
    auto wround = [&](int r) {
#pragma unroll
      for (int k = 0; k < 3; k++)
#pragma unroll
        for (int i = 0; i < 4; i++)
          dma16(wdr, lds + wslot(r * 3 + k) + lw * 12288 + i * 1024, vwd, ((lw * 14 + r * 3 + k) * 32 + i * 8) * 1024);
    };
    auto loader = [&](auto piece, auto np_) {
      constexpr int NP = decltype(np_)::value;
      auto pieces = [&](int s, int a, int b) {
#pragma unroll
        for (int k = a; k < b; k++) piece(s, k);
      };
      pieces(0, 0, NP); pieces(1, 0, 5); WAITV(5); BARRIER();
      pieces(1, 5, NP); pieces(2, 0, 8); WAITV(8); BARRIER();
      pieces(2, 8, NP); pieces(3, 0, NP); WAITV(NP); BARRIER();
      pieces(4, 0, NP); ldc(0, 4); WAITV(NP + 4); BARRIER();
      pieces(5, 0, NP); ldc(4, 8); WAITV(NP + 8); BARRIER();
      pieces(6, 0, NP); ldc(8, 12); WAITV(NP + 8); BARRIER();
      pieces(7, 0, NP); ldc(12, 16); WAITV(NP + 8); BARRIER();
      wround(0); ldc(16, 20); WAITV(4 + 12 + 4); BARRIER();
      wround(1); ldc(20, 24); BARRIER();
    };
    if (role == 0) {
      const int r0 = min(rt * 160, L - 160);
      const rsrc_t xr = mkrsrc(p.x, L * D * 2), gr = mkrsrc(p.wg, D * F * 2), ur = mkrsrc(p.wu, D * F * 2);
      const int xrow = 8 * lw + (lane >> 3);
      const unsigned vx = (r0 + xrow) * 1024 + (((lane & 7) ^ ((xrow >> 1) & 7)) << 4);
      const int krow = 4 * lw + (lane >> 4);
      const int swz = (krow & 3) | (((krow >> 3) & 1) << 2);
      const unsigned vw = krow * (F * 2) + ct * 256 + (((lane & 15) ^ (swz << 1)) << 4);
      loader([&](int s, int k) {
        LAS char* b = lds + (s % 3) * SLOT + lw * 1024;
        if (k < 5) dma16(xr, b + k * 4096, vx, s * 128 + k * 32768);
        else { const int i = (k - 5) >> 1; dma16(((k - 5) & 1) ? ur : gr, b + 20480 + ((k - 5) & 1) * 16384 + i * 4096, vw, (s * 64 + i * 16) * (F * 2)); }
      }, std::integral_constant<int, 13>{});
      wround(2); ldc(24, 32);
    } else if (role == 1) {
      const int r0 = rt * 256;
      const rsrc_t xr = mkrsrc(p.x, L * D * 2), br = mkrsrc(p.bre, P * D * 2), bi = mkrsrc(p.bim, P * D * 2);
      const int ii = 8 * (lw & 1) + (lane >> 3);
      const int csw = ((lane & 7) ^ ((ii >> 1) & 7)) << 4;
      const unsigned vx = (r0 + 32 * (ii >> 2) + 16 * (ii & 1) + ((ii >> 1) & 1) + 2 * (lw >> 1)) * 1024 + csw;
      const unsigned vb = (ct * 64 + 8 * lw + (lane >> 3)) * 1024 + csw;
      loader([&](int s, int k) {
        LAS char* b = lds + (s % 3) * SLOT + lw * 1024;
        if (k < 4) dma16((k & 1) ? bi : br, b + 32768 + (k & 1) * 8192 + (k >> 1) * 4096, vb, s * 128 + (k >> 1) * 32768);
        else { const int i = k - 4; dma16(xr, b + i * 4096, vx, s * 128 + (128 * (i >> 2) + 4 * (i & 3)) * 1024); }
      }, std::integral_constant<int, 12>{});
      LAS f32x2* eloc = (LAS f32x2*)(lds + SLOT + 49152);
      if (lw == 0) {
        tag = *(LAS u64*)(lds + LDS_CTL);
        const f32x2 l16 = *(LAS f32x2*)(lds + LDS_CTL + 64 + lane * 8);
        float lr = l16.x, li = l16.y;
        csq(lr, li);
        float mr = lr, mi = li;
        csq(mr, mi); csq(mr, mi); csq(mr, mi);
        BARRIER();
        f32x2 e[8];
#pragma unroll
        for (int s = 0; s < 8; s++) e[s] = eloc[s * 64 + lane];
        float ar = e[0].x, ai = e[0].y;
#pragma unroll
        for (int s = 1; s < 8; s++) { const float t = lr * ar - li * ai + e[s].x; ai = lr * ai + li * ar + e[s].y; ar = t; }
        gu64* eg = (gu64*)(p.ws + E_OFF);
        const u64 gt = tag << 32;
        const int ch = ct * 64 + lane;
        __hip_atomic_store(eg + (rt * 2 + 0) * 512 + ch, gt | __float_as_uint(ar), RLX_AGENT);
        __hip_atomic_store(eg + (rt * 2 + 1) * 512 + ch, gt | __float_as_uint(ai), RLX_AGENT);
        STAMP(3);
        float hr_ = 0.f, hi_ = 0.f;
        if (rt > 0) {
          float vr[7], vi[7];
          for (unsigned spins = 0; spins < SPIN_CAP; spins++) {
            bool ok = true;
#pragma unroll
            for (int c = 0; c < 7; c++) {
              const int cc = c < rt ? c : 0;
              const u64 a = __hip_atomic_load(eg + (cc * 2 + 0) * 512 + ch, RLX_AGENT);
              const u64 b = __hip_atomic_load(eg + (cc * 2 + 1) * 512 + ch, RLX_AGENT);
              ok &= (unsigned)(a >> 32) == (unsigned)tag && (unsigned)(b >> 32) == (unsigned)tag;
              vr[c] = __uint_as_float((unsigned)a); vi[c] = __uint_as_float((unsigned)b);
            }
            if (__all(ok)) break;
            __builtin_amdgcn_s_sleep(1);
          }
#pragma unroll
          for (int c = 0; c < 7; c++)
            if (c < rt) { const float t = mr * hr_ - mi * hi_ + vr[c]; hi_ = mr * hi_ + mi * hr_ + vi[c]; hr_ = t; }
        }
#pragma unroll
        for (int s = 0; s < 8; s++) {
          eloc[s * 64 + lane] = (f32x2){hr_, hi_};
          const float t = lr * hr_ - li * hi_ + e[s].x; hi_ = lr * hi_ + li * hr_ + e[s].y; hr_ = t;
        }
        STAMP(4);
        BARRIER();
        ldc(24, 32);
      } else {
        ldc(24, 32);
        BARRIER();
        BARRIER();
      }
    } else {
      wround(0); wround(1); ldc(0, 32);
      WAITV(32);
      wround(2);
    }
    WAITV(0);
    STAMP(5);
    BARRIER();
    tag = *(LAS u64*)(lds + LDS_CTL);
    {
      const int src = (n * 4 + g) * 4;
#pragma unroll
      for (int k = 0; k < 8; k++)
#pragma unroll
        for (int c = 0; c < 4; c++) {
          u32x4 v = __builtin_bit_cast(u32x4, cf[k][c]);
#pragma unroll
          for (int e = 0; e < 4; e++) v[e] = __builtin_amdgcn_ds_bpermute(src, v[e]);
          cf[k][c] = __builtin_bit_cast(bf16x8, v);
          PIN(cf[k][c]);
        }
    }
    STAMP(6);
    poll_flags(p, lane == 2 ? 246 + (rb >> 2) : 182 + (rb >> 2) * 8 + lw * 2 + (lane & 1), tag);
    STAMP(7);
#pragma unroll
    for (int c = 0; c < 4; c++)
#pragma unroll
      for (int m = 0; m < 4; m++) acc2[c][m] = (f32x4){0.f, 0.f, 0.f, 0.f};
    bf16x8 hf[3][4];
    const int ks0 = 56 + lw * 8;
#pragma unroll
    for (int k = 0; k < 3; k++)
#pragma unroll
      for (int m = 0; m < 4; m++) hf[k][m] = ldh(ks0 + k, m);
    SCHED();
#pragma unroll
    for (int k = 0; k < 8; k++) {
#pragma unroll
      for (int m = 0; m < 4; m++) {
#pragma unroll
        for (int c = 0; c < 4; c++) acc2[c][m] = __builtin_amdgcn_mfma_f32_16x16x32_bf16(cf[k][c], hf[k % 3][m], acc2[c][m], 0, 0, 0);
        if (k + 3 < 8) hf[k % 3][m] = ldh(ks0 + k + 3, m);
        SCHED();
      }
    }
  } else {
    const int wr = wid >> 1, wc = wid & 1;
    const int chl = wc * 32 + (lane & 31), chg = (ct * 64 + chl) & (P - 1);
    const u16 c_ldt = p.ldt[chg], c_lre = p.lre[chg], c_lim = p.lim[chg];
    {
      u64 w0;
      asm volatile("s_load_dwordx2 %0, %1, 0x0\n\ts_waitcnt lgkmcnt(0)" : "=s"(w0) : "s"(p.ws + EPOCH_OFF) : "memory");
      tag = w0 + TAG_STEP;
      if (tid == 0) *(LAS u64*)(lds + LDS_CTL) = tag;
    }
    if (role == 0) {
      const int r0 = min(rt * 160, L - 160);
      f32x4 acc[8][5];
      STAMP(1);
      main_loop<true, 5>(lds, wr, wc, lane, acc);
      STAMP(2);
      const int mt0 = (r0 >> 4) + wr * 5;
#pragma unroll
      for (int m = 0; m < 5; m++) {
        const int mt = mt0 + m;
        if (mt < rt * 10) continue;
#pragma unroll
        for (int h = 0; h < 2; h++) {
          u32x4 o;
#pragma unroll
          for (int e = 0; e < 4; e++) {
            const int c = 2 * h + (e >> 1);
            const f32x2 gv = (e & 1) ? hi2(acc[c][m]) : lo2(acc[c][m]);
            const f32x2 uv = (e & 1) ? hi2(acc[4 + c][m]) : lo2(acc[4 + c][m]);
            f32x2 t = gv * -1.4426950408889634f;
            t = (f32x2){__builtin_amdgcn_exp2f(t.x), __builtin_amdgcn_exp2f(t.y)} + 1.0f;
            t = (f32x2){__builtin_amdgcn_rcpf(t.x), __builtin_amdgcn_rcpf(t.y)} * uv;
            o[e] = pack2(t.x, t.y);
          }
          const int ks = ct * 4 + wc * 2 + h;
          __builtin_amdgcn_raw_buffer_store_b128(o, hr, lane * 16 + (((mt >> 2) * KS2 + ks) * 4 + (mt & 3)) * 1024, 0, SC1);
        }
      }
    } else if (role == 1) {
      const int r0 = rt * 256;
      LAS f32x2* eloc = (LAS f32x2*)(lds + SLOT + 49152);
      float lr[2], li[2], cr[2], ci[2], sr[2], si[2];
      {
        float a0, a1, a2, a3;
        ssm_consts(c_ldt, c_lre, c_lim, a0, a1, a2, a3);
        share2(a0, lr); share2(a1, li); share2(2.f * a2, cr); share2(2.f * a3, ci);
        csq(a0, a1); csq(a0, a1); csq(a0, a1); csq(a0, a1);
        *(LAS f32x2*)(lds + LDS_CTL + 64 + chl * 8) = (f32x2){a0, a1};
        share2(a0, sr); share2(a1, si);
#pragma unroll
        for (int u = 0; u < 2; u++) { PIN(lr[u]); PIN(li[u]); PIN(cr[u]); PIN(ci[u]); PIN(sr[u]); PIN(si[u]); }
      }
      f32x4 acc[8][4];
      STAMP(1);
      main_loop<false, 4>(lds, wr, wc, lane, acc);
      STAMP(2);
      float e0r[2], e0i[2];
#pragma unroll
      for (int u = 0; u < 2; u++) {
        const f32x2 l2 = {lr[u], lr[u]}, i2 = {li[u], li[u]};
        f32x2 er = lo2(acc[0][u]), ei = lo2(acc[0][2 + u]);
#pragma unroll
        for (int st = 1; st < 16; st++) {
          const f32x2 vr = (st & 1) ? hi2(acc[st >> 1][u]) : lo2(acc[st >> 1][u]);
          const f32x2 vi = (st & 1) ? hi2(acc[st >> 1][2 + u]) : lo2(acc[st >> 1][2 + u]);
          const f32x2 t = fma2(-i2, ei, fma2(l2, er, vr));
          ei = fma2(i2, er, fma2(l2, ei, vi));
          er = t;
        }
        e0r[u] = er.x; e0i[u] = ei.x;
        eloc[(wr * 4 + g) * 64 + wc * 32 + u * 16 + n] =
            (f32x2){sr[u] * er.x - si[u] * ei.x + er.y, sr[u] * ei.x + si[u] * er.x + ei.y};
      }
      STAMP(3);
      BARRIER();
#pragma unroll
      for (int u = 0; u < 2; u++) {
        PIN(cr[u]);
#pragma unroll
        for (int m = 0; m < 8; m++) {
          const f32x4 a = acc[m][u], b = acc[m][2 + u];
          acc[m][u] = cr[u] * a - ci[u] * b;
          acc[m][2 + u] = cr[u] * b + ci[u] * a;
          PIN(acc[m][u]); PIN(acc[m][2 + u]);
        }
      }
      BARRIER();
      STAMP(4);
#pragma unroll
      for (int u = 0; u < 2; u++) {
        const f32x2 c0 = eloc[(wr * 4 + g) * 64 + wc * 32 + u * 16 + n];
        const float c1r = sr[u] * c0.x - si[u] * c0.y + e0r[u], c1i = sr[u] * c0.y + si[u] * c0.x + e0i[u];
        const f32x2 a = {c0.x, c1r}, b = {c0.y, c1i};
        f32x2 hr_ = cr[u] * a - ci[u] * b, hi_ = cr[u] * b + ci[u] * a;
        const f32x2 l2 = {lr[u], lr[u]}, i2 = {li[u], li[u]};
#pragma unroll
        for (int m = 0; m < 8; m++) {
          const f32x2 r0_ = fma2(-i2, hi_, fma2(l2, hr_, lo2(acc[m][u])));
          const f32x2 i0_ = fma2(i2, hr_, fma2(l2, hi_, lo2(acc[m][2 + u])));
          hr_ = fma2(-i2, i0_, fma2(l2, r0_, hi2(acc[m][u])));
          hi_ = fma2(i2, r0_, fma2(l2, i0_, hi2(acc[m][2 + u])));
          acc[m][u][0] = __uint_as_float(pack2(r0_.x, hr_.x)); acc[m][u][1] = __uint_as_float(pack2(r0_.y, hr_.y));
          acc[m][2 + u][0] = __uint_as_float(pack2(-i0_.x, -hi_.x)); acc[m][2 + u][1] = __uint_as_float(pack2(-i0_.y, -hi_.y));
        }
      }
      LAS char* tb = lds + SLOT + wid * 12288;
      LAS char* rbp = tb + (8 * g + (n >> 2)) * 272 + (n & 3) * 8;
      const int G = ct * 2 + wc;
#pragma unroll
      for (int part = 0; part < 2; part++) {
#pragma unroll
        for (int u = 0; u < 2; u++)
#pragma unroll
          for (int c = 0; c < 2; c++)
#pragma unroll
            for (int hh = 0; hh < 2; hh++) {
              const int a = part * 2 + u;
              *(LAS f32x4*)(tb + (u * 16 + n) * 272 + g * 64 + c * 32 + hh * 16) =
                  (f32x4){acc[hh * 4][a][c], acc[hh * 4 + 1][a][c], acc[hh * 4 + 2][a][c], acc[hh * 4 + 3][a][c]};
            }
        WAITL0();
#pragma unroll
        for (int m = 0; m < 8; m++) {
          const bf16x8 v = ldtr(rbp + m * 32, rbp + m * 32 + 4 * 272);
          const int mt = (r0 >> 4) + wr * 8 + m, ks = 56 + G * 2 + part;
          __builtin_amdgcn_raw_buffer_store_b128(__builtin_bit_cast(u32x4, v), hr,
                                                 lane * 16 + (((mt >> 2) * KS2 + ks) * 4 + (mt & 3)) * 1024, 0, SC1);
        }
        WAITL0();
      }
    } else if (role == 2) {
      const u32x4 gwv = *(const u32x4*)(p.gw + lane * 8);
      f32x2 gwf[4];
#pragma unroll
      for (int e = 0; e < 4; e++) gwf[e] = (f32x2){__uint_as_float(gwv[e] << 16), __uint_as_float(gwv[e] & 0xffff0000u)};
      const bf16x2 ones = {(__bf16)1.0f, (__bf16)1.0f};
#pragma unroll 1
      for (int it = 0; it < 2; it++) {
        const int r0 = rt * 256 + wid * 64 + it * 32;
        u32x4 xv[32];
#pragma unroll
        for (int r = 0; r < 32; r++) xv[r] = *(const u32x4*)(p.x + (size_t)(r0 + r) * D + lane * 8);
#pragma unroll
        for (int hb = 0; hb < 2; hb++) {
          float s[16];
#pragma unroll
          for (int r = 0; r < 16; r++) {
            s[r] = 0.f;
#pragma unroll
            for (int e = 0; e < 4; e++) {
              const unsigned w = xv[hb * 16 + r][e];
              const f32x2 pr = (f32x2){__uint_as_float(w << 16), __uint_as_float(w & 0xffff0000u)} * gwf[e];
              const bf16x2 pb = {(__bf16)pr.x, (__bf16)pr.y};
              s[r] = __builtin_amdgcn_fdot2_f32_bf16(pb, ones, s[r], false);
            }
          }
#pragma unroll
          for (int r = 0; r < 8; r++) {
            const auto t = __builtin_amdgcn_permlane32_swap(__float_as_uint(s[r]), __float_as_uint(s[r + 8]), false, false);
            s[r] = __uint_as_float(t[0]) + __uint_as_float(t[1]);
          }
#pragma unroll
          for (int r = 0; r < 4; r++) {
            const auto t = __builtin_amdgcn_permlane16_swap(__float_as_uint(s[r]), __float_as_uint(s[r + 4]), false, false);
            s[r] = __uint_as_float(t[0]) + __uint_as_float(t[1]);
          }
#pragma unroll
          for (int r = 0; r < 4; r++) {
            s[r] += __uint_as_float(__builtin_amdgcn_update_dpp(0, __float_as_uint(s[r]), 0x128, 0xf, 0xf, false));
            s[r] += __uint_as_float(__builtin_amdgcn_update_dpp(0, __float_as_uint(s[r]), 0x124, 0xf, 0xf, false));
            s[r] += __uint_as_float(__builtin_amdgcn_update_dpp(0, __float_as_uint(s[r]), 0x122, 0xf, 0xf, false));
            s[r] += __uint_as_float(__builtin_amdgcn_update_dpp(0, __float_as_uint(s[r]), 0x121, 0xf, 0xf, false));
          }
          const float sv = (n & 2) ? ((n & 1) ? s[3] : s[2]) : ((n & 1) ? s[1] : s[0]);
          if (n < 4)
            __hip_atomic_store((gu32*)(p.ws + G_OFF) + r0 + hb * 16 + n + 4 * (g & 1) + 8 * (g >> 1),
                               __float_as_uint(rbf(sigmoidf_(rbf(sv)))), RLX_AGENT);
        }
      }
    }
    STAMP(5);
    WAITV(0);
    STAMP(6);
    BARRIER();
    if (tid == 0) __hip_atomic_store((gu64*)(p.ws + FLAG_OFF) + myid * 16, tag, RLX_AGENT);
    const int q = n >> 2, pp = n & 3, swz = (2 * g + (q >> 1)) & 3;
    unsigned ra[4];
#pragma unroll
    for (int c = 0; c < 4; c++) ra[c] = wid * 12288 + (4 * g + q) * 128 + ((c ^ swz) << 5) + pp * 8;
    auto ldw = [&](int i, int c) -> bf16x8 { LAS char* a = lds + ra[c] + wslot(i < 9 ? i : i - 9); return ldtr(a, a + 2048); };
    const char* wsrc = (const char*)p.wd + vwd + wid * 14 * 32768;
    auto late = [&](int i) {
#pragma unroll
      for (int k = 0; k < 4; k++) dma16_asm(wsrc + (i * 32 + k * 8) * 1024, wslot(i < 9 ? i : i - 9) + wid * 12288 + k * 1024);
    };
    if (role == 1) { late(6); late(7); late(8); }
    bf16x8 wf[6][4];
#pragma unroll
    for (int i = 0; i < 5; i++)
#pragma unroll
      for (int c = 0; c < 4; c++) wf[i][c] = ldw(i, c);
    WAITL0();
#pragma unroll
    for (int i = 9; i < 14; i++) late(i);
    {
      const int ct0 = (wid * 14) >> 2;
      const int rta = (rb * 4) / 10, rtb = (rb * 4 + 3) / 10;
      poll_flags(p, lane == 8 ? 246 + (rb >> 2) : ((lane & 4) ? rtb : rta) * 14 + ct0 + (lane & 3), tag);
    }
    STAMP(7);
#pragma unroll
    for (int c = 0; c < 4; c++)
#pragma unroll
      for (int m = 0; m < 4; m++) acc2[c][m] = (f32x4){0.f, 0.f, 0.f, 0.f};
    bf16x8 hf[4][4];
    const int ks0 = wid * 14;
#pragma unroll
    for (int k = 0; k < 4; k++)
#pragma unroll
      for (int m = 0; m < 4; m++) hf[k][m] = ldh(ks0 + k, m);
    SCHED();
#pragma unroll
    for (int k = 0; k < 14; k++) {
#pragma unroll
      for (int m = 0; m < 4; m++) {
        if (k + 1 >= 5 && k + 1 < 14) wf[(k + 1) % 6][m] = ldw(k + 1, m);
#pragma unroll
        for (int c = 0; c < 4; c++) acc2[c][m] = __builtin_amdgcn_mfma_f32_16x16x32_bf16(wf[k % 6][c], hf[k & 3][m], acc2[c][m], 0, 0, 0);
        if (k + 4 < 14) hf[k & 3][m] = ldh(ks0 + k + 4, m);
        SCHED();
      }
    }
  }
  STAMP(8);
  float gv[2];
  u32x2 xv[2];
  const int dcol = cb * 64 + n * 4;
#pragma unroll
  for (int u = 0; u < 2; u++) {
    const int t = rb * 64 + wid * 8 + u * 4 + g;
    gv[u] = __uint_as_float(__hip_atomic_load((gu32*)(p.ws + G_OFF) + t, RLX_AGENT));
    xv[u] = *(const u32x2*)(p.x + (size_t)t * D + dcol);
  }
  const u32x2 dv = *(const u32x2*)(p.dsk + dcol);
  BARRIER();
#pragma unroll
  for (int c = 0; c < 4; c++)
#pragma unroll
    for (int m = 0; m < 4; m++) *(LAS f32x4*)(lds + (wid * 16 + m * 4 + c) * 1024 + g * 256 + ((n ^ (4 * c + g)) << 4)) = acc2[c][m];
  BARRIER();
  STAMP(9);
#pragma unroll
  for (int u = 0; u < 2; u++) {
    const int row = wid * 8 + u * 4 + g;
    LAS char* src = lds + (row >> 4) * 4096 + n * 256 + (((row & 15) ^ n) << 4);
    f32x4 b = {0.f, 0.f, 0.f, 0.f}, a = {0.f, 0.f, 0.f, 0.f};
#pragma unroll
    for (int w = 0; w < 4; w++) {
      b += *(LAS f32x4*)(src + w * 16384);
      a += *(LAS f32x4*)(src + (w + 4) * 16384);
    }
    const float omg = rbf(1.0f - gv[u]);
    f32x4 o;
#pragma unroll
    for (int e = 0; e < 4; e++) {
      const unsigned xw = xv[u][e >> 1], dw = dv[e >> 1];
      const float xf = __uint_as_float((e & 1) ? (xw & 0xffff0000u) : (xw << 16));
      const float df = __uint_as_float((e & 1) ? (dw & 0xffff0000u) : (dw << 16));
      o[e] = gv[u] * (a[e] + rbf(xf * df)) + omg * b[e];
    }
    *(f32x4*)(p.out + (size_t)(rb * 64 + row) * D + dcol) = o;
  }
  STAMP(10);
  if (myid == 255 && wid == 7) {
#pragma unroll 1
    for (int k = 0; k < 4; k++) poll_flags(p, k * 64 + lane, tag);
    if (lane == 0) __hip_atomic_store((gu64*)(p.ws + EPOCH_OFF), tag, RLX_AGENT);
  }
  while (__builtin_amdgcn_s_memrealtime() < pm + 5000) __builtin_amdgcn_s_sleep(1);
}

extern "C" void kernel_launch(void* const* d_in, const int* in_sizes, int n_in, void* d_out, int out_size, void* d_ws,
                              size_t ws_size, hipStream_t stream) {
  Params p;
  p.x = (const u16*)d_in[0]; p.lre = (const u16*)d_in[1]; p.lim = (const u16*)d_in[2];
  p.bre = (const u16*)d_in[3]; p.bim = (const u16*)d_in[4]; p.cre = (const u16*)d_in[5]; p.cim = (const u16*)d_in[6];
  p.ldt = (const u16*)d_in[7]; p.dsk = (const u16*)d_in[8];
  p.wg = (const u16*)d_in[10]; p.wu = (const u16*)d_in[11]; p.wd = (const u16*)d_in[12]; p.gw = (const u16*)d_in[13];
  p.ws = (char*)d_ws; p.out = (float*)d_out;
  (void)hipFuncSetAttribute((const void*)k_fused, hipFuncAttributeMaxDynamicSharedMemorySize, LDS_BYTES);
  k_fused<<<256, 512, LDS_BYTES, stream>>>(p);
}
```

```cpp
#include <hip/hip_runtime.h>
#include <stdint.h>

typedef unsigned short u16;
typedef unsigned long long u64;
typedef __bf16 bf16x8 __attribute__((ext_vector_type(8)));
typedef __bf16 bf16x4 __attribute__((ext_vector_type(4)));
typedef __bf16 bf16x2 __attribute__((ext_vector_type(2)));
typedef float f32x4 __attribute__((ext_vector_type(4)));
typedef float f32x2 __attribute__((ext_vector_type(2)));
typedef unsigned u32x4 __attribute__((ext_vector_type(4)));
typedef unsigned u32x2 __attribute__((ext_vector_type(2)));
#define LAS __attribute__((address_space(3)))
#define GAS __attribute__((address_space(1)))
#define RLX_AGENT __ATOMIC_RELAXED, __HIP_MEMORY_SCOPE_AGENT
typedef GAS unsigned gu32;
typedef GAS u64 gu64;
typedef __amdgpu_buffer_rsrc_t rsrc_t;

constexpr int L = 2048, D = 512, P = 512, F = 1792;
constexpr int KS2 = 88;
constexpr int SLOT = 53248;
constexpr int LDS_CTL = 3 * SLOT;
constexpr int LDS_BYTES = 163840;
constexpr int SC1 = 16;
constexpr size_t EPOCH_OFF = 0;
constexpr size_t G_OFF = 65536;
constexpr size_t E_OFF = 131072, E_BYTES = 16 * 512 * 16;
#define FS 128
constexpr unsigned FA_OFF = 262144, FB_OFF = FA_OFF + 1024 * FS, FC_OFF = FB_OFF + 1024 * FS;
constexpr size_t CT_OFF = 1u << 20, CT_BYTES = 1u << 20;
constexpr int HM = KS2 * 1024;
constexpr size_t H_OFF = CT_OFF + CT_BYTES, H_BYTES = (size_t)128 * HM;
constexpr u64 TAG_STEP = 0x9E3779B97F4A7C15ull;
#define OAUX 0
#define PSL 2
#define MAP2 1
#define ESL 4
#define DMAUX ""
#define PWL 1
#define NOG 0
#define GNA 3
#define GNB 4
#define SNB 4
#define PUBA 3
#define S1A 2
#define S2A 6
#define S3A 10
#define CHKB 3
constexpr int CODE_KB = 40;
constexpr unsigned OOR = 0x40000000u;
constexpr unsigned SPIN_CAP = 1u << 18;

#define BARRIER() asm volatile("s_waitcnt lgkmcnt(0)\n\ts_barrier" ::: "memory")
#define WAITV(n) asm volatile("s_waitcnt vmcnt(%0)" ::"i"(n) : "memory")
#define WAITL0() asm volatile("s_waitcnt lgkmcnt(0)" ::: "memory")
#define SCHED() __builtin_amdgcn_sched_barrier(0)
#define PIN(x) asm volatile("" : "+v"(x))

#define STAMP(id) do {} while (0)
#define LST(id) do {} while (0)

__device__ __forceinline__ float bf2f(u16 v) { return __uint_as_float(((unsigned)v) << 16); }
__device__ __forceinline__ float rbf(float f) { return (float)(__bf16)f; }
__device__ __forceinline__ unsigned pack2(float a, float b) {
  bf16x2 v = {(__bf16)a, (__bf16)b};
  return __builtin_bit_cast(unsigned, v);
}
__device__ __forceinline__ rsrc_t mkrsrc(const void* p, unsigned bytes) {
  return __builtin_amdgcn_make_buffer_rsrc((void*)p, 0, bytes, 0x00020000);
}
__device__ __forceinline__ u32x4 mkdesc(const void* p, unsigned bytes) {
  const u64 a = (u64)p;
  return (u32x4){(unsigned)a, (unsigned)(a >> 32) & 0xffffu, bytes, 0x00020000u};
}
__device__ __forceinline__ void dma16(u32x4 d, unsigned lds_dst, unsigned voff, unsigned soff) {
  asm volatile("s_mov_b32 m0, %2\n\ts_nop 3\n\tbuffer_load_dwordx4 %0, %1, %3 offen" DMAUX " lds" ::"v"(voff), "s"(d), "s"(lds_dst), "s"(soff) : "memory");
}
__device__ __forceinline__ u32x4 ld16_async(u32x4 d, unsigned voff, unsigned soff) {
  u32x4 r;
  asm volatile("s_nop 4\n\tbuffer_load_dwordx4 %0, %1, %2, %3 offen sc1" : "=v"(r) : "v"(voff), "s"(d), "s"(soff) : "memory");
  return r;
}
__device__ __forceinline__ u64 ld8_async(u32x4 d, unsigned voff) {
  u64 r;
  asm volatile("s_nop 4\n\tbuffer_load_dwordx2 %0, %1, %2, 0 offen sc1" : "=v"(r) : "v"(voff), "s"(d) : "memory");
  return r;
}
__device__ __forceinline__ void st16_async(u32x4 d, unsigned voff, u32x4 v) {
  asm volatile("s_nop 4\n\tbuffer_store_dwordx4 %0, %1, %2, 0 offen sc1\n\ts_nop 1" ::"v"(v), "v"(voff), "s"(d) : "memory");
}
__device__ __forceinline__ void lds_set(LAS char* p, unsigned v = 1u) { __hip_atomic_store((LAS unsigned*)p, v, __ATOMIC_RELAXED, __HIP_MEMORY_SCOPE_WORKGROUP); }
__device__ __forceinline__ void touch(u32x4 d, unsigned lds_dst, unsigned voff, unsigned soff) {
  asm volatile("s_mov_b32 m0, %2\n\ts_nop 3\n\tbuffer_load_dword %0, %1, %3 offen lds" ::"v"(voff), "s"(d), "s"(lds_dst), "s"(soff) : "memory");
}
__device__ __forceinline__ void lds_wait(LAS char* p, unsigned want = 1u) {
#pragma unroll 1
  for (unsigned spins = 0; spins < SPIN_CAP * 8; spins++) {
    if ((unsigned)__builtin_amdgcn_readfirstlane(__hip_atomic_load((LAS unsigned*)p, __ATOMIC_RELAXED, __HIP_MEMORY_SCOPE_WORKGROUP)) >= want) break;
    __builtin_amdgcn_s_sleep(1);
  }
  asm volatile("" ::: "memory");
}
__device__ __forceinline__ bf16x8 ld128(LAS char* p) { return *(LAS bf16x8*)p; }
__device__ __forceinline__ bf16x8 ldtr(LAS char* p0, LAS char* p1) {
  bf16x4 a = __builtin_amdgcn_ds_read_tr16_b64_v4bf16((LAS bf16x4*)p0);
  bf16x4 b = __builtin_amdgcn_ds_read_tr16_b64_v4bf16((LAS bf16x4*)p1);
  return __builtin_shufflevector(a, b, 0, 1, 2, 3, 4, 5, 6, 7);
}
__device__ __forceinline__ float sigmoidf_(float g) {
  return __builtin_amdgcn_rcpf(1.0f + __builtin_amdgcn_exp2f(-1.4426950408889634f * g));
}
__device__ __forceinline__ f32x2 fma2(f32x2 a, f32x2 b, f32x2 c) { return __builtin_elementwise_fma(a, b, c); }
__device__ __forceinline__ f32x2 lo2(f32x4 v) { return __builtin_shufflevector(v, v, 0, 1); }
__device__ __forceinline__ f32x2 hi2(f32x4 v) { return __builtin_shufflevector(v, v, 2, 3); }
__device__ __forceinline__ void csq(float& r, float& i) { const float t = r * r - i * i; i = 2.f * r * i; r = t; }

struct Params {
  const u16 *x, *lre, *lim, *bre, *bim, *cre, *cim, *ldt, *dsk, *wg, *wu, *wd, *gw;
  char* ws;
  float* out;
};

__device__ __forceinline__ void ssm_consts(u16 ldt, u16 lre, u16 lim, float& lr, float& li, float& cr, float& ci) {
  const float dt = rbf(expf(bf2f(ldt)));
  const float a = bf2f(lre), b = bf2f(lim);
  const float m = expf(dt * a);
  float s, c;
  sincosf(dt * b, &s, &c);
  lr = m * c;
  li = m * s;
  const float nr = lr - 1.0f, ni = li, rden = 1.0f / (a * a + b * b);
  cr = (nr * a + ni * b) * rden;
  ci = (ni * a - nr * b) * rden;
}
__device__ __forceinline__ void share2(float v, float (&o)[2]) {
  const auto t = __builtin_amdgcn_permlane16_swap(__float_as_uint(v), __float_as_uint(v), false, false);
  o[0] = __uint_as_float(t[0]); o[1] = __uint_as_float(t[1]);
}

__device__ __forceinline__ u64 ld_flag(rsrc_t r, unsigned voff, unsigned soff) {
  return __builtin_bit_cast(u64, __builtin_amdgcn_raw_buffer_load_b64(r, voff, soff, SC1));
}
__device__ __forceinline__ void st_flag(rsrc_t r, unsigned voff, unsigned soff, u64 tag) {
  __builtin_amdgcn_raw_buffer_store_b64(__builtin_bit_cast(u32x2, tag), r, voff, soff, SC1);
}
__device__ __forceinline__ void poll_flags(rsrc_t r, unsigned voff, unsigned soff, u64 tag) {
#pragma unroll 1
  for (unsigned spins = 0; spins < SPIN_CAP; spins++) {
    asm volatile("" ::: "memory");
    if (__all(ld_flag(r, voff, soff) == tag)) break;
    __builtin_amdgcn_s_sleep(PSL);
  }
  asm volatile("" ::: "memory");
}

template <bool GLU, int NR>
__device__ __forceinline__ void main_loop(LAS char* lds, int wr, int wc, int lane, f32x4 (&acc)[8][NR]) {
  const int i = lane & 15, g = lane >> 4;
  const int sw = (i >> 1) & 7;
  LAS char *resA[2], *rolA[4];
  if constexpr (GLU) {
    resA[0] = lds + (wr * 80 + i) * 128 + ((g ^ sw) << 4);
    resA[1] = lds + (wr * 80 + i) * 128 + ((g ^ sw ^ 4) << 4);
    const int q = (lane & 15) >> 2, pp = lane & 3;
    const int swz = q | ((g & 1) << 2);
#pragma unroll
    for (int c = 0; c < 4; c++) rolA[c] = lds + 20480 + (8 * g + q) * 256 + (((wc * 4 + c) ^ swz) << 5) + pp * 8;
  } else {
    resA[0] = lds + 32768 + (wc * 32 + i) * 128 + ((g ^ sw) << 4);
    resA[1] = lds + 32768 + (wc * 32 + i) * 128 + ((g ^ sw ^ 4) << 4);
    rolA[0] = lds + (wr * 128 + i) * 128 + ((g ^ sw) << 4);
    rolA[1] = lds + (wr * 128 + i) * 128 + ((g ^ sw ^ 4) << 4);
    rolA[2] = rolA[3] = lds;
  }
  auto ld_res = [&](int ks, int r) -> bf16x8 {
    if constexpr (GLU) return ld128(resA[ks] + r * 2048);
    else return ld128(resA[ks] + (r & 1) * 2048 + (r >> 1) * 8192);
  };
  auto ld_rol = [&](int j) -> bf16x8 {
    const int ks = j >> 3, c = j & 7;
    if constexpr (GLU) {
      LAS char* b = rolA[c & 3] + (c >> 2) * 16384 + ks * 8192;
      return ldtr(b, b + 1024);
    } else {
      return ld128(rolA[ks] + c * 2048);
    }
  };
#pragma unroll
  for (int c = 0; c < 8; c++)
#pragma unroll
    for (int r = 0; r < NR; r++) { acc[c][r] = (f32x4){0.f, 0.f, 0.f, 0.f}; PIN(acc[c][r]); }
  bf16x8 res[2][NR], rol[5];
  BARRIER();
  rol[0] = ld_rol(0); SCHED(); res[0][0] = ld_res(0, 0); SCHED(); res[0][1] = ld_res(0, 1); SCHED();
  rol[1] = ld_rol(1); SCHED(); res[0][2] = ld_res(0, 2); SCHED(); res[0][3] = ld_res(0, 3); SCHED();
  rol[2] = ld_rol(2); SCHED();
  if constexpr (NR > 4) res[0][NR - 1] = ld_res(0, NR - 1);
  SCHED();
  int slot = 0;
#pragma unroll 1
  for (unsigned s = 0; s < 8; s++) {
#pragma unroll
    for (int j = 0; j < 16; j++) {
      int nds = 0;
      constexpr int RD = GLU ? 2 : 1;
      if (j == 12) {
        const int delta = (slot == 2) ? -2 * SLOT : SLOT;
        slot = (slot == 2) ? 0 : slot + 1;
        resA[0] += delta; resA[1] += delta;
        rolA[0] += delta; rolA[1] += delta;
        PIN(resA[0]); PIN(resA[1]); PIN(rolA[0]); PIN(rolA[1]);
        if constexpr (GLU) { rolA[2] += delta; rolA[3] += delta; PIN(rolA[2]); PIN(rolA[3]); }
      }
      if (j == 13) {
        BARRIER();
        SCHED();
      }
      if (j != 12) { rol[(j + 3) & 3] = ld_rol((j + 3) & 15); nds += RD; }
      if (j == 10) { rol[4] = ld_rol(15); nds += RD; }
      if (j >= 2 && j < 2 + NR) { res[1][j - 2] = ld_res(1, j - 2); nds++; }
      if (j == 13) { res[0][0] = ld_res(0, 0); res[0][1] = ld_res(0, 1); nds += 2; }
      if (j == 14) { res[0][2] = ld_res(0, 2); res[0][3] = ld_res(0, 3); nds += 2; }
      if (j == 15 && NR > 4) { res[0][NR - 1] = ld_res(0, NR - 1); nds++; }
#pragma unroll
      for (int r = 0; r < NR; r++)
        acc[j & 7][r] = __builtin_amdgcn_mfma_f32_16x16x32_bf16(rol[j == 15 ? 4 : (j & 3)], res[j >> 3][r], acc[j & 7][r], 0, 0, 0);
#pragma unroll
      for (int r = 0; r < NR; r++) {
        __builtin_amdgcn_sched_group_barrier(0x8, 1, 0);
        if (j == 12) __builtin_amdgcn_sched_group_barrier(0x2, 2, 0);
        if (r < nds) __builtin_amdgcn_sched_group_barrier(0x100, 1, 0);
      }
      SCHED();
    }
  }
}

__device__ __forceinline__ constexpr int wslot(int i) { return (i / 3 == 0 ? 2 : (i / 3 == 1 ? 0 : 1)) * SLOT + (i % 3) * 4096; }
__device__ __forceinline__ constexpr int gstep(int i) { return i < 7 ? 2 * i : 2 * i - 13; }
constexpr int C_TAG = LDS_CTL, C_F0 = LDS_CTL + 16, C_F1 = LDS_CTL + 32, C_F2 = LDS_CTL + 48, C_F3 = LDS_CTL + 64, C_FA = LDS_CTL + 80;
constexpr int C_DUMP = LDS_CTL + 1024;

__global__ __launch_bounds__(512, 2) void k_fused(Params p) {
  extern __shared__ __attribute__((aligned(16))) char smem[];
  LAS char* lds = (LAS char*)smem;
  const int tid = threadIdx.x, lane = tid & 63;
  const int wid = __builtin_amdgcn_readfirstlane(tid >> 6);
  const int xg = blockIdx.x & 7, idx = blockIdx.x >> 3;
  int role, rt, ct = 0, myid;
  const int hh = xg & 1, cj = xg >> 1, odd = cj & 1;
  const int ncol = 4 - odd, col0 = (cj >> 1) * 7 + odd * 4, nssm = 4 + odd * 8;
  if (idx < nssm) {
    role = 1; rt = hh * 4 + (idx & 3); ct = (cj >> 1) * 4 + odd + (idx >> 2); myid = 182 + rt * 8 + ct;
  } else {
    const int e = idx - nssm, nfull = ncol * 6, nmid = hh ? ncol - 2 : 2;
    if (e < nfull) { role = 0; const int c = e / 6; rt = hh * 7 + (e - c * 6); ct = col0 + c; }
    else if (e < nfull + nmid) { role = 0; rt = 6; ct = col0 + hh * 2 + (e - nfull); }
    else if (!odd) { role = 2; rt = cj * 2 + hh * 2 + (e - nfull - nmid); }
    else { role = 3; rt = 0; }
    myid = role == 0 ? rt * 14 + ct : (role == 2 ? 246 + rt : 254 + (cj >> 1));
  }
  const int rb = MAP2 ? (xg >> 1) * 8 + (idx >> 2) : xg * 4 + (idx >> 3), cb = MAP2 ? (xg & 1) * 4 + (idx & 3) : idx & 7;
  const int w4 = wid & 3;
  const rsrc_t hr = mkrsrc(p.ws + H_OFF, (unsigned)H_BYTES), wsr = mkrsrc(p.ws, (unsigned)H_OFF);
  const int n = lane & 15, g = lane >> 4;
  STAMP(0);
  f32x4 acc2[4][4];
  u64 tag;
  unsigned vh[4];
  auto ldh = [&](int k, int m) -> bf16x8 {
    return __builtin_bit_cast(bf16x8, __builtin_amdgcn_raw_buffer_load_b128(hr, vh[m] + (k & 3) * 1024, (k & ~3) * 1024, SC1));
  };
  auto set_vh = [&]() { vh[1] = vh[0] + HM; vh[2] = vh[0] + 2 * HM; vh[3] = vh[0] + 3 * HM; PIN(vh[1]); PIN(vh[2]); PIN(vh[3]); };
  const unsigned gflag = (246 * 4 + rb) * FS;
  auto glu_flags = [&]() -> unsigned {
    const int l14 = lane < 14 ? lane : 0, j = l14 >= 7 ? l14 - 7 : l14, pr = 7 * w4 + j;
    const int mt = rb * 4 + (l14 >= 7 ? 3 : 0), om = mt < 120 ? mt / 5 : (mt < 123 ? 24 : 25);
    return lane == 14 ? gflag : (((om >> 1) * 14 + (pr >> 1)) * 4 + (om & 1) * 2 + (pr & 1)) * FS;
  };
  float gv[2];
  u32x2 xv[2], dv;
  auto ld_final = [&]() {
    int ln = lane;
    PIN(ln);
    const int t = rb * 64 + wid * 8 + (ln >> 4), dcol = cb * 64 + (ln & 15) * 4;
    const rsrc_t xr_ = mkrsrc(p.x, L * D * 2);
#pragma unroll
    for (int u = 0; u < 2; u++) {
      gv[u] = __uint_as_float(__builtin_amdgcn_raw_buffer_load_b32(wsr, t * 4 + u * 16, (unsigned)G_OFF, SC1));
      xv[u] = __builtin_amdgcn_raw_buffer_load_b64(xr_, t * 1024 + dcol * 2, u * 4096, 0);
    }
    dv = *(const u32x2*)(p.dsk + dcol);
  };

  LAS char *sb[2], *sa[2];
  f32x4 sk[2];
  float omg[2];
  unsigned vo;
  auto prep_final = [&]() {
    int ln = lane;
    PIN(ln);
    const int nn = ln & 15, mt = wid >> 1;
    const int ob = mt < 3 ? wslot(0) + mt * 4096 : wslot(3), oa = mt == 0 ? wslot(5) : wslot(6) + (mt - 1) * 4096;
    vo = ((rb * 64 + wid * 8 + (ln >> 4)) * D + cb * 64 + nn * 4) * 4;
#pragma unroll
    for (int u = 0; u < 2; u++) {
      const int sl = ((wid & 1) * 8 + u * 4 + (ln >> 4)) ^ nn;
      sb[u] = lds + ob + nn * 256 + (sl << 4);
      sa[u] = lds + oa + nn * 256 + ((sl ^ (4 * mt)) << 4);
      omg[u] = rbf(1.0f - gv[u]);
#pragma unroll
      for (int e = 0; e < 4; e++) {
        const unsigned xw = xv[u][e >> 1], dw = dv[e >> 1];
        const float xf = __uint_as_float((e & 1) ? (xw & 0xffff0000u) : (xw << 16));
        const float df = __uint_as_float((e & 1) ? (dw & 0xffff0000u) : (dw << 16));
        sk[u][e] = rbf(xf * df);
      }
      PIN(sb[u]); PIN(sa[u]); PIN(omg[u]); PIN(sk[u]);
    }
    PIN(vo);
  };

  if (wid >= 4) {
    const int lw = w4;
    const u32x4 wsd = mkdesc(p.ws, (unsigned)H_OFF);
    const u32x4 wdr = mkdesc(p.wd, F * D * 2), xr = mkdesc(p.x, L * D * 2);
    const unsigned vwd = (lw * 14 * 32 + (lane >> 3)) * 1024 + cb * 128 + (((lane & 7) ^ (((lane >> 4) & 3) << 1)) << 4);
    const int k0 = (2 * cj + 4 * hh) & 7, gam = NOG ? 0 : (rt + ct) & 1;
    u32x4 ctv;
    u64 cfl;
    auto ct_load = [&]() {
      const int b = blockIdx.x;
      ctv = ld16_async(mkdesc((b & 1) ? p.cim : p.cre, D * P * 2),
                       ((b >> 5) * 64 + ((b >> 1) & 3) * 16 + n) * 1024 + ((b >> 3) & 3) * 256 + lw * 64 + g * 16, 0);
    };
    auto ct_store = [&]() { st16_async(wsd, (unsigned)CT_OFF + (blockIdx.x * 4 + lw) * 1024 + lane * 16, ctv); };
    auto ct_flag = [&]() { st_flag(wsr, lane == 0 ? (blockIdx.x * 4 + lw) * FS : OOR, FC_OFF, tag); };
    const unsigned cfo = FC_OFF + ((cb * 4 + lw) * 32 + (lane & 31)) * FS;
    auto ct_ask = [&]() { cfl = ld8_async(wsd, cfo); };
    auto ct_check = [&]() { PIN(cfl); if (!__all(cfl == tag)) poll_flags(wsr, cfo, 0, tag); };
    u32x4 cf[32];
    const unsigned vcf = (cb * 4 + lw) * 32768 + lane * 16;
    auto ldc = [&](int a, int b) {
#pragma unroll
      for (int f = a; f < b; f++) cf[f] = ld16_async(wsd, vcf, (unsigned)CT_OFF + f * 1024);
    };
    auto wpiece = [&](int i, int k) { dma16(wdr, wslot(i < 9 ? i : i - 9) + lw * 12288 + k * 1024, vwd, (gstep(i) * 32 + k * 8) * 1024); };
    auto wround = [&](int r) {
#pragma unroll
      for (int q = 0; q < 12; q++) wpiece(r * 3 + (q >> 2), q & 3);
    };
    const u32x4 gr = mkdesc(p.wg, D * F * 2), ur = mkdesc(p.wu, D * F * 2);
    auto pre_1 = [&]() {
      const unsigned v = (((k0 + 2) & 7) * 64 + idx * 4 + lw) * (F * 2) + col0 * 256 + (lane < ncol * 16 ? lane : 0) * 16;
      dma16(gr, C_DUMP, v, 0);
      dma16(ur, C_DUMP, v, 0);
    };
    auto pre_w = [&]() { touch(wdr, C_DUMP, (lane < 14 ? lane : 0) * 128, (blockIdx.x * 4 + lw) * 1792); };
    auto loader = [&](auto piece, auto np_, auto na_, auto nb_) {
      constexpr int NP = decltype(np_)::value, NA = decltype(na_)::value, NB = decltype(nb_)::value;
      auto pieces = [&](int s, int a, int b) {
        const int sg = ((s ^ gam) + k0) & 7;
#pragma unroll
        for (int k = a; k < b; k++) piece(s, sg, k);
      };
      LST(27); pieces(0, 0, 1); LST(28); pieces(0, 1, NP); LST(29); ct_load(); pre_1(); if (!PWL) pre_w(); pieces(1, 0, S1A); LST(0); WAITV(4 - PWL + S1A); LST(1); BARRIER(); LST(2);
      tag = *(LAS u64*)(lds + C_TAG);
      pieces(1, S1A, NP); pieces(2, 0, S2A); LST(3); WAITV(S2A); LST(4); BARRIER(); LST(5);
      ct_store(); pieces(2, S2A, NP); pieces(3, 0, S3A); LST(6); WAITV(S3A); ct_flag(); LST(7); BARRIER(); LST(8);
      pieces(3, S3A, NP);
      pieces(4, 0, NP); LST(9); WAITV(NP); if (PWL) pre_w(); LST(10); BARRIER(); LST(11);
      if (NA) ct_ask();
      pieces(5, 0, NP); LST(12); WAITV(NP);
      if (NA) ct_check();
      ldc(0, NA); LST(13); BARRIER(); LST(14);
      pieces(6, 0, NP); ldc(NA, 2 * NA); LST(15); WAITV(NP + 2 * NA); LST(16); BARRIER(); LST(17);
      if (!NA) ct_ask();
      pieces(7, 0, NP); ldc(2 * NA, 3 * NA); LST(18); WAITV(NP + 2 * NA); LST(19); BARRIER(); LST(20);
      if (!NA) ct_check();
      wround(0); ldc(3 * NA, 3 * NA + NB); LST(21); WAITV(NA + 12 + NB); LST(22); BARRIER(); LST(23);
      wround(1); ldc(3 * NA + NB, 3 * NA + 2 * NB); LST(25); BARRIER(); LST(26);
      WAITV(NB);
      lds_set(lds + C_F0 + lw * 4);
    };
    if (role == 0) {
      const int r0 = min(rt * 160, L - 160);
      const int xrow = 8 * lw + (lane >> 3);
      const unsigned vx = (r0 + xrow) * 1024 + (((lane & 7) ^ ((xrow >> 1) & 7)) << 4);
      const int krow = 4 * lw + (lane >> 4);
      const int swz = (krow & 3) | (((krow >> 3) & 1) << 2);
      const unsigned vw = krow * (F * 2) + ct * 256 + (((lane & 15) ^ (swz << 1)) << 4);
      loader([&](int s, int sg, int k) {
        const unsigned b = (s % 3) * SLOT + lw * 1024;
        if (k < 5) dma16(xr, b + k * 4096, vx, sg * 128 + k * 32768);
        else { const int i = (k - 5) >> 1; dma16(((k - 5) & 1) ? ur : gr, b + 20480 + ((k - 5) & 1) * 16384 + i * 4096, vw, (sg * 64 + i * 16) * (F * 2)); }
      }, std::integral_constant<int, 13>{}, std::integral_constant<int, GNA>{}, std::integral_constant<int, GNB>{});
      STAMP(1);
      ldc(3 * GNA + 2 * GNB, 32);
      wround(2);
      WAITV(12);
    } else {
      if (role == 1) {
        const int r0 = rt * 256;
        const u32x4 br = mkdesc(p.bre, P * D * 2), bi = mkdesc(p.bim, P * D * 2);
        const int ii = 8 * (lw & 1) + (lane >> 3);
        const int csw = ((lane & 7) ^ ((ii >> 1) & 7)) << 4;
        const unsigned vx = (r0 + 32 * (ii >> 2) + 16 * (ii & 1) + ((ii >> 1) & 1) + 2 * (lw >> 1)) * 1024 + csw;
        const unsigned vb = (ct * 64 + 8 * lw + (lane >> 3)) * 1024 + csw;
        loader([&](int s, int sg, int k) {
          const unsigned b = (s % 3) * SLOT + lw * 1024;
          if (k < 4) dma16((k & 1) ? bi : br, b + 32768 + (k & 1) * 8192 + (k >> 1) * 4096, vb, sg * 128 + (k >> 1) * 32768);
          else { const int i = k - 4; dma16(xr, b + i * 4096, vx, sg * 128 + (128 * (i >> 2) + 4 * (i & 3)) * 1024); }
        }, std::integral_constant<int, 12>{}, std::integral_constant<int, 0>{}, std::integral_constant<int, SNB>{});
        wround(2);
        WAITV(12);
      } else {
        ct_load();
        BARRIER();
        tag = *(LAS u64*)(lds + C_TAG);
        WAITV(0); ct_store(); WAITV(0); ct_flag();
        pre_1(); pre_w();
        wround(0); wround(1); wround(2);
        WAITV(12);
        lds_set(lds + C_F0 + lw * 4);
        poll_flags(wsr, cfo, 0, tag);
        ldc(0, 2 * SNB);
        WAITV(0);
        if (myid == 255 && lw == 3) {
#pragma unroll 1
          for (int k = 0; k < 4; k++) poll_flags(wsr, (k * 64 + lane) * 4 * FS, FC_OFF, tag);
          st_flag(wsr, lane == 0 ? (unsigned)EPOCH_OFF : OOR, 0, tag);
        }
      }
      ldc(2 * SNB, 32);
      WAITV(0);
    }
    const unsigned fs = lane < 16 ? FA_OFF + glu_flags()
                                  : FB_OFF + ((182 + (rb >> 2) * 8 + lw * 2 + ((lane >> 1) & 1)) * 4 + (rb & 2) + (lane & 1)) * FS;
    vh[0] = rb * 4 * HM + (56 + lw * 8) * 1024 + lane * 16;
    set_vh();
    LAS char* pb[4];
#pragma unroll
    for (int k = 0; k < 4; k++) {
      const int mt = n >> 2;
      pb[k] = lds + (mt == 0 ? wslot(5) : wslot(6) + (mt - 1) * 4096) + lw * 12288 + g * 256 + ((((n & 3) ^ g) | ((k ^ mt) << 2)) << 4);
      PIN(pb[k]);
    }
    STAMP(2);
    lds_wait(lds + C_F1 + lw * 4);
    STAMP(3);
    {
      u64 miss = ~0ull;
      bool sent = false;
      auto look = [&]() {
        cfl = ld8_async(wsd, fs);
        WAITV(0);
        PIN(cfl);
        miss = __ballot(cfl != tag);
        if (!sent && (miss & 0xffffull) == 0) { sent = true; lds_set(lds + C_FA + lw * 4); }
      };
#pragma unroll
      for (int i = 9; i < 14; i++) {
#pragma unroll
        for (int k = 0; k < 4; k++) wpiece(i, k);
        if (!sent || i == 13) look();
        if (i == 9) lds_set(lds + C_F2 + lw * 4);
      }
      lds_set(lds + C_F2 + lw * 4, 2u);
      STAMP(6);
#pragma unroll 1
      for (unsigned spins = 0; miss != 0 && spins < SPIN_CAP; spins++) {
        __builtin_amdgcn_s_sleep(PSL);
        look();
      }
      if (!sent) lds_set(lds + C_FA + lw * 4);
      asm volatile("" ::: "memory");
    }
    STAMP(7);
    bf16x8 hf[3][4];
#pragma unroll
    for (int k = 0; k < 3; k++)
#pragma unroll
      for (int m = 0; m < 4; m++) hf[k][m] = ldh(k, m);
#pragma unroll
    for (int f = 0; f < 32; f++) PIN(cf[f]);
#pragma unroll
    for (int c = 0; c < 4; c++)
#pragma unroll
      for (int m = 0; m < 4; m++) acc2[c][m] = (f32x4){0.f, 0.f, 0.f, 0.f};
    SCHED();
#pragma unroll
    for (int k = 0; k < 8; k++) {
      if (k == 3) ld_final();
      if (k == 6) prep_final();
      if (k == 7) lds_wait(lds + C_F3 + lw * 4);
#pragma unroll
      for (int m = 0; m < 4; m++) {
#pragma unroll
        for (int c = 0; c < 4; c++)
          acc2[c][m] = __builtin_amdgcn_mfma_f32_16x16x32_bf16(__builtin_bit_cast(bf16x8, cf[c * 8 + (k & 1) * 4 + (k >> 1)]), hf[k % 3][m], acc2[c][m], 0, 0, 0);
        if (k + 3 < 8) hf[k % 3][m] = ldh(k + 3, m);
        if (k == 7 && m > 0) {
#pragma unroll
          for (int c = 0; c < 4; c++) *(LAS f32x4*)(pb[(m - 1) ^ c] + c * 1024) = acc2[c][m - 1];
        }
        SCHED();
      }
    }
    STAMP(8);
#pragma unroll
    for (int c = 0; c < 4; c++) *(LAS f32x4*)(pb[3 ^ c] + c * 1024) = acc2[c][3];
  } else {
    const int wr = wid >> 1, wc = wid & 1;
    const int chg = (ct * 64 + wc * 32 + (lane & 31)) & (P - 1);
    const u16 c_ldt = p.ldt[chg], c_lre = p.lre[chg], c_lim = p.lim[chg];
    if (wid >= 2 && idx * 2 + (wid & 1) < CODE_KB) {
      const u32x4 cd = mkdesc((const void*)(__builtin_amdgcn_s_getpc() & ~255ull), CODE_KB * 1024);
      dma16(cd, C_DUMP, lane * 16, (idx * 2 + (wid & 1)) * 1024);
    }
    if (wid == 0) {
      u64 w0;
      asm volatile("s_load_dwordx2 %0, %1, 0x0\n\ts_waitcnt lgkmcnt(0)" : "=s"(w0) : "s"(p.ws + EPOCH_OFF) : "memory");
      *(LAS u64*)(lds + C_TAG + lane * 8) = lane == 0 ? w0 + TAG_STEP : 0ull;
    }
    unsigned vfo = lane == 0 ? (myid * 4 + wid) * FS : OOR;
    unsigned vfi = glu_flags();
    vh[0] = rb * 4 * HM + wid * 14 * 1024 + lane * 16;
    LAS char* ra0 = lds + wid * 12288 + (4 * g + (n >> 2)) * 128 + (((2 * g + (n >> 3)) & 3) << 5) + (n & 3) * 8;
    PIN(vfo); PIN(vfi); PIN(vh[0]); PIN(ra0);
    bf16x8 wf[6][4];
    LAS char* ra[3][4];
    auto ldw = [&](int i, int c) -> bf16x8 {
      const int k = i < 9 ? i : i - 9;
      LAS char* a = ra[k / 3][c] + (k % 3) * 4096;
      return ldtr(a, a + 2048);
    };
    auto grab_w = [&]() {
#pragma unroll
      for (int c = 0; c < 4; c++) {
        ra[1][c] = (LAS char*)((unsigned)(size_t)ra0 ^ (c << 5));
        ra[0][c] = ra[1][c] + 2 * SLOT;
        ra[2][c] = ra[1][c] + SLOT;
      }
      lds_wait(lds + C_F0 + wid * 4);
#pragma unroll
      for (int i = 0; i < 5; i++)
#pragma unroll
        for (int c = 0; c < 4; c++) wf[i][c] = ldw(i, c);
      asm volatile("" ::: "memory");
      lds_set(lds + C_F1 + wid * 4);
    };
    if (role == 0) {
      const int mt0 = (min(rt * 160, L - 160) >> 4) + wr * 5;
      unsigned vs[5];
#pragma unroll
      for (int m = 0; m < 5; m++) { vs[m] = mt0 + m < rt * 10 ? OOR : (mt0 + m) * HM + (ct * 4 + wc * 2) * 1024 + lane * 16; PIN(vs[m]); }
      f32x4 acc[8][5];
      STAMP(1);
      main_loop<true, 5>(lds, wr, wc, lane, acc);
      STAMP(2);
      tag = *(LAS u64*)(lds + C_TAG);
#pragma unroll
      for (int h = 0; h < 2; h++) {
        if (h == 1) { SCHED(); STAMP(13); grab_w(); STAMP(14); SCHED(); }
#pragma unroll
        for (int m = 0; m < 5; m++) {
          if (h == 1 && m == PUBA) { WAITV(PUBA); st_flag(wsr, vfo, FA_OFF, tag); STAMP(3); }
          u32x4 o;
#pragma unroll
          for (int e = 0; e < 4; e++) {
            const int c = 2 * h + (e >> 1);
            const f32x2 gv_ = (e & 1) ? hi2(acc[c][m]) : lo2(acc[c][m]);
            const f32x2 uv = (e & 1) ? hi2(acc[4 + c][m]) : lo2(acc[4 + c][m]);
            f32x2 t = gv_ * -1.4426950408889634f;
            t = (f32x2){__builtin_amdgcn_exp2f(t.x), __builtin_amdgcn_exp2f(t.y)} + 1.0f;
            t = (f32x2){__builtin_amdgcn_rcpf(t.x), __builtin_amdgcn_rcpf(t.y)} * uv;
            o[e] = pack2(t.x, t.y);
          }
          __builtin_amdgcn_raw_buffer_store_b128(o, hr, vs[m] + h * 1024, 0, SC1);
        }
      }
    } else {
      if (role == 1) {
        float lr[2], li[2], cr[2], ci[2], sr[2], si[2], mr[2], mi[2], wr_[2], wi_[2], qr, qi, zr, zi;
        {
          float a2, a3;
          ssm_consts(c_ldt, c_lre, c_lim, qr, qi, a2, a3);
          share2(qr, lr); share2(qi, li); share2(2.f * a2, cr); share2(2.f * a3, ci);
          csq(qr, qi); csq(qr, qi); csq(qr, qi); csq(qr, qi);
          share2(qr, sr); share2(qi, si);
          csq(qr, qi);
          share2(qr, mr); share2(qi, mi);
          csq(qr, qi); csq(qr, qi);
          zr = qr; zi = qi;
          csq(zr, zi); csq(zr, zi); csq(zr, zi);
          if (rt * 2 + wr <= 8) { zr = 0.f; zi = 0.f; }
#pragma unroll
          for (int u = 0; u < 2; u++) {
            float ar = mr[u], ai = mi[u];
            csq(ar, ai);
            const float br_ = (g & 1) ? mr[u] : 1.f, bi_ = (g & 1) ? mi[u] : 0.f;
            wr_[u] = (g & 2) ? ar * br_ - ai * bi_ : br_;
            wi_[u] = (g & 2) ? ar * bi_ + ai * br_ : bi_;
            PIN(mr[u]); PIN(mi[u]); PIN(lr[u]); PIN(li[u]); PIN(cr[u]); PIN(ci[u]); PIN(sr[u]); PIN(si[u]); PIN(wr_[u]); PIN(wi_[u]);
          }
          PIN(qr); PIN(qi); PIN(zr); PIN(zi);
        }
        const int ck = rt * 2 + wr;
        unsigned veo = lane < 32 ? (unsigned)E_OFF + (ck * 512 + chg) * 16 : OOR;
        const int c0 = ck - 1 - 8 * (lane >> 5);
        int vei = (c0 * 512 + chg) * 16, nval = min(max(c0 + 1, 0), 8);
        const unsigned tb = (wid >> 1) * 2 * SLOT + 49152 + (wid & 1) * 2048;
        LAS char* twp = lds + tb + n * 64 + ((g ^ (((n >> 1) & 1) | ((((n >> 2) ^ (n >> 3)) & 1) << 1))) << 4);
        LAS char* trp[2];
#pragma unroll
        for (int t = 0; t < 2; t++)
          trp[t] = lds + tb + (8 * g + (n >> 2)) * 64 + (((((n >> 1) ^ (n >> 3)) & 1) | (((g ^ t) & 1) << 1)) << 4) + (n & 1) * 8;
        unsigned vso[8];
#pragma unroll
        for (int k = 0; k < 8; k++) { vso[k] = (rt * 16 + wr * 8 + k) * HM + (56 + (ct * 2 + wc) * 2) * 1024 + lane * 16; PIN(vso[k]); }
        PIN(veo); PIN(vei); PIN(nval); PIN(twp); PIN(trp[0]); PIN(trp[1]);
        f32x4 acc[8][4];
        STAMP(1);
        main_loop<false, 4>(lds, wr, wc, lane, acc);
        STAMP(2);
        tag = *(LAS u64*)(lds + C_TAG);
        f32x2 er[2], ei[2];
#pragma unroll
        for (int u = 0; u < 2; u++) { er[u] = lo2(acc[0][u]); ei[u] = lo2(acc[0][2 + u]); }
#pragma unroll
        for (int st = 1; st < 16; st++)
#pragma unroll
          for (int u = 0; u < 2; u++) {
            const f32x2 l2 = {lr[u], lr[u]}, i2 = {li[u], li[u]};
            const f32x2 vr = (st & 1) ? hi2(acc[st >> 1][u]) : lo2(acc[st >> 1][u]);
            const f32x2 vi = (st & 1) ? hi2(acc[st >> 1][2 + u]) : lo2(acc[st >> 1][2 + u]);
            const f32x2 t = fma2(-i2, ei[u], fma2(l2, er[u], vr));
            ei[u] = fma2(i2, er[u], fma2(l2, ei[u], vi));
            er[u] = t;
          }
        float xs[2][4], ys[2][4];
#pragma unroll
        for (int u = 0; u < 2; u++) {
          const float a = fmaf(-si[u], ei[u].x, fmaf(sr[u], er[u].x, er[u].y)), b = fmaf(si[u], er[u].x, fmaf(sr[u], ei[u].x, ei[u].y));
          float a2[2], b2[2];
          share2(a, a2); share2(b, b2);
#pragma unroll
          for (int k = 0; k < 2; k++) {
            const auto ta = __builtin_amdgcn_permlane32_swap(__float_as_uint(a2[k]), __float_as_uint(a2[k]), false, false);
            const auto tb_ = __builtin_amdgcn_permlane32_swap(__float_as_uint(b2[k]), __float_as_uint(b2[k]), false, false);
            xs[u][k] = __uint_as_float(ta[0]); xs[u][2 + k] = __uint_as_float(ta[1]);
            ys[u][k] = __uint_as_float(tb_[0]); ys[u][2 + k] = __uint_as_float(tb_[1]);
          }
#pragma unroll
          for (int k = 1; k < 4; k++) {
            xs[u][k] = fmaf(-mi[u], ys[u][k - 1], fmaf(mr[u], xs[u][k - 1], xs[u][k]));
            ys[u][k] = fmaf(mi[u], xs[u][k - 1], fmaf(mr[u], ys[u][k - 1], ys[u][k]));
            PIN(xs[u][k]); PIN(ys[u][k]);
          }
        }
        const unsigned tg = (unsigned)tag;
        __builtin_amdgcn_raw_buffer_store_b128((u32x4){__float_as_uint(g ? xs[1][3] : xs[0][3]), tg, __float_as_uint(g ? ys[1][3] : ys[0][3]), tg},
                                               wsr, veo, 0, SC1);
        STAMP(3);
        SCHED();
        float pr[2], pi[2];
#pragma unroll
        for (int u = 0; u < 2; u++) {
          pr[u] = g == 0 ? 0.f : (g == 1 ? xs[u][0] : (g == 2 ? xs[u][1] : xs[u][2]));
          pi[u] = g == 0 ? 0.f : (g == 1 ? ys[u][0] : (g == 2 ? ys[u][1] : ys[u][2]));
          PIN(pr[u]); PIN(pi[u]);
        }
        const rsrc_t erc = mkrsrc(p.ws + E_OFF, (unsigned)E_BYTES);
        u32x4 v[8];
        auto ask = [&]() {
#pragma unroll
          for (int d = 0; d < 8; d++) v[d] = __builtin_amdgcn_raw_buffer_load_b128(erc, vei - d * 8192, 0, SC1);
        };
#pragma unroll
        for (int u = 0; u < 2; u++) {
          if (u == 1) { SCHED(); ask(); SCHED(); }
          PIN(cr[u]);
#pragma unroll
          for (int m = 0; m < 8; m++) {
            const f32x4 a = acc[m][u], b = acc[m][2 + u];
            acc[m][u] = cr[u] * a - ci[u] * b;
            acc[m][2 + u] = cr[u] * b + ci[u] * a;
            PIN(acc[m][u]); PIN(acc[m][2 + u]);
          }
        }
#pragma unroll 1
        for (unsigned spins = 0; spins < SPIN_CAP; spins++) {
          int cnt = 0;
#pragma unroll
          for (int d = 0; d < 8; d++) cnt += ((v[d][1] ^ tg) | (v[d][3] ^ tg)) == 0u;
          if (__all(cnt >= nval)) break;
          asm volatile("" ::: "memory");
          __builtin_amdgcn_s_sleep(ESL);
          ask();
        }
        float kr[2], ki[2];
        {
          float xr_ = __uint_as_float(v[7][0]), xi_ = __uint_as_float(v[7][2]);
#pragma unroll
          for (int d = 6; d >= 0; d--) {
            const float t = fmaf(-qi, xi_, fmaf(qr, xr_, __uint_as_float(v[d][0])));
            xi_ = fmaf(qi, xr_, fmaf(qr, xi_, __uint_as_float(v[d][2])));
            xr_ = t;
            PIN(xr_); PIN(xi_);
          }
          const auto ta = __builtin_amdgcn_permlane32_swap(__float_as_uint(xr_), __float_as_uint(xr_), false, false);
          const auto tb_ = __builtin_amdgcn_permlane32_swap(__float_as_uint(xi_), __float_as_uint(xi_), false, false);
          const float fr = __uint_as_float(ta[1]), fi = __uint_as_float(tb_[1]);
          share2(fmaf(-zi, fi, fmaf(zr, fr, __uint_as_float(ta[0]))), kr);
          share2(fmaf(zi, fr, fmaf(zr, fi, __uint_as_float(tb_[0]))), ki);
        }
        STAMP(4);
        f32x2 hr_[2], hi_[2];
#pragma unroll
        for (int u = 0; u < 2; u++) {
          f32x2 a, b;
          a.x = fmaf(-wi_[u], ki[u], fmaf(wr_[u], kr[u], pr[u])); b.x = fmaf(wi_[u], kr[u], fmaf(wr_[u], ki[u], pi[u]));
          a.y = fmaf(-si[u], b.x, fmaf(sr[u], a.x, er[u].x)); b.y = fmaf(si[u], a.x, fmaf(sr[u], b.x, ei[u].x));
          PIN(a); PIN(b);
          hr_[u] = cr[u] * a - ci[u] * b; hi_[u] = cr[u] * b + ci[u] * a;
        }
        bf16x8 tv[4];
#pragma unroll
        for (int qt = 0; qt < 5; qt++) {
          u32x4 o[2][2];
          if (qt < 4) {
#pragma unroll
            for (int mm = 0; mm < 2; mm++)
#pragma unroll
              for (int u = 0; u < 2; u++) {
                const int m = qt * 2 + mm;
                const f32x2 l2 = {lr[u], lr[u]}, i2 = {li[u], li[u]};
                const f32x2 r0_ = fma2(-i2, hi_[u], fma2(l2, hr_[u], lo2(acc[m][u])));
                const f32x2 i0_ = fma2(i2, hr_[u], fma2(l2, hi_[u], lo2(acc[m][2 + u])));
                hr_[u] = fma2(-i2, i0_, fma2(l2, r0_, hi2(acc[m][u])));
                hi_[u] = fma2(i2, r0_, fma2(l2, i0_, hi2(acc[m][2 + u])));
                o[0][u][mm] = pack2(r0_.x, hr_[u].x); o[0][u][2 + mm] = pack2(r0_.y, hr_[u].y);
                o[1][u][mm] = pack2(-i0_.x, -hi_[u].x); o[1][u][2 + mm] = pack2(-i0_.y, -hi_[u].y);
              }
          }
          SCHED();
          if (qt > 0) {
#pragma unroll
            for (int k = 0; k < 4; k++) __builtin_amdgcn_raw_buffer_store_b128(__builtin_bit_cast(u32x4, tv[k]), hr, vso[(k & 1) * 4 + qt - 1] + (k >> 1) * 1024, 0, SC1);
          }
          if (qt < 4) {
#pragma unroll
            for (int part = 0; part < 2; part++) {
              asm volatile("" ::: "memory");
              *(LAS u32x4*)twp = o[part][0];
              *(LAS u32x4*)(twp + 1024) = o[part][1];
              asm volatile("" ::: "memory");
#pragma unroll
              for (int rho = 0; rho < 2; rho++) tv[part * 2 + rho] = ldtr(trp[rho], trp[rho ^ 1] + 256);
            }
          }
          SCHED();
        }
        STAMP(13);
      } else {
        BARRIER();
        tag = *(LAS u64*)(lds + C_TAG);
        if (role == 2) {
          const u32x4 gwv = *(const u32x4*)(p.gw + lane * 8);
          f32x2 gwf[4];
#pragma unroll
          for (int e = 0; e < 4; e++) gwf[e] = (f32x2){__uint_as_float(gwv[e] << 16), __uint_as_float(gwv[e] & 0xffff0000u)};
          const bf16x2 ones = {(__bf16)1.0f, (__bf16)1.0f};
#pragma unroll 1
          for (int it = 0; it < 4; it++) {
            const int r0 = rt * 256 + wid * 64 + it * 16;
            u32x4 xw[16];
#pragma unroll
            for (int r = 0; r < 16; r++) xw[r] = *(const u32x4*)(p.x + (size_t)(r0 + r) * D + lane * 8);
            float s[16];
#pragma unroll
            for (int r = 0; r < 16; r++) {
              s[r] = 0.f;
#pragma unroll
              for (int e = 0; e < 4; e++) {
                const unsigned w = xw[r][e];
                const f32x2 pr = (f32x2){__uint_as_float(w << 16), __uint_as_float(w & 0xffff0000u)} * gwf[e];
                const bf16x2 pb = {(__bf16)pr.x, (__bf16)pr.y};
                s[r] = __builtin_amdgcn_fdot2_f32_bf16(pb, ones, s[r], false);
              }
            }
#pragma unroll
            for (int r = 0; r < 8; r++) {
              const auto t = __builtin_amdgcn_permlane32_swap(__float_as_uint(s[r]), __float_as_uint(s[r + 8]), false, false);
              s[r] = __uint_as_float(t[0]) + __uint_as_float(t[1]);
            }
#pragma unroll
            for (int r = 0; r < 4; r++) {
              const auto t = __builtin_amdgcn_permlane16_swap(__float_as_uint(s[r]), __float_as_uint(s[r + 4]), false, false);
              s[r] = __uint_as_float(t[0]) + __uint_as_float(t[1]);
            }
#pragma unroll
            for (int r = 0; r < 4; r++) {
              s[r] += __uint_as_float(__builtin_amdgcn_update_dpp(0, __float_as_uint(s[r]), 0x128, 0xf, 0xf, false));
              s[r] += __uint_as_float(__builtin_amdgcn_update_dpp(0, __float_as_uint(s[r]), 0x124, 0xf, 0xf, false));
              s[r] += __uint_as_float(__builtin_amdgcn_update_dpp(0, __float_as_uint(s[r]), 0x122, 0xf, 0xf, false));
              s[r] += __uint_as_float(__builtin_amdgcn_update_dpp(0, __float_as_uint(s[r]), 0x121, 0xf, 0xf, false));
            }
            const float sv = (n & 2) ? ((n & 1) ? s[3] : s[2]) : ((n & 1) ? s[1] : s[0]);
            __builtin_amdgcn_raw_buffer_store_b32(__float_as_uint(rbf(sigmoidf_(rbf(sv)))), wsr,
                                                  n < 4 ? (unsigned)G_OFF + (r0 + n + 4 * (g & 1) + 8 * (g >> 1)) * 4 : OOR, 0, SC1);
          }
          WAITV(0);
          st_flag(wsr, vfo, FA_OFF, tag);
        }
      }
      grab_w();
    }
    STAMP(5);
    set_vh();
    const bool early = __builtin_amdgcn_readfirstlane(__hip_atomic_load((LAS unsigned*)(lds + C_FA + wid * 4), __ATOMIC_RELAXED, __HIP_MEMORY_SCOPE_WORKGROUP));
    if (!early) {
      WAITV(0);
      st_flag(wsr, vfo, FB_OFF, tag);
      lds_wait(lds + C_FA + wid * 4);
    }
    asm volatile("" ::: "memory");
    STAMP(7);
    bf16x8 hf[4][4];
#pragma unroll
    for (int k = 0; k < 4; k++)
#pragma unroll
      for (int m = 0; m < 4; m++) hf[k][m] = ldh(gstep(k), m);
    if (early) { WAITV(16); st_flag(wsr, vfo, FB_OFF, tag); }
    STAMP(6);
#pragma unroll
    for (int c = 0; c < 4; c++)
#pragma unroll
      for (int m = 0; m < 4; m++) acc2[c][m] = (f32x4){0.f, 0.f, 0.f, 0.f};
    LAS char* pw[4];
#pragma unroll
    for (int c = 0; c < 4; c++) { pw[c] = lds + wid * 12288 + c * 1024 + g * 256 + ((n ^ (4 * c + g)) << 4); PIN(pw[c]); }
    SCHED();
    u64 fbv = 0;
#pragma unroll
    for (int k = 0; k < 14; k++) {
      if (k == 2) fbv = ld_flag(wsr, vfi, FB_OFF);
      if (k == 3) { if (!__all(fbv == tag)) poll_flags(wsr, vfi, FB_OFF, tag); asm volatile("" ::: "memory"); STAMP(12); }
      if (k == 5) lds_wait(lds + C_F2 + wid * 4);
      if (k == 8) lds_wait(lds + C_F2 + wid * 4, 2u);
      if (k == 9) lds_set(lds + C_F3 + wid * 4);
      if (k == 7) ld_final();
      if (k == 11) prep_final();
#pragma unroll
      for (int m = 0; m < 4; m++) {
        if (k + 1 >= 5 && k + 1 < 14) wf[(k + 1) % 6][m] = ldw(k + 1, m);
#pragma unroll
        for (int c = 0; c < 4; c++) acc2[c][m] = __builtin_amdgcn_mfma_f32_16x16x32_bf16(wf[k % 6][c], hf[k & 3][m], acc2[c][m], 0, 0, 0);
        if (k + 4 < 14) hf[k & 3][m] = ldh(gstep(k + 4), m);
        if (k == 13 && m > 0) {
#pragma unroll
          for (int c = 0; c < 4; c++) *(LAS f32x4*)(pw[c] + wslot(m - 1)) = acc2[c][m - 1];
        }
        SCHED();
      }
    }
    STAMP(8);
#pragma unroll
    for (int c = 0; c < 4; c++) *(LAS f32x4*)(pw[c] + wslot(3)) = acc2[c][3];
  }
  BARRIER();
  STAMP(9);
  {
    const rsrc_t orc = mkrsrc(p.out, L * D * 4);
#pragma unroll
    for (int u = 0; u < 2; u++) {
      f32x4 b = *(LAS f32x4*)sb[u], a = sk[u] + *(LAS f32x4*)sa[u];
#pragma unroll
      for (int w = 1; w < 4; w++) {
        b += *(LAS f32x4*)(sb[u] + w * 12288);
        a += *(LAS f32x4*)(sa[u] + w * 12288);
      }
      const f32x4 o = gv[u] * a + omg[u] * b;
      __builtin_amdgcn_raw_buffer_store_b128(__builtin_bit_cast(u32x4, o), orc, vo + u * 8192, 0, OAUX);
    }
  }
  STAMP(10);
}

extern "C" void kernel_launch(void* const* d_in, const int* in_sizes, int n_in, void* d_out, int out_size, void* d_ws,
                              size_t ws_size, hipStream_t stream) {
  Params p;
  p.x = (const u16*)d_in[0]; p.lre = (const u16*)d_in[1]; p.lim = (const u16*)d_in[2];
  p.bre = (const u16*)d_in[3]; p.bim = (const u16*)d_in[4]; p.cre = (const u16*)d_in[5]; p.cim = (const u16*)d_in[6];
  p.ldt = (const u16*)d_in[7]; p.dsk = (const u16*)d_in[8];
  p.wg = (const u16*)d_in[10]; p.wu = (const u16*)d_in[11]; p.wd = (const u16*)d_in[12]; p.gw = (const u16*)d_in[13];
  p.ws = (char*)d_ws; p.out = (float*)d_out;
  (void)hipFuncSetAttribute((const void*)k_fused, hipFuncAttributeMaxDynamicSharedMemorySize, LDS_BYTES);
  k_fused<<<256, 512, LDS_BYTES, stream>>>(p);
}
```

```cpp
#define PROBE 99
#define PROBE_ROLE 0
#define PROBE_HALF 0
#include <hip/hip_runtime.h>
#include <stdint.h>

typedef unsigned short u16;
typedef unsigned long long u64;
typedef __bf16 bf16x8 __attribute__((ext_vector_type(8)));
typedef __bf16 bf16x4 __attribute__((ext_vector_type(4)));
typedef __bf16 bf16x2 __attribute__((ext_vector_type(2)));
typedef float f32x4 __attribute__((ext_vector_type(4)));
typedef float f32x2 __attribute__((ext_vector_type(2)));
typedef unsigned u32x4 __attribute__((ext_vector_type(4)));
typedef unsigned u32x2 __attribute__((ext_vector_type(2)));
#define LAS __attribute__((address_space(3)))
#define GAS __attribute__((address_space(1)))
#define RLX_AGENT __ATOMIC_RELAXED, __HIP_MEMORY_SCOPE_AGENT
typedef GAS unsigned gu32;
typedef GAS u64 gu64;
typedef __amdgpu_buffer_rsrc_t rsrc_t;

constexpr int L = 2048, D = 512, P = 512, F = 1792;
constexpr int KS2 = 88;
constexpr int SLOT = 53248;
constexpr int LDS_CTL = 3 * SLOT;
constexpr int LDS_BYTES = 163840;
constexpr int SC1 = 16;
constexpr size_t EPOCH_OFF = 0;
constexpr size_t G_OFF = 65536;
constexpr size_t E_OFF = 131072, E_BYTES = 16 * 512 * 16;
#define FS 128
constexpr unsigned FA_OFF = 262144, FB_OFF = FA_OFF + 1024 * FS, FC_OFF = FB_OFF + 1024 * FS;
constexpr size_t CT_OFF = 1u << 20, CT_BYTES = 1u << 20;
constexpr int HM = KS2 * 1024;
constexpr size_t H_OFF = CT_OFF + CT_BYTES, H_BYTES = (size_t)128 * HM;
constexpr u64 TAG_STEP = 0x9E3779B97F4A7C15ull;
#define OAUX 0
#define PSL 2
#define MAP2 1
#define ESL 4
#define DMAUX ""
#define PWL 1
#define NOG 0
#define GNA 3
#define GNB 4
#define SNB 4
#define PUBA 3
#define S1A 2
#define S2A 6
#define S3A 10
#define CHKB 3
constexpr int CODE_KB = 40;
constexpr unsigned OOR = 0x40000000u;
constexpr unsigned SPIN_CAP = 1u << 18;

#define BARRIER() asm volatile("s_waitcnt lgkmcnt(0)\n\ts_barrier" ::: "memory")
#define WAITV(n) asm volatile("s_waitcnt vmcnt(%0)" ::"i"(n) : "memory")
#define WAITL0() asm volatile("s_waitcnt lgkmcnt(0)" ::: "memory")
#define SCHED() __builtin_amdgcn_sched_barrier(0)
#define PIN(x) asm volatile("" : "+v"(x))

#define LST(id) do {} while (0)
#define STAMP(id) do { if ((id) == 0) { pt0 = pm = __builtin_amdgcn_s_memrealtime(); } else if ((id) == PROBE) { SCHED(); if (role == PROBE_ROLE && (wid >> 2) == PROBE_HALF) pm = __builtin_amdgcn_s_memrealtime(); SCHED(); } } while (0)

__device__ __forceinline__ float bf2f(u16 v) { return __uint_as_float(((unsigned)v) << 16); }
__device__ __forceinline__ float rbf(float f) { return (float)(__bf16)f; }
__device__ __forceinline__ unsigned pack2(float a, float b) {
  bf16x2 v = {(__bf16)a, (__bf16)b};
  return __builtin_bit_cast(unsigned, v);
}
__device__ __forceinline__ rsrc_t mkrsrc(const void* p, unsigned bytes) {
  return __builtin_amdgcn_make_buffer_rsrc((void*)p, 0, bytes, 0x00020000);
}
__device__ __forceinline__ u32x4 mkdesc(const void* p, unsigned bytes) {
  const u64 a = (u64)p;
  return (u32x4){(unsigned)a, (unsigned)(a >> 32) & 0xffffu, bytes, 0x00020000u};
}
__device__ __forceinline__ void dma16(u32x4 d, unsigned lds_dst, unsigned voff, unsigned soff) {
  asm volatile("s_mov_b32 m0, %2\n\ts_nop 3\n\tbuffer_load_dwordx4 %0, %1, %3 offen" DMAUX " lds" ::"v"(voff), "s"(d), "s"(lds_dst), "s"(soff) : "memory");
}
__device__ __forceinline__ u32x4 ld16_async(u32x4 d, unsigned voff, unsigned soff) {
  u32x4 r;
  asm volatile("s_nop 4\n\tbuffer_load_dwordx4 %0, %1, %2, %3 offen sc1" : "=v"(r) : "v"(voff), "s"(d), "s"(soff) : "memory");
  return r;
}
__device__ __forceinline__ u64 ld8_async(u32x4 d, unsigned voff) {
  u64 r;
  asm volatile("s_nop 4\n\tbuffer_load_dwordx2 %0, %1, %2, 0 offen sc1" : "=v"(r) : "v"(voff), "s"(d) : "memory");
  return r;
}
__device__ __forceinline__ void st16_async(u32x4 d, unsigned voff, u32x4 v) {
  asm volatile("s_nop 4\n\tbuffer_store_dwordx4 %0, %1, %2, 0 offen sc1\n\ts_nop 1" ::"v"(v), "v"(voff), "s"(d) : "memory");
}
__device__ __forceinline__ void lds_set(LAS char* p, unsigned v = 1u) { __hip_atomic_store((LAS unsigned*)p, v, __ATOMIC_RELAXED, __HIP_MEMORY_SCOPE_WORKGROUP); }
__device__ __forceinline__ void touch(u32x4 d, unsigned lds_dst, unsigned voff, unsigned soff) {
  asm volatile("s_mov_b32 m0, %2\n\ts_nop 3\n\tbuffer_load_dword %0, %1, %3 offen lds" ::"v"(voff), "s"(d), "s"(lds_dst), "s"(soff) : "memory");
}
__device__ __forceinline__ void lds_wait(LAS char* p, unsigned want = 1u) {
#pragma unroll 1
  for (unsigned spins = 0; spins < SPIN_CAP * 8; spins++) {
    if ((unsigned)__builtin_amdgcn_readfirstlane(__hip_atomic_load((LAS unsigned*)p, __ATOMIC_RELAXED, __HIP_MEMORY_SCOPE_WORKGROUP)) >= want) break;
    __builtin_amdgcn_s_sleep(1);
  }
  asm volatile("" ::: "memory");
}
__device__ __forceinline__ bf16x8 ld128(LAS char* p) { return *(LAS bf16x8*)p; }
__device__ __forceinline__ bf16x8 ldtr(LAS char* p0, LAS char* p1) {
  bf16x4 a = __builtin_amdgcn_ds_read_tr16_b64_v4bf16((LAS bf16x4*)p0);
  bf16x4 b = __builtin_amdgcn_ds_read_tr16_b64_v4bf16((LAS bf16x4*)p1);
  return __builtin_shufflevector(a, b, 0, 1, 2, 3, 4, 5, 6, 7);
}
__device__ __forceinline__ float sigmoidf_(float g) {
  return __builtin_amdgcn_rcpf(1.0f + __builtin_amdgcn_exp2f(-1.4426950408889634f * g));
}
__device__ __forceinline__ f32x2 fma2(f32x2 a, f32x2 b, f32x2 c) { return __builtin_elementwise_fma(a, b, c); }
__device__ __forceinline__ f32x2 lo2(f32x4 v) { return __builtin_shufflevector(v, v, 0, 1); }
__device__ __forceinline__ f32x2 hi2(f32x4 v) { return __builtin_shufflevector(v, v, 2, 3); }
__device__ __forceinline__ void csq(float& r, float& i) { const float t = r * r - i * i; i = 2.f * r * i; r = t; }

struct Params {
  const u16 *x, *lre, *lim, *bre, *bim, *cre, *cim, *ldt, *dsk, *wg, *wu, *wd, *gw;
  char* ws;
  float* out;
};

__device__ __forceinline__ void ssm_consts(u16 ldt, u16 lre, u16 lim, float& lr, float& li, float& cr, float& ci) {
  const float dt = rbf(expf(bf2f(ldt)));
  const float a = bf2f(lre), b = bf2f(lim);
  const float m = expf(dt * a);
  float s, c;
  sincosf(dt * b, &s, &c);
  lr = m * c;
  li = m * s;
  const float nr = lr - 1.0f, ni = li, rden = 1.0f / (a * a + b * b);
  cr = (nr * a + ni * b) * rden;
  ci = (ni * a - nr * b) * rden;
}
__device__ __forceinline__ void share2(float v, float (&o)[2]) {
  const auto t = __builtin_amdgcn_permlane16_swap(__float_as_uint(v), __float_as_uint(v), false, false);
  o[0] = __uint_as_float(t[0]); o[1] = __uint_as_float(t[1]);
}

__device__ __forceinline__ u64 ld_flag(rsrc_t r, unsigned voff, unsigned soff) {
  return __builtin_bit_cast(u64, __builtin_amdgcn_raw_buffer_load_b64(r, voff, soff, SC1));
}
__device__ __forceinline__ void st_flag(rsrc_t r, unsigned voff, unsigned soff, u64 tag) {
  __builtin_amdgcn_raw_buffer_store_b64(__builtin_bit_cast(u32x2, tag), r, voff, soff, SC1);
}
__device__ __forceinline__ void poll_flags(rsrc_t r, unsigned voff, unsigned soff, u64 tag) {
#pragma unroll 1
  for (unsigned spins = 0; spins < SPIN_CAP; spins++) {
    asm volatile("" ::: "memory");
    if (__all(ld_flag(r, voff, soff) == tag)) break;
    __builtin_amdgcn_s_sleep(PSL);
  }
  asm volatile("" ::: "memory");
}

template <bool GLU, int NR>
__device__ __forceinline__ void main_loop(LAS char* lds, int wr, int wc, int lane, f32x4 (&acc)[8][NR]) {
  const int i = lane & 15, g = lane >> 4;
  const int sw = (i >> 1) & 7;
  LAS char *resA[2], *rolA[4];
  if constexpr (GLU) {
    resA[0] = lds + (wr * 80 + i) * 128 + ((g ^ sw) << 4);
    resA[1] = lds + (wr * 80 + i) * 128 + ((g ^ sw ^ 4) << 4);
    const int q = (lane & 15) >> 2, pp = lane & 3;
    const int swz = q | ((g & 1) << 2);
#pragma unroll
    for (int c = 0; c < 4; c++) rolA[c] = lds + 20480 + (8 * g + q) * 256 + (((wc * 4 + c) ^ swz) << 5) + pp * 8;
  } else {
    resA[0] = lds + 32768 + (wc * 32 + i) * 128 + ((g ^ sw) << 4);
    resA[1] = lds + 32768 + (wc * 32 + i) * 128 + ((g ^ sw ^ 4) << 4);
    rolA[0] = lds + (wr * 128 + i) * 128 + ((g ^ sw) << 4);
    rolA[1] = lds + (wr * 128 + i) * 128 + ((g ^ sw ^ 4) << 4);
    rolA[2] = rolA[3] = lds;
  }
  auto ld_res = [&](int ks, int r) -> bf16x8 {
    if constexpr (GLU) return ld128(resA[ks] + r * 2048);
    else return ld128(resA[ks] + (r & 1) * 2048 + (r >> 1) * 8192);
  };
  auto ld_rol = [&](int j) -> bf16x8 {
    const int ks = j >> 3, c = j & 7;
    if constexpr (GLU) {
      LAS char* b = rolA[c & 3] + (c >> 2) * 16384 + ks * 8192;
      return ldtr(b, b + 1024);
    } else {
      return ld128(rolA[ks] + c * 2048);
    }
  };
#pragma unroll
  for (int c = 0; c < 8; c++)
#pragma unroll
    for (int r = 0; r < NR; r++) { acc[c][r] = (f32x4){0.f, 0.f, 0.f, 0.f}; PIN(acc[c][r]); }
  bf16x8 res[2][NR], rol[5];
  BARRIER();
  rol[0] = ld_rol(0); SCHED(); res[0][0] = ld_res(0, 0); SCHED(); res[0][1] = ld_res(0, 1); SCHED();
  rol[1] = ld_rol(1); SCHED(); res[0][2] = ld_res(0, 2); SCHED(); res[0][3] = ld_res(0, 3); SCHED();
  rol[2] = ld_rol(2); SCHED();
  if constexpr (NR > 4) res[0][NR - 1] = ld_res(0, NR - 1);
  SCHED();
  int slot = 0;
#pragma unroll 1
  for (unsigned s = 0; s < 8; s++) {
#pragma unroll
    for (int j = 0; j < 16; j++) {
      int nds = 0;
      constexpr int RD = GLU ? 2 : 1;
      if (j == 12) {
        const int delta = (slot == 2) ? -2 * SLOT : SLOT;
        slot = (slot == 2) ? 0 : slot + 1;
        resA[0] += delta; resA[1] += delta;
        rolA[0] += delta; rolA[1] += delta;
        PIN(resA[0]); PIN(resA[1]); PIN(rolA[0]); PIN(rolA[1]);
        if constexpr (GLU) { rolA[2] += delta; rolA[3] += delta; PIN(rolA[2]); PIN(rolA[3]); }
      }
      if (j == 13) {
        BARRIER();
        SCHED();
      }
      if (j != 12) { rol[(j + 3) & 3] = ld_rol((j + 3) & 15); nds += RD; }
      if (j == 10) { rol[4] = ld_rol(15); nds += RD; }
      if (j >= 2 && j < 2 + NR) { res[1][j - 2] = ld_res(1, j - 2); nds++; }
      if (j == 13) { res[0][0] = ld_res(0, 0); res[0][1] = ld_res(0, 1); nds += 2; }
      if (j == 14) { res[0][2] = ld_res(0, 2); res[0][3] = ld_res(0, 3); nds += 2; }
      if (j == 15 && NR > 4) { res[0][NR - 1] = ld_res(0, NR - 1); nds++; }
#pragma unroll
      for (int r = 0; r < NR; r++)
        acc[j & 7][r] = __builtin_amdgcn_mfma_f32_16x16x32_bf16(rol[j == 15 ? 4 : (j & 3)], res[j >> 3][r], acc[j & 7][r], 0, 0, 0);
#pragma unroll
      for (int r = 0; r < NR; r++) {
        __builtin_amdgcn_sched_group_barrier(0x8, 1, 0);
        if (j == 12) __builtin_amdgcn_sched_group_barrier(0x2, 2, 0);
        if (r < nds) __builtin_amdgcn_sched_group_barrier(0x100, 1, 0);
      }
      SCHED();
    }
  }
}

__device__ __forceinline__ constexpr int wslot(int i) { return (i / 3 == 0 ? 2 : (i / 3 == 1 ? 0 : 1)) * SLOT + (i % 3) * 4096; }
__device__ __forceinline__ constexpr int gstep(int i) { return i < 7 ? 2 * i : 2 * i - 13; }
constexpr int C_TAG = LDS_CTL, C_F0 = LDS_CTL + 16, C_F1 = LDS_CTL + 32, C_F2 = LDS_CTL + 48, C_F3 = LDS_CTL + 64, C_FA = LDS_CTL + 80;
constexpr int C_DUMP = LDS_CTL + 1024;

__global__ __launch_bounds__(512, 2) void k_fused(Params p) {
  extern __shared__ __attribute__((aligned(16))) char smem[];
  LAS char* lds = (LAS char*)smem;
  const int tid = threadIdx.x, lane = tid & 63;
  const int wid = __builtin_amdgcn_readfirstlane(tid >> 6);
  const int xg = blockIdx.x & 7, idx = blockIdx.x >> 3;
  int role, rt, ct = 0, myid;
  const int hh = xg & 1, cj = xg >> 1, odd = cj & 1;
  const int ncol = 4 - odd, col0 = (cj >> 1) * 7 + odd * 4, nssm = 4 + odd * 8;
  if (idx < nssm) {
    role = 1; rt = hh * 4 + (idx & 3); ct = (cj >> 1) * 4 + odd + (idx >> 2); myid = 182 + rt * 8 + ct;
  } else {
    const int e = idx - nssm, nfull = ncol * 6, nmid = hh ? ncol - 2 : 2;
    if (e < nfull) { role = 0; const int c = e / 6; rt = hh * 7 + (e - c * 6); ct = col0 + c; }
    else if (e < nfull + nmid) { role = 0; rt = 6; ct = col0 + hh * 2 + (e - nfull); }
    else if (!odd) { role = 2; rt = cj * 2 + hh * 2 + (e - nfull - nmid); }
    else { role = 3; rt = 0; }
    myid = role == 0 ? rt * 14 + ct : (role == 2 ? 246 + rt : 254 + (cj >> 1));
  }
  const int rb = MAP2 ? (xg >> 1) * 8 + (idx >> 2) : xg * 4 + (idx >> 3), cb = MAP2 ? (xg & 1) * 4 + (idx & 3) : idx & 7;
  const int w4 = wid & 3;
  const rsrc_t hr = mkrsrc(p.ws + H_OFF, (unsigned)H_BYTES), wsr = mkrsrc(p.ws, (unsigned)H_OFF);
  const int n = lane & 15, g = lane >> 4;
  u64 pt0, pm;
  STAMP(0);
  f32x4 acc2[4][4];
  u64 tag;
  unsigned vh[4];
  auto ldh = [&](int k, int m) -> bf16x8 {
    return __builtin_bit_cast(bf16x8, __builtin_amdgcn_raw_buffer_load_b128(hr, vh[m] + (k & 3) * 1024, (k & ~3) * 1024, SC1));
  };
  auto set_vh = [&]() { vh[1] = vh[0] + HM; vh[2] = vh[0] + 2 * HM; vh[3] = vh[0] + 3 * HM; PIN(vh[1]); PIN(vh[2]); PIN(vh[3]); };
  const unsigned gflag = (246 * 4 + rb) * FS;
  auto glu_flags = [&]() -> unsigned {
    const int l14 = lane < 14 ? lane : 0, j = l14 >= 7 ? l14 - 7 : l14, pr = 7 * w4 + j;
    const int mt = rb * 4 + (l14 >= 7 ? 3 : 0), om = mt < 120 ? mt / 5 : (mt < 123 ? 24 : 25);
    return lane == 14 ? gflag : (((om >> 1) * 14 + (pr >> 1)) * 4 + (om & 1) * 2 + (pr & 1)) * FS;
  };
  float gv[2];
  u32x2 xv[2], dv;
  auto ld_final = [&]() {
    int ln = lane;
    PIN(ln);
    const int t = rb * 64 + wid * 8 + (ln >> 4), dcol = cb * 64 + (ln & 15) * 4;
    const rsrc_t xr_ = mkrsrc(p.x, L * D * 2);
#pragma unroll
    for (int u = 0; u < 2; u++) {
      gv[u] = __uint_as_float(__builtin_amdgcn_raw_buffer_load_b32(wsr, t * 4 + u * 16, (unsigned)G_OFF, SC1));
      xv[u] = __builtin_amdgcn_raw_buffer_load_b64(xr_, t * 1024 + dcol * 2, u * 4096, 0);
    }
    dv = *(const u32x2*)(p.dsk + dcol);
  };

  LAS char *sb[2], *sa[2];
  f32x4 sk[2];
  float omg[2];
  unsigned vo;
  auto prep_final = [&]() {
    int ln = lane;
    PIN(ln);
    const int nn = ln & 15, mt = wid >> 1;
    const int ob = mt < 3 ? wslot(0) + mt * 4096 : wslot(3), oa = mt == 0 ? wslot(5) : wslot(6) + (mt - 1) * 4096;
    vo = ((rb * 64 + wid * 8 + (ln >> 4)) * D + cb * 64 + nn * 4) * 4;
#pragma unroll
    for (int u = 0; u < 2; u++) {
      const int sl = ((wid & 1) * 8 + u * 4 + (ln >> 4)) ^ nn;
      sb[u] = lds + ob + nn * 256 + (sl << 4);
      sa[u] = lds + oa + nn * 256 + ((sl ^ (4 * mt)) << 4);
      omg[u] = rbf(1.0f - gv[u]);
#pragma unroll
      for (int e = 0; e < 4; e++) {
        const unsigned xw = xv[u][e >> 1], dw = dv[e >> 1];
        const float xf = __uint_as_float((e & 1) ? (xw & 0xffff0000u) : (xw << 16));
        const float df = __uint_as_float((e & 1) ? (dw & 0xffff0000u) : (dw << 16));
        sk[u][e] = rbf(xf * df);
      }
      PIN(sb[u]); PIN(sa[u]); PIN(omg[u]); PIN(sk[u]);
    }
    PIN(vo);
  };

  if (wid >= 4) {
    const int lw = w4;
    const u32x4 wsd = mkdesc(p.ws, (unsigned)H_OFF);
    const u32x4 wdr = mkdesc(p.wd, F * D * 2), xr = mkdesc(p.x, L * D * 2);
    const unsigned vwd = (lw * 14 * 32 + (lane >> 3)) * 1024 + cb * 128 + (((lane & 7) ^ (((lane >> 4) & 3) << 1)) << 4);
    const int k0 = (2 * cj + 4 * hh) & 7, gam = NOG ? 0 : (rt + ct) & 1;
    u32x4 ctv;
    u64 cfl;
    auto ct_load = [&]() {
      const int b = blockIdx.x;
      ctv = ld16_async(mkdesc((b & 1) ? p.cim : p.cre, D * P * 2),
                       ((b >> 5) * 64 + ((b >> 1) & 3) * 16 + n) * 1024 + ((b >> 3) & 3) * 256 + lw * 64 + g * 16, 0);
    };
    auto ct_store = [&]() { st16_async(wsd, (unsigned)CT_OFF + (blockIdx.x * 4 + lw) * 1024 + lane * 16, ctv); };
    auto ct_flag = [&]() { st_flag(wsr, lane == 0 ? (blockIdx.x * 4 + lw) * FS : OOR, FC_OFF, tag); };
    const unsigned cfo = FC_OFF + ((cb * 4 + lw) * 32 + (lane & 31)) * FS;
    auto ct_ask = [&]() { cfl = ld8_async(wsd, cfo); };
    auto ct_check = [&]() { PIN(cfl); if (!__all(cfl == tag)) poll_flags(wsr, cfo, 0, tag); };
    u32x4 cf[32];
    const unsigned vcf = (cb * 4 + lw) * 32768 + lane * 16;
    auto ldc = [&](int a, int b) {
#pragma unroll
      for (int f = a; f < b; f++) cf[f] = ld16_async(wsd, vcf, (unsigned)CT_OFF + f * 1024);
    };
    auto wpiece = [&](int i, int k) { dma16(wdr, wslot(i < 9 ? i : i - 9) + lw * 12288 + k * 1024, vwd, (gstep(i) * 32 + k * 8) * 1024); };
    auto wround = [&](int r) {
#pragma unroll
      for (int q = 0; q < 12; q++) wpiece(r * 3 + (q >> 2), q & 3);
    };
    const u32x4 gr = mkdesc(p.wg, D * F * 2), ur = mkdesc(p.wu, D * F * 2);
    auto pre_1 = [&]() {
      const unsigned v = (((k0 + 2) & 7) * 64 + idx * 4 + lw) * (F * 2) + col0 * 256 + (lane < ncol * 16 ? lane : 0) * 16;
      dma16(gr, C_DUMP, v, 0);
      dma16(ur, C_DUMP, v, 0);
    };
    auto pre_w = [&]() { touch(wdr, C_DUMP, (lane < 14 ? lane : 0) * 128, (blockIdx.x * 4 + lw) * 1792); };
    auto loader = [&](auto piece, auto np_, auto na_, auto nb_) {
      constexpr int NP = decltype(np_)::value, NA = decltype(na_)::value, NB = decltype(nb_)::value;
      auto pieces = [&](int s, int a, int b) {
        const int sg = ((s ^ gam) + k0) & 7;
#pragma unroll
        for (int k = a; k < b; k++) piece(s, sg, k);
      };
      LST(27); pieces(0, 0, 1); LST(28); pieces(0, 1, NP); LST(29); ct_load(); pre_1(); if (!PWL) pre_w(); pieces(1, 0, S1A); LST(0); WAITV(4 - PWL + S1A); LST(1); BARRIER(); LST(2);
      tag = *(LAS u64*)(lds + C_TAG);
      pieces(1, S1A, NP); pieces(2, 0, S2A); LST(3); WAITV(S2A); LST(4); BARRIER(); LST(5);
      ct_store(); pieces(2, S2A, NP); pieces(3, 0, S3A); LST(6); WAITV(S3A); ct_flag(); LST(7); BARRIER(); LST(8);
      pieces(3, S3A, NP);
      pieces(4, 0, NP); LST(9); WAITV(NP); if (PWL) pre_w(); LST(10); BARRIER(); LST(11);
      if (NA) ct_ask();
      pieces(5, 0, NP); LST(12); WAITV(NP);
      if (NA) ct_check();
      ldc(0, NA); LST(13); BARRIER(); LST(14);
      pieces(6, 0, NP); ldc(NA, 2 * NA); LST(15); WAITV(NP + 2 * NA); LST(16); BARRIER(); LST(17);
      if (!NA) ct_ask();
      pieces(7, 0, NP); ldc(2 * NA, 3 * NA); LST(18); WAITV(NP + 2 * NA); LST(19); BARRIER(); LST(20);
      if (!NA) ct_check();
      wround(0); ldc(3 * NA, 3 * NA + NB); LST(21); WAITV(NA + 12 + NB); LST(22); BARRIER(); LST(23);
      wround(1); ldc(3 * NA + NB, 3 * NA + 2 * NB); LST(25); BARRIER(); LST(26);
      WAITV(NB);
      lds_set(lds + C_F0 + lw * 4);
    };
    if (role == 0) {
      const int r0 = min(rt * 160, L - 160);
      const int xrow = 8 * lw + (lane >> 3);
      const unsigned vx = (r0 + xrow) * 1024 + (((lane & 7) ^ ((xrow >> 1) & 7)) << 4);
      const int krow = 4 * lw + (lane >> 4);
      const int swz = (krow & 3) | (((krow >> 3) & 1) << 2);
      const unsigned vw = krow * (F * 2) + ct * 256 + (((lane & 15) ^ (swz << 1)) << 4);
      loader([&](int s, int sg, int k) {
        const unsigned b = (s % 3) * SLOT + lw * 1024;
        if (k < 5) dma16(xr, b + k * 4096, vx, sg * 128 + k * 32768);
        else { const int i = (k - 5) >> 1; dma16(((k - 5) & 1) ? ur : gr, b + 20480 + ((k - 5) & 1) * 16384 + i * 4096, vw, (sg * 64 + i * 16) * (F * 2)); }
      }, std::integral_constant<int, 13>{}, std::integral_constant<int, GNA>{}, std::integral_constant<int, GNB>{});
      STAMP(1);
      ldc(3 * GNA + 2 * GNB, 32);
      wround(2);
      WAITV(12);
    } else {
      if (role == 1) {
        const int r0 = rt * 256;
        const u32x4 br = mkdesc(p.bre, P * D * 2), bi = mkdesc(p.bim, P * D * 2);
        const int ii = 8 * (lw & 1) + (lane >> 3);
        const int csw = ((lane & 7) ^ ((ii >> 1) & 7)) << 4;
        const unsigned vx = (r0 + 32 * (ii >> 2) + 16 * (ii & 1) + ((ii >> 1) & 1) + 2 * (lw >> 1)) * 1024 + csw;
        const unsigned vb = (ct * 64 + 8 * lw + (lane >> 3)) * 1024 + csw;
        loader([&](int s, int sg, int k) {
          const unsigned b = (s % 3) * SLOT + lw * 1024;
          if (k < 4) dma16((k & 1) ? bi : br, b + 32768 + (k & 1) * 8192 + (k >> 1) * 4096, vb, sg * 128 + (k >> 1) * 32768);
          else { const int i = k - 4; dma16(xr, b + i * 4096, vx, sg * 128 + (128 * (i >> 2) + 4 * (i & 3)) * 1024); }
        }, std::integral_constant<int, 12>{}, std::integral_constant<int, 0>{}, std::integral_constant<int, SNB>{});
        wround(2);
        WAITV(12);
      } else {
        ct_load();
        BARRIER();
        tag = *(LAS u64*)(lds + C_TAG);
        WAITV(0); ct_store(); WAITV(0); ct_flag();
        pre_1(); pre_w();
        wround(0); wround(1); wround(2);
        WAITV(12);
        lds_set(lds + C_F0 + lw * 4);
        poll_flags(wsr, cfo, 0, tag);
        ldc(0, 2 * SNB);
        WAITV(0);
        if (myid == 255 && lw == 3) {
#pragma unroll 1
          for (int k = 0; k < 4; k++) poll_flags(wsr, (k * 64 + lane) * 4 * FS, FC_OFF, tag);
          st_flag(wsr, lane == 0 ? (unsigned)EPOCH_OFF : OOR, 0, tag);
        }
      }
      ldc(2 * SNB, 32);
      WAITV(0);
    }
    const unsigned fs = lane < 16 ? FA_OFF + glu_flags()
                                  : FB_OFF + ((182 + (rb >> 2) * 8 + lw * 2 + ((lane >> 1) & 1)) * 4 + (rb & 2) + (lane & 1)) * FS;
    vh[0] = rb * 4 * HM + (56 + lw * 8) * 1024 + lane * 16;
    set_vh();
    LAS char* pb[4];
#pragma unroll
    for (int k = 0; k < 4; k++) {
      const int mt = n >> 2;
      pb[k] = lds + (mt == 0 ? wslot(5) : wslot(6) + (mt - 1) * 4096) + lw * 12288 + g * 256 + ((((n & 3) ^ g) | ((k ^ mt) << 2)) << 4);
      PIN(pb[k]);
    }
    STAMP(2);
    lds_wait(lds + C_F1 + lw * 4);
    STAMP(3);
    {
      u64 miss = ~0ull;
      bool sent = false;
      auto look = [&]() {
        cfl = ld8_async(wsd, fs);
        WAITV(0);
        PIN(cfl);
        miss = __ballot(cfl != tag);
        if (!sent && (miss & 0xffffull) == 0) { sent = true; lds_set(lds + C_FA + lw * 4); }
      };
#pragma unroll
      for (int i = 9; i < 14; i++) {
#pragma unroll
        for (int k = 0; k < 4; k++) wpiece(i, k);
        if (!sent || i == 13) look();
        if (i == 9) lds_set(lds + C_F2 + lw * 4);
      }
      lds_set(lds + C_F2 + lw * 4, 2u);
      STAMP(6);
#pragma unroll 1
      for (unsigned spins = 0; miss != 0 && spins < SPIN_CAP; spins++) {
        __builtin_amdgcn_s_sleep(PSL);
        look();
      }
      if (!sent) lds_set(lds + C_FA + lw * 4);
      asm volatile("" ::: "memory");
    }
    STAMP(7);
    bf16x8 hf[3][4];
#pragma unroll
    for (int k = 0; k < 3; k++)
#pragma unroll
      for (int m = 0; m < 4; m++) hf[k][m] = ldh(k, m);
#pragma unroll
    for (int f = 0; f < 32; f++) PIN(cf[f]);
#pragma unroll
    for (int c = 0; c < 4; c++)
#pragma unroll
      for (int m = 0; m < 4; m++) acc2[c][m] = (f32x4){0.f, 0.f, 0.f, 0.f};
    SCHED();
#pragma unroll
    for (int k = 0; k < 8; k++) {
      if (k == 3) ld_final();
      if (k == 6) prep_final();
      if (k == 7) lds_wait(lds + C_F3 + lw * 4);
#pragma unroll
      for (int m = 0; m < 4; m++) {
#pragma unroll
        for (int c = 0; c < 4; c++)
          acc2[c][m] = __builtin_amdgcn_mfma_f32_16x16x32_bf16(__builtin_bit_cast(bf16x8, cf[c * 8 + (k & 1) * 4 + (k >> 1)]), hf[k % 3][m], acc2[c][m], 0, 0, 0);
        if (k + 3 < 8) hf[k % 3][m] = ldh(k + 3, m);
        if (k == 7 && m > 0) {
#pragma unroll
          for (int c = 0; c < 4; c++) *(LAS f32x4*)(pb[(m - 1) ^ c] + c * 1024) = acc2[c][m - 1];
        }
        SCHED();
      }
    }
    STAMP(8);
#pragma unroll
    for (int c = 0; c < 4; c++) *(LAS f32x4*)(pb[3 ^ c] + c * 1024) = acc2[c][3];
  } else {
    const int wr = wid >> 1, wc = wid & 1;
    const int chg = (ct * 64 + wc * 32 + (lane & 31)) & (P - 1);
    const u16 c_ldt = p.ldt[chg], c_lre = p.lre[chg], c_lim = p.lim[chg];
    if (wid >= 2 && idx * 2 + (wid & 1) < CODE_KB) {
      const u32x4 cd = mkdesc((const void*)(__builtin_amdgcn_s_getpc() & ~255ull), CODE_KB * 1024);
      dma16(cd, C_DUMP, lane * 16, (idx * 2 + (wid & 1)) * 1024);
    }
    if (wid == 0) {
      u64 w0;
      asm volatile("s_load_dwordx2 %0, %1, 0x0\n\ts_waitcnt lgkmcnt(0)" : "=s"(w0) : "s"(p.ws + EPOCH_OFF) : "memory");
      *(LAS u64*)(lds + C_TAG + lane * 8) = lane == 0 ? w0 + TAG_STEP : 0ull;
    }
    unsigned vfo = lane == 0 ? (myid * 4 + wid) * FS : OOR;
    unsigned vfi = glu_flags();
    vh[0] = rb * 4 * HM + wid * 14 * 1024 + lane * 16;
    LAS char* ra0 = lds + wid * 12288 + (4 * g + (n >> 2)) * 128 + (((2 * g + (n >> 3)) & 3) << 5) + (n & 3) * 8;
    PIN(vfo); PIN(vfi); PIN(vh[0]); PIN(ra0);
    bf16x8 wf[6][4];
    LAS char* ra[3][4];
    auto ldw = [&](int i, int c) -> bf16x8 {
      const int k = i < 9 ? i : i - 9;
      LAS char* a = ra[k / 3][c] + (k % 3) * 4096;
      return ldtr(a, a + 2048);
    };
    auto grab_w = [&]() {
#pragma unroll
      for (int c = 0; c < 4; c++) {
        ra[1][c] = (LAS char*)((unsigned)(size_t)ra0 ^ (c << 5));
        ra[0][c] = ra[1][c] + 2 * SLOT;
        ra[2][c] = ra[1][c] + SLOT;
      }
      lds_wait(lds + C_F0 + wid * 4);
#pragma unroll
      for (int i = 0; i < 5; i++)
#pragma unroll
        for (int c = 0; c < 4; c++) wf[i][c] = ldw(i, c);
      asm volatile("" ::: "memory");
      lds_set(lds + C_F1 + wid * 4);
    };
    if (role == 0) {
      const int mt0 = (min(rt * 160, L - 160) >> 4) + wr * 5;
      unsigned vs[5];
#pragma unroll
      for (int m = 0; m < 5; m++) { vs[m] = mt0 + m < rt * 10 ? OOR : (mt0 + m) * HM + (ct * 4 + wc * 2) * 1024 + lane * 16; PIN(vs[m]); }
      f32x4 acc[8][5];
      STAMP(1);
      main_loop<true, 5>(lds, wr, wc, lane, acc);
      STAMP(2);
      tag = *(LAS u64*)(lds + C_TAG);
#pragma unroll
      for (int h = 0; h < 2; h++) {
        if (h == 1) { SCHED(); STAMP(13); grab_w(); STAMP(14); SCHED(); }
#pragma unroll
        for (int m = 0; m < 5; m++) {
          if (h == 1 && m == PUBA) { WAITV(PUBA); st_flag(wsr, vfo, FA_OFF, tag); STAMP(3); }
          u32x4 o;
#pragma unroll
          for (int e = 0; e < 4; e++) {
            const int c = 2 * h + (e >> 1);
            const f32x2 gv_ = (e & 1) ? hi2(acc[c][m]) : lo2(acc[c][m]);
            const f32x2 uv = (e & 1) ? hi2(acc[4 + c][m]) : lo2(acc[4 + c][m]);
            f32x2 t = gv_ * -1.4426950408889634f;
            t = (f32x2){__builtin_amdgcn_exp2f(t.x), __builtin_amdgcn_exp2f(t.y)} + 1.0f;
            t = (f32x2){__builtin_amdgcn_rcpf(t.x), __builtin_amdgcn_rcpf(t.y)} * uv;
            o[e] = pack2(t.x, t.y);
          }
          __builtin_amdgcn_raw_buffer_store_b128(o, hr, vs[m] + h * 1024, 0, SC1);
        }
      }
    } else {
      if (role == 1) {
        float lr[2], li[2], cr[2], ci[2], sr[2], si[2], mr[2], mi[2], wr_[2], wi_[2], qr, qi, zr, zi;
        {
          float a2, a3;
          ssm_consts(c_ldt, c_lre, c_lim, qr, qi, a2, a3);
          share2(qr, lr); share2(qi, li); share2(2.f * a2, cr); share2(2.f * a3, ci);
          csq(qr, qi); csq(qr, qi); csq(qr, qi); csq(qr, qi);
          share2(qr, sr); share2(qi, si);
          csq(qr, qi);
          share2(qr, mr); share2(qi, mi);
          csq(qr, qi); csq(qr, qi);
          zr = qr; zi = qi;
          csq(zr, zi); csq(zr, zi); csq(zr, zi);
          if (rt * 2 + wr <= 8) { zr = 0.f; zi = 0.f; }
#pragma unroll
          for (int u = 0; u < 2; u++) {
            float ar = mr[u], ai = mi[u];
            csq(ar, ai);
            const float br_ = (g & 1) ? mr[u] : 1.f, bi_ = (g & 1) ? mi[u] : 0.f;
            wr_[u] = (g & 2) ? ar * br_ - ai * bi_ : br_;
            wi_[u] = (g & 2) ? ar * bi_ + ai * br_ : bi_;
            PIN(mr[u]); PIN(mi[u]); PIN(lr[u]); PIN(li[u]); PIN(cr[u]); PIN(ci[u]); PIN(sr[u]); PIN(si[u]); PIN(wr_[u]); PIN(wi_[u]);
          }
          PIN(qr); PIN(qi); PIN(zr); PIN(zi);
        }
        const int ck = rt * 2 + wr;
        unsigned veo = lane < 32 ? (unsigned)E_OFF + (ck * 512 + chg) * 16 : OOR;
        const int c0 = ck - 1 - 8 * (lane >> 5);
        int vei = (c0 * 512 + chg) * 16, nval = min(max(c0 + 1, 0), 8);
        const unsigned tb = (wid >> 1) * 2 * SLOT + 49152 + (wid & 1) * 2048;
        LAS char* twp = lds + tb + n * 64 + ((g ^ (((n >> 1) & 1) | ((((n >> 2) ^ (n >> 3)) & 1) << 1))) << 4);
        LAS char* trp[2];
#pragma unroll
        for (int t = 0; t < 2; t++)
          trp[t] = lds + tb + (8 * g + (n >> 2)) * 64 + (((((n >> 1) ^ (n >> 3)) & 1) | (((g ^ t) & 1) << 1)) << 4) + (n & 1) * 8;
        unsigned vso[8];
#pragma unroll
        for (int k = 0; k < 8; k++) { vso[k] = (rt * 16 + wr * 8 + k) * HM + (56 + (ct * 2 + wc) * 2) * 1024 + lane * 16; PIN(vso[k]); }
        PIN(veo); PIN(vei); PIN(nval); PIN(twp); PIN(trp[0]); PIN(trp[1]);
        f32x4 acc[8][4];
        STAMP(1);
        main_loop<false, 4>(lds, wr, wc, lane, acc);
        STAMP(2);
        tag = *(LAS u64*)(lds + C_TAG);
        f32x2 er[2], ei[2];
#pragma unroll
        for (int u = 0; u < 2; u++) { er[u] = lo2(acc[0][u]); ei[u] = lo2(acc[0][2 + u]); }
#pragma unroll
        for (int st = 1; st < 16; st++)
#pragma unroll
          for (int u = 0; u < 2; u++) {
            const f32x2 l2 = {lr[u], lr[u]}, i2 = {li[u], li[u]};
            const f32x2 vr = (st & 1) ? hi2(acc[st >> 1][u]) : lo2(acc[st >> 1][u]);
            const f32x2 vi = (st & 1) ? hi2(acc[st >> 1][2 + u]) : lo2(acc[st >> 1][2 + u]);
            const f32x2 t = fma2(-i2, ei[u], fma2(l2, er[u], vr));
            ei[u] = fma2(i2, er[u], fma2(l2, ei[u], vi));
            er[u] = t;
          }
        float xs[2][4], ys[2][4];
#pragma unroll
        for (int u = 0; u < 2; u++) {
          const float a = fmaf(-si[u], ei[u].x, fmaf(sr[u], er[u].x, er[u].y)), b = fmaf(si[u], er[u].x, fmaf(sr[u], ei[u].x, ei[u].y));
          float a2[2], b2[2];
          share2(a, a2); share2(b, b2);
#pragma unroll
          for (int k = 0; k < 2; k++) {
            const auto ta = __builtin_amdgcn_permlane32_swap(__float_as_uint(a2[k]), __float_as_uint(a2[k]), false, false);
            const auto tb_ = __builtin_amdgcn_permlane32_swap(__float_as_uint(b2[k]), __float_as_uint(b2[k]), false, false);
            xs[u][k] = __uint_as_float(ta[0]); xs[u][2 + k] = __uint_as_float(ta[1]);
            ys[u][k] = __uint_as_float(tb_[0]); ys[u][2 + k] = __uint_as_float(tb_[1]);
          }
#pragma unroll
          for (int k = 1; k < 4; k++) {
            xs[u][k] = fmaf(-mi[u], ys[u][k - 1], fmaf(mr[u], xs[u][k - 1], xs[u][k]));
            ys[u][k] = fmaf(mi[u], xs[u][k - 1], fmaf(mr[u], ys[u][k - 1], ys[u][k]));
            PIN(xs[u][k]); PIN(ys[u][k]);
          }
        }
        const unsigned tg = (unsigned)tag;
        __builtin_amdgcn_raw_buffer_store_b128((u32x4){__float_as_uint(g ? xs[1][3] : xs[0][3]), tg, __float_as_uint(g ? ys[1][3] : ys[0][3]), tg},
                                               wsr, veo, 0, SC1);
        STAMP(3);
        SCHED();
        float pr[2], pi[2];
#pragma unroll
        for (int u = 0; u < 2; u++) {
          pr[u] = g == 0 ? 0.f : (g == 1 ? xs[u][0] : (g == 2 ? xs[u][1] : xs[u][2]));
          pi[u] = g == 0 ? 0.f : (g == 1 ? ys[u][0] : (g == 2 ? ys[u][1] : ys[u][2]));
          PIN(pr[u]); PIN(pi[u]);
        }
        const rsrc_t erc = mkrsrc(p.ws + E_OFF, (unsigned)E_BYTES);
        u32x4 v[8];
        auto ask = [&]() {
#pragma unroll
          for (int d = 0; d < 8; d++) v[d] = __builtin_amdgcn_raw_buffer_load_b128(erc, vei - d * 8192, 0, SC1);
        };
#pragma unroll
        for (int u = 0; u < 2; u++) {
          if (u == 1) { SCHED(); ask(); SCHED(); }
          PIN(cr[u]);
#pragma unroll
          for (int m = 0; m < 8; m++) {
            const f32x4 a = acc[m][u], b = acc[m][2 + u];
            acc[m][u] = cr[u] * a - ci[u] * b;
            acc[m][2 + u] = cr[u] * b + ci[u] * a;
            PIN(acc[m][u]); PIN(acc[m][2 + u]);
          }
        }
#pragma unroll 1
        for (unsigned spins = 0; spins < SPIN_CAP; spins++) {
          int cnt = 0;
#pragma unroll
          for (int d = 0; d < 8; d++) cnt += ((v[d][1] ^ tg) | (v[d][3] ^ tg)) == 0u;
          if (__all(cnt >= nval)) break;
          asm volatile("" ::: "memory");
          __builtin_amdgcn_s_sleep(ESL);
          ask();
        }
        float kr[2], ki[2];
        {
          float xr_ = __uint_as_float(v[7][0]), xi_ = __uint_as_float(v[7][2]);
#pragma unroll
          for (int d = 6; d >= 0; d--) {
            const float t = fmaf(-qi, xi_, fmaf(qr, xr_, __uint_as_float(v[d][0])));
            xi_ = fmaf(qi, xr_, fmaf(qr, xi_, __uint_as_float(v[d][2])));
            xr_ = t;
            PIN(xr_); PIN(xi_);
          }
          const auto ta = __builtin_amdgcn_permlane32_swap(__float_as_uint(xr_), __float_as_uint(xr_), false, false);
          const auto tb_ = __builtin_amdgcn_permlane32_swap(__float_as_uint(xi_), __float_as_uint(xi_), false, false);
          const float fr = __uint_as_float(ta[1]), fi = __uint_as_float(tb_[1]);
          share2(fmaf(-zi, fi, fmaf(zr, fr, __uint_as_float(ta[0]))), kr);
          share2(fmaf(zi, fr, fmaf(zr, fi, __uint_as_float(tb_[0]))), ki);
        }
        STAMP(4);
        f32x2 hr_[2], hi_[2];
#pragma unroll
        for (int u = 0; u < 2; u++) {
          f32x2 a, b;
          a.x = fmaf(-wi_[u], ki[u], fmaf(wr_[u], kr[u], pr[u])); b.x = fmaf(wi_[u], kr[u], fmaf(wr_[u], ki[u], pi[u]));
          a.y = fmaf(-si[u], b.x, fmaf(sr[u], a.x, er[u].x)); b.y = fmaf(si[u], a.x, fmaf(sr[u], b.x, ei[u].x));
          PIN(a); PIN(b);
          hr_[u] = cr[u] * a - ci[u] * b; hi_[u] = cr[u] * b + ci[u] * a;
        }
        bf16x8 tv[4];
#pragma unroll
        for (int qt = 0; qt < 5; qt++) {
          u32x4 o[2][2];
          if (qt < 4) {
#pragma unroll
            for (int mm = 0; mm < 2; mm++)
#pragma unroll
              for (int u = 0; u < 2; u++) {
                const int m = qt * 2 + mm;
                const f32x2 l2 = {lr[u], lr[u]}, i2 = {li[u], li[u]};
                const f32x2 r0_ = fma2(-i2, hi_[u], fma2(l2, hr_[u], lo2(acc[m][u])));
                const f32x2 i0_ = fma2(i2, hr_[u], fma2(l2, hi_[u], lo2(acc[m][2 + u])));
                hr_[u] = fma2(-i2, i0_, fma2(l2, r0_, hi2(acc[m][u])));
                hi_[u] = fma2(i2, r0_, fma2(l2, i0_, hi2(acc[m][2 + u])));
                o[0][u][mm] = pack2(r0_.x, hr_[u].x); o[0][u][2 + mm] = pack2(r0_.y, hr_[u].y);
                o[1][u][mm] = pack2(-i0_.x, -hi_[u].x); o[1][u][2 + mm] = pack2(-i0_.y, -hi_[u].y);
              }
          }
          SCHED();
          if (qt > 0) {
#pragma unroll
            for (int k = 0; k < 4; k++) __builtin_amdgcn_raw_buffer_store_b128(__builtin_bit_cast(u32x4, tv[k]), hr, vso[(k & 1) * 4 + qt - 1] + (k >> 1) * 1024, 0, SC1);
          }
          if (qt < 4) {
#pragma unroll
            for (int part = 0; part < 2; part++) {
              asm volatile("" ::: "memory");
              *(LAS u32x4*)twp = o[part][0];
              *(LAS u32x4*)(twp + 1024) = o[part][1];
              asm volatile("" ::: "memory");
#pragma unroll
              for (int rho = 0; rho < 2; rho++) tv[part * 2 + rho] = ldtr(trp[rho], trp[rho ^ 1] + 256);
            }
          }
          SCHED();
        }
        STAMP(13);
      } else {
        BARRIER();
        tag = *(LAS u64*)(lds + C_TAG);
        if (role == 2) {
          const u32x4 gwv = *(const u32x4*)(p.gw + lane * 8);
          f32x2 gwf[4];
#pragma unroll
          for (int e = 0; e < 4; e++) gwf[e] = (f32x2){__uint_as_float(gwv[e] << 16), __uint_as_float(gwv[e] & 0xffff0000u)};
          const bf16x2 ones = {(__bf16)1.0f, (__bf16)1.0f};
#pragma unroll 1
          for (int it = 0; it < 4; it++) {
            const int r0 = rt * 256 + wid * 64 + it * 16;
            u32x4 xw[16];
#pragma unroll
            for (int r = 0; r < 16; r++) xw[r] = *(const u32x4*)(p.x + (size_t)(r0 + r) * D + lane * 8);
            float s[16];
#pragma unroll
            for (int r = 0; r < 16; r++) {
              s[r] = 0.f;
#pragma unroll
              for (int e = 0; e < 4; e++) {
                const unsigned w = xw[r][e];
                const f32x2 pr = (f32x2){__uint_as_float(w << 16), __uint_as_float(w & 0xffff0000u)} * gwf[e];
                const bf16x2 pb = {(__bf16)pr.x, (__bf16)pr.y};
                s[r] = __builtin_amdgcn_fdot2_f32_bf16(pb, ones, s[r], false);
              }
            }
#pragma unroll
            for (int r = 0; r < 8; r++) {
              const auto t = __builtin_amdgcn_permlane32_swap(__float_as_uint(s[r]), __float_as_uint(s[r + 8]), false, false);
              s[r] = __uint_as_float(t[0]) + __uint_as_float(t[1]);
            }
#pragma unroll
            for (int r = 0; r < 4; r++) {
              const auto t = __builtin_amdgcn_permlane16_swap(__float_as_uint(s[r]), __float_as_uint(s[r + 4]), false, false);
              s[r] = __uint_as_float(t[0]) + __uint_as_float(t[1]);
            }
#pragma unroll
            for (int r = 0; r < 4; r++) {
              s[r] += __uint_as_float(__builtin_amdgcn_update_dpp(0, __float_as_uint(s[r]), 0x128, 0xf, 0xf, false));
              s[r] += __uint_as_float(__builtin_amdgcn_update_dpp(0, __float_as_uint(s[r]), 0x124, 0xf, 0xf, false));
              s[r] += __uint_as_float(__builtin_amdgcn_update_dpp(0, __float_as_uint(s[r]), 0x122, 0xf, 0xf, false));
              s[r] += __uint_as_float(__builtin_amdgcn_update_dpp(0, __float_as_uint(s[r]), 0x121, 0xf, 0xf, false));
            }
            const float sv = (n & 2) ? ((n & 1) ? s[3] : s[2]) : ((n & 1) ? s[1] : s[0]);
            __builtin_amdgcn_raw_buffer_store_b32(__float_as_uint(rbf(sigmoidf_(rbf(sv)))), wsr,
                                                  n < 4 ? (unsigned)G_OFF + (r0 + n + 4 * (g & 1) + 8 * (g >> 1)) * 4 : OOR, 0, SC1);
          }
          WAITV(0);
          st_flag(wsr, vfo, FA_OFF, tag);
        }
      }
      grab_w();
    }
    STAMP(5);
    set_vh();
    const bool early = __builtin_amdgcn_readfirstlane(__hip_atomic_load((LAS unsigned*)(lds + C_FA + wid * 4), __ATOMIC_RELAXED, __HIP_MEMORY_SCOPE_WORKGROUP));
    if (!early) {
      WAITV(0);
      st_flag(wsr, vfo, FB_OFF, tag);
      lds_wait(lds + C_FA + wid * 4);
    }
    asm volatile("" ::: "memory");
    STAMP(7);
    bf16x8 hf[4][4];
#pragma unroll
    for (int k = 0; k < 4; k++)
#pragma unroll
      for (int m = 0; m < 4; m++) hf[k][m] = ldh(gstep(k), m);
    if (early) { WAITV(16); st_flag(wsr, vfo, FB_OFF, tag); }
    STAMP(6);
#pragma unroll
    for (int c = 0; c < 4; c++)
#pragma unroll
      for (int m = 0; m < 4; m++) acc2[c][m] = (f32x4){0.f, 0.f, 0.f, 0.f};
    LAS char* pw[4];
#pragma unroll
    for (int c = 0; c < 4; c++) { pw[c] = lds + wid * 12288 + c * 1024 + g * 256 + ((n ^ (4 * c + g)) << 4); PIN(pw[c]); }
    SCHED();
    u64 fbv = 0;
#pragma unroll
    for (int k = 0; k < 14; k++) {
      if (k == 2) fbv = ld_flag(wsr, vfi, FB_OFF);
      if (k == 3) { if (!__all(fbv == tag)) poll_flags(wsr, vfi, FB_OFF, tag); asm volatile("" ::: "memory"); STAMP(12); }
      if (k == 5) lds_wait(lds + C_F2 + wid * 4);
      if (k == 8) lds_wait(lds + C_F2 + wid * 4, 2u);
      if (k == 9) lds_set(lds + C_F3 + wid * 4);
      if (k == 7) ld_final();
      if (k == 11) prep_final();
#pragma unroll
      for (int m = 0; m < 4; m++) {
        if (k + 1 >= 5 && k + 1 < 14) wf[(k + 1) % 6][m] = ldw(k + 1, m);
#pragma unroll
        for (int c = 0; c < 4; c++) acc2[c][m] = __builtin_amdgcn_mfma_f32_16x16x32_bf16(wf[k % 6][c], hf[k & 3][m], acc2[c][m], 0, 0, 0);
        if (k + 4 < 14) hf[k & 3][m] = ldh(gstep(k + 4), m);
        if (k == 13 && m > 0) {
#pragma unroll
          for (int c = 0; c < 4; c++) *(LAS f32x4*)(pw[c] + wslot(m - 1)) = acc2[c][m - 1];
        }
        SCHED();
      }
    }
    STAMP(8);
#pragma unroll
    for (int c = 0; c < 4; c++) *(LAS f32x4*)(pw[c] + wslot(3)) = acc2[c][3];
  }
  BARRIER();
  STAMP(9);
  {
    const rsrc_t orc = mkrsrc(p.out, L * D * 4);
#pragma unroll
    for (int u = 0; u < 2; u++) {
      f32x4 b = *(LAS f32x4*)sb[u], a = sk[u] + *(LAS f32x4*)sa[u];
#pragma unroll
      for (int w = 1; w < 4; w++) {
        b += *(LAS f32x4*)(sb[u] + w * 12288);
        a += *(LAS f32x4*)(sa[u] + w * 12288);
      }
      const f32x4 o = gv[u] * a + omg[u] * b;
      __builtin_amdgcn_raw_buffer_store_b128(__builtin_bit_cast(u32x4, o), orc, vo + u * 8192, 0, OAUX);
    }
  }
  STAMP(10);
  while (__builtin_amdgcn_s_memrealtime() < pm + 5000) __builtin_amdgcn_s_sleep(1);
}

extern "C" void kernel_launch(void* const* d_in, const int* in_sizes, int n_in, void* d_out, int out_size, void* d_ws,
                              size_t ws_size, hipStream_t stream) {
  Params p;
  p.x = (const u16*)d_in[0]; p.lre = (const u16*)d_in[1]; p.lim = (const u16*)d_in[2];
  p.bre = (const u16*)d_in[3]; p.bim = (const u16*)d_in[4]; p.cre = (const u16*)d_in[5]; p.cim = (const u16*)d_in[6];
  p.ldt = (const u16*)d_in[7]; p.dsk = (const u16*)d_in[8];
  p.wg = (const u16*)d_in[10]; p.wu = (const u16*)d_in[11]; p.wd = (const u16*)d_in[12]; p.gw = (const u16*)d_in[13];
  p.ws = (char*)d_ws; p.out = (float*)d_out;
  (void)hipFuncSetAttribute((const void*)k_fused, hipFuncAttributeMaxDynamicSharedMemorySize, LDS_BYTES);
  k_fused<<<256, 512, LDS_BYTES, stream>>>(p);
}
```

```cpp
#include <hip/hip_runtime.h>
#include <stdint.h>

typedef unsigned short u16;
typedef unsigned long long u64;
typedef __bf16 bf16x8 __attribute__((ext_vector_type(8)));
typedef __bf16 bf16x4 __attribute__((ext_vector_type(4)));
typedef __bf16 bf16x2 __attribute__((ext_vector_type(2)));
typedef float f32x4 __attribute__((ext_vector_type(4)));
typedef float f32x2 __attribute__((ext_vector_type(2)));
typedef unsigned u32x4 __attribute__((ext_vector_type(4)));
typedef unsigned u32x2 __attribute__((ext_vector_type(2)));
#define LAS __attribute__((address_space(3)))
#define GAS __attribute__((address_space(1)))
#define RLX_AGENT __ATOMIC_RELAXED, __HIP_MEMORY_SCOPE_AGENT
typedef GAS unsigned gu32;
typedef GAS u64 gu64;
typedef __amdgpu_buffer_rsrc_t rsrc_t;

constexpr int L = 2048, D = 512, P = 512, F = 1792;
constexpr int KS2 = 88;
constexpr int SLOT = 53248;
constexpr int LDS_CTL = 3 * SLOT;
constexpr int LDS_BYTES = 163840;
constexpr int SC1 = 16;
constexpr size_t EPOCH_OFF = 0;
constexpr size_t G_OFF = 65536;
constexpr size_t E_OFF = 131072, E_BYTES = 16 * 512 * 16;
#define FS 128
constexpr unsigned FA_OFF = 262144, FB_OFF = FA_OFF + 1024 * FS, FC_OFF = FB_OFF + 1024 * FS;
constexpr size_t CT_OFF = 1u << 20, CT_BYTES = 1u << 20;
constexpr int HM = KS2 * 1024;
constexpr size_t H_OFF = CT_OFF + CT_BYTES, H_BYTES = (size_t)128 * HM;
constexpr u64 TAG_STEP = 0x9E3779B97F4A7C15ull;
#define OAUX 18
#define PSL 2
#define MAP2 1
#define ESL 4
#define DMAUX ""
#define PWL 1
#define NOG 0
#define GNA 3
#define GNB 4
#define SNB 4
#define PUBA 3
#define S1A 2
#define S2A 6
#define S3A 10
#define CHKB 3
constexpr int CODE_KB = 40;
constexpr unsigned OOR = 0x40000000u;
constexpr unsigned SPIN_CAP = 1u << 18;

#define BARRIER() asm volatile("s_waitcnt lgkmcnt(0)\n\ts_barrier" ::: "memory")
#define WAITV(n) asm volatile("s_waitcnt vmcnt(%0)" ::"i"(n) : "memory")
#define WAITL0() asm volatile("s_waitcnt lgkmcnt(0)" ::: "memory")
#define SCHED() __builtin_amdgcn_sched_barrier(0)
#define PIN(x) asm volatile("" : "+v"(x))

#define STAMP(id) do {} while (0)
#define LST(id) do {} while (0)

__device__ __forceinline__ float bf2f(u16 v) { return __uint_as_float(((unsigned)v) << 16); }
__device__ __forceinline__ float rbf(float f) { return (float)(__bf16)f; }
__device__ __forceinline__ unsigned pack2(float a, float b) {
  bf16x2 v = {(__bf16)a, (__bf16)b};
  return __builtin_bit_cast(unsigned, v);
}
__device__ __forceinline__ rsrc_t mkrsrc(const void* p, unsigned bytes) {
  return __builtin_amdgcn_make_buffer_rsrc((void*)p, 0, bytes, 0x00020000);
}
__device__ __forceinline__ u32x4 mkdesc(const void* p, unsigned bytes) {
  const u64 a = (u64)p;
  return (u32x4){(unsigned)a, (unsigned)(a >> 32) & 0xffffu, bytes, 0x00020000u};
}
__device__ __forceinline__ void dma16(u32x4 d, unsigned lds_dst, unsigned voff, unsigned soff) {
  asm volatile("s_mov_b32 m0, %2\n\ts_nop 3\n\tbuffer_load_dwordx4 %0, %1, %3 offen" DMAUX " lds" ::"v"(voff), "s"(d), "s"(lds_dst), "s"(soff) : "memory");
}
__device__ __forceinline__ u32x4 ld16_async(u32x4 d, unsigned voff, unsigned soff) {
  u32x4 r;
  asm volatile("s_nop 4\n\tbuffer_load_dwordx4 %0, %1, %2, %3 offen sc1" : "=v"(r) : "v"(voff), "s"(d), "s"(soff) : "memory");
  return r;
}
__device__ __forceinline__ u64 ld8_async(u32x4 d, unsigned voff) {
  u64 r;
  asm volatile("s_nop 4\n\tbuffer_load_dwordx2 %0, %1, %2, 0 offen sc1" : "=v"(r) : "v"(voff), "s"(d) : "memory");
  return r;
}
__device__ __forceinline__ void st16_async(u32x4 d, unsigned voff, u32x4 v) {
  asm volatile("s_nop 4\n\tbuffer_store_dwordx4 %0, %1, %2, 0 offen sc1\n\ts_nop 1" ::"v"(v), "v"(voff), "s"(d) : "memory");
}
__device__ __forceinline__ void lds_set(LAS char* p, unsigned v = 1u) { __hip_atomic_store((LAS unsigned*)p, v, __ATOMIC_RELAXED, __HIP_MEMORY_SCOPE_WORKGROUP); }
__device__ __forceinline__ void touch(u32x4 d, unsigned lds_dst, unsigned voff, unsigned soff) {
  asm volatile("s_mov_b32 m0, %2\n\ts_nop 3\n\tbuffer_load_dword %0, %1, %3 offen lds" ::"v"(voff), "s"(d), "s"(lds_dst), "s"(soff) : "memory");
}
__device__ __forceinline__ void lds_wait(LAS char* p, unsigned want = 1u) {
#pragma unroll 1
  for (unsigned spins = 0; spins < SPIN_CAP * 8; spins++) {
    if ((unsigned)__builtin_amdgcn_readfirstlane(__hip_atomic_load((LAS unsigned*)p, __ATOMIC_RELAXED, __HIP_MEMORY_SCOPE_WORKGROUP)) >= want) break;
    __builtin_amdgcn_s_sleep(1);
  }
  asm volatile("" ::: "memory");
}
__device__ __forceinline__ bf16x8 ld128(LAS char* p) { return *(LAS bf16x8*)p; }
__device__ __forceinline__ bf16x8 ldtr(LAS char* p0, LAS char* p1) {
  bf16x4 a = __builtin_amdgcn_ds_read_tr16_b64_v4bf16((LAS bf16x4*)p0);
  bf16x4 b = __builtin_amdgcn_ds_read_tr16_b64_v4bf16((LAS bf16x4*)p1);
  return __builtin_shufflevector(a, b, 0, 1, 2, 3, 4, 5, 6, 7);
}
__device__ __forceinline__ float sigmoidf_(float g) {
  return __builtin_amdgcn_rcpf(1.0f + __builtin_amdgcn_exp2f(-1.4426950408889634f * g));
}
__device__ __forceinline__ f32x2 fma2(f32x2 a, f32x2 b, f32x2 c) { return __builtin_elementwise_fma(a, b, c); }
__device__ __forceinline__ f32x2 lo2(f32x4 v) { return __builtin_shufflevector(v, v, 0, 1); }
__device__ __forceinline__ f32x2 hi2(f32x4 v) { return __builtin_shufflevector(v, v, 2, 3); }
__device__ __forceinline__ void csq(float& r, float& i) { const float t = r * r - i * i; i = 2.f * r * i; r = t; }

struct Params {
  const u16 *x, *lre, *lim, *bre, *bim, *cre, *cim, *ldt, *dsk, *wg, *wu, *wd, *gw;
  char* ws;
  float* out;
};

__device__ __forceinline__ void ssm_consts(u16 ldt, u16 lre, u16 lim, float& lr, float& li, float& cr, float& ci) {
  const float dt = rbf(expf(bf2f(ldt)));
  const float a = bf2f(lre), b = bf2f(lim);
  const float m = expf(dt * a);
  float s, c;
  sincosf(dt * b, &s, &c);
  lr = m * c;
  li = m * s;
  const float nr = lr - 1.0f, ni = li, rden = 1.0f / (a * a + b * b);
  cr = (nr * a + ni * b) * rden;
  ci = (ni * a - nr * b) * rden;
}
__device__ __forceinline__ void share2(float v, float (&o)[2]) {
  const auto t = __builtin_amdgcn_permlane16_swap(__float_as_uint(v), __float_as_uint(v), false, false);
  o[0] = __uint_as_float(t[0]); o[1] = __uint_as_float(t[1]);
}

__device__ __forceinline__ u64 ld_flag(rsrc_t r, unsigned voff, unsigned soff) {
  return __builtin_bit_cast(u64, __builtin_amdgcn_raw_buffer_load_b64(r, voff, soff, SC1));
}
__device__ __forceinline__ void st_flag(rsrc_t r, unsigned voff, unsigned soff, u64 tag) {
  __builtin_amdgcn_raw_buffer_store_b64(__builtin_bit_cast(u32x2, tag), r, voff, soff, SC1);
}
__device__ __forceinline__ void poll_flags(rsrc_t r, unsigned voff, unsigned soff, u64 tag) {
#pragma unroll 1
  for (unsigned spins = 0; spins < SPIN_CAP; spins++) {
    asm volatile("" ::: "memory");
    if (__all(ld_flag(r, voff, soff) == tag)) break;
    __builtin_amdgcn_s_sleep(PSL);
  }
  asm volatile("" ::: "memory");
}

template <bool GLU, int NR>
__device__ __forceinline__ void main_loop(LAS char* lds, int wr, int wc, int lane, f32x4 (&acc)[8][NR]) {
  const int i = lane & 15, g = lane >> 4;
  const int sw = (i >> 1) & 7;
  LAS char *resA[2], *rolA[4];
  if constexpr (GLU) {
    resA[0] = lds + (wr * 80 + i) * 128 + ((g ^ sw) << 4);
    resA[1] = lds + (wr * 80 + i) * 128 + ((g ^ sw ^ 4) << 4);
    const int q = (lane & 15) >> 2, pp = lane & 3;
    const int swz = q | ((g & 1) << 2);
#pragma unroll
    for (int c = 0; c < 4; c++) rolA[c] = lds + 20480 + (8 * g + q) * 256 + (((wc * 4 + c) ^ swz) << 5) + pp * 8;
  } else {
    resA[0] = lds + 32768 + (wc * 32 + i) * 128 + ((g ^ sw) << 4);
    resA[1] = lds + 32768 + (wc * 32 + i) * 128 + ((g ^ sw ^ 4) << 4);
    rolA[0] = lds + (wr * 128 + i) * 128 + ((g ^ sw) << 4);
    rolA[1] = lds + (wr * 128 + i) * 128 + ((g ^ sw ^ 4) << 4);
    rolA[2] = rolA[3] = lds;
  }
  auto ld_res = [&](int ks, int r) -> bf16x8 {
    if constexpr (GLU) return ld128(resA[ks] + r * 2048);
    else return ld128(resA[ks] + (r & 1) * 2048 + (r >> 1) * 8192);
  };
  auto ld_rol = [&](int j) -> bf16x8 {
    const int ks = j >> 3, c = j & 7;
    if constexpr (GLU) {
      LAS char* b = rolA[c & 3] + (c >> 2) * 16384 + ks * 8192;
      return ldtr(b, b + 1024);
    } else {
      return ld128(rolA[ks] + c * 2048);
    }
  };
#pragma unroll
  for (int c = 0; c < 8; c++)
#pragma unroll
    for (int r = 0; r < NR; r++) { acc[c][r] = (f32x4){0.f, 0.f, 0.f, 0.f}; PIN(acc[c][r]); }
  bf16x8 res[2][NR], rol[5];
  BARRIER();
  rol[0] = ld_rol(0); SCHED(); res[0][0] = ld_res(0, 0); SCHED(); res[0][1] = ld_res(0, 1); SCHED();
  rol[1] = ld_rol(1); SCHED(); res[0][2] = ld_res(0, 2); SCHED(); res[0][3] = ld_res(0, 3); SCHED();
  rol[2] = ld_rol(2); SCHED();
  if constexpr (NR > 4) res[0][NR - 1] = ld_res(0, NR - 1);
  SCHED();
  int slot = 0;
#pragma unroll 1
  for (unsigned s = 0; s < 8; s++) {
#pragma unroll
    for (int j = 0; j < 16; j++) {
      int nds = 0;
      constexpr int RD = GLU ? 2 : 1;
      if (j == 12) {
        const int delta = (slot == 2) ? -2 * SLOT : SLOT;
        slot = (slot == 2) ? 0 : slot + 1;
        resA[0] += delta; resA[1] += delta;
        rolA[0] += delta; rolA[1] += delta;
        PIN(resA[0]); PIN(resA[1]); PIN(rolA[0]); PIN(rolA[1]);
        if constexpr (GLU) { rolA[2] += delta; rolA[3] += delta; PIN(rolA[2]); PIN(rolA[3]); }
      }
      if (j == 13) {
        BARRIER();
        SCHED();
      }
      if (j != 12) { rol[(j + 3) & 3] = ld_rol((j + 3) & 15); nds += RD; }
      if (j == 10) { rol[4] = ld_rol(15); nds += RD; }
      if (j >= 2 && j < 2 + NR) { res[1][j - 2] = ld_res(1, j - 2); nds++; }
      if (j == 13) { res[0][0] = ld_res(0, 0); res[0][1] = ld_res(0, 1); nds += 2; }
      if (j == 14) { res[0][2] = ld_res(0, 2); res[0][3] = ld_res(0, 3); nds += 2; }
      if (j == 15 && NR > 4) { res[0][NR - 1] = ld_res(0, NR - 1); nds++; }
#pragma unroll
      for (int r = 0; r < NR; r++)
        acc[j & 7][r] = __builtin_amdgcn_mfma_f32_16x16x32_bf16(rol[j == 15 ? 4 : (j & 3)], res[j >> 3][r], acc[j & 7][r], 0, 0, 0);
#pragma unroll
      for (int r = 0; r < NR; r++) {
        __builtin_amdgcn_sched_group_barrier(0x8, 1, 0);
        if (j == 12) __builtin_amdgcn_sched_group_barrier(0x2, 2, 0);
        if (r < nds) __builtin_amdgcn_sched_group_barrier(0x100, 1, 0);
      }
      SCHED();
    }
  }
}

__device__ __forceinline__ constexpr int wslot(int i) { return (i / 3 == 0 ? 2 : (i / 3 == 1 ? 0 : 1)) * SLOT + (i % 3) * 4096; }
__device__ __forceinline__ constexpr int gstep(int i) { return i < 7 ? 2 * i : 2 * i - 13; }
constexpr int C_TAG = LDS_CTL, C_F0 = LDS_CTL + 16, C_F1 = LDS_CTL + 32, C_F2 = LDS_CTL + 48, C_F3 = LDS_CTL + 64, C_FA = LDS_CTL + 80;
constexpr int C_DUMP = LDS_CTL + 1024;

__global__ __launch_bounds__(512, 2) void k_fused(Params p) {
  extern __shared__ __attribute__((aligned(16))) char smem[];
  LAS char* lds = (LAS char*)smem;
  const int tid = threadIdx.x, lane = tid & 63;
  const int wid = __builtin_amdgcn_readfirstlane(tid >> 6);
  const int xg = blockIdx.x & 7, idx = blockIdx.x >> 3;
  int role, rt, ct = 0, myid;
  const int hh = xg & 1, cj = xg >> 1, odd = cj & 1;
  const int ncol = 4 - odd, col0 = (cj >> 1) * 7 + odd * 4, nssm = 4 + odd * 8;
  if (idx < nssm) {
    role = 1; rt = hh * 4 + (idx & 3); ct = (cj >> 1) * 4 + odd + (idx >> 2); myid = 182 + rt * 8 + ct;
  } else {
    const int e = idx - nssm, nfull = ncol * 6, nmid = hh ? ncol - 2 : 2;
    if (e < nfull) { role = 0; const int c = e / 6; rt = hh * 7 + (e - c * 6); ct = col0 + c; }
    else if (e < nfull + nmid) { role = 0; rt = 6; ct = col0 + hh * 2 + (e - nfull); }
    else if (!odd) { role = 2; rt = cj * 2 + hh * 2 + (e - nfull - nmid); }
    else { role = 3; rt = 0; }
    myid = role == 0 ? rt * 14 + ct : (role == 2 ? 246 + rt : 254 + (cj >> 1));
  }
  const int rb = MAP2 ? (xg >> 1) * 8 + (idx >> 2) : xg * 4 + (idx >> 3), cb = MAP2 ? (xg & 1) * 4 + (idx & 3) : idx & 7;
  const int w4 = wid & 3;
  const rsrc_t hr = mkrsrc(p.ws + H_OFF, (unsigned)H_BYTES), wsr = mkrsrc(p.ws, (unsigned)H_OFF);
  const int n = lane & 15, g = lane >> 4;
  STAMP(0);
  f32x4 acc2[4][4];
  u64 tag;
  unsigned vh[4];
  auto ldh = [&](int k, int m) -> bf16x8 {
    return __builtin_bit_cast(bf16x8, __builtin_amdgcn_raw_buffer_load_b128(hr, vh[m] + (k & 3) * 1024, (k & ~3) * 1024, SC1));
  };
  auto set_vh = [&]() { vh[1] = vh[0] + HM; vh[2] = vh[0] + 2 * HM; vh[3] = vh[0] + 3 * HM; PIN(vh[1]); PIN(vh[2]); PIN(vh[3]); };
  const unsigned gflag = (246 * 4 + rb) * FS;
  auto glu_flags = [&]() -> unsigned {
    const int l14 = lane < 14 ? lane : 0, j = l14 >= 7 ? l14 - 7 : l14, pr = 7 * w4 + j;
    const int mt = rb * 4 + (l14 >= 7 ? 3 : 0), om = mt < 120 ? mt / 5 : (mt < 123 ? 24 : 25);
    return lane == 14 ? gflag : (((om >> 1) * 14 + (pr >> 1)) * 4 + (om & 1) * 2 + (pr & 1)) * FS;
  };
  float gv[2];
  u32x2 xv[2], dv;
  auto ld_final = [&]() {
    int ln = lane;
    PIN(ln);
    const int t = rb * 64 + wid * 8 + (ln >> 4), dcol = cb * 64 + (ln & 15) * 4;
    const rsrc_t xr_ = mkrsrc(p.x, L * D * 2);
#pragma unroll
    for (int u = 0; u < 2; u++) {
      gv[u] = __uint_as_float(__builtin_amdgcn_raw_buffer_load_b32(wsr, t * 4 + u * 16, (unsigned)G_OFF, SC1));
      xv[u] = __builtin_amdgcn_raw_buffer_load_b64(xr_, t * 1024 + dcol * 2, u * 4096, 0);
    }
    dv = *(const u32x2*)(p.dsk + dcol);
  };

  LAS char *sb[2], *sa[2];
  f32x4 sk[2];
  float omg[2];
  unsigned vo;
  auto prep_final = [&]() {
    int ln = lane;
    PIN(ln);
    const int nn = ln & 15, mt = wid >> 1;
    const int ob = mt < 3 ? wslot(0) + mt * 4096 : wslot(3), oa = mt == 0 ? wslot(5) : wslot(6) + (mt - 1) * 4096;
    vo = ((rb * 64 + wid * 8 + (ln >> 4)) * D + cb * 64 + nn * 4) * 4;
#pragma unroll
    for (int u = 0; u < 2; u++) {
      const int sl = ((wid & 1) * 8 + u * 4 + (ln >> 4)) ^ nn;
      sb[u] = lds + ob + nn * 256 + (sl << 4);
      sa[u] = lds + oa + nn * 256 + ((sl ^ (4 * mt)) << 4);
      omg[u] = rbf(1.0f - gv[u]);
#pragma unroll
      for (int e = 0; e < 4; e++) {
        const unsigned xw = xv[u][e >> 1], dw = dv[e >> 1];
        const float xf = __uint_as_float((e & 1) ? (xw & 0xffff0000u) : (xw << 16));
        const float df = __uint_as_float((e & 1) ? (dw & 0xffff0000u) : (dw << 16));
        sk[u][e] = rbf(xf * df);
      }
      PIN(sb[u]); PIN(sa[u]); PIN(omg[u]); PIN(sk[u]);
    }
    PIN(vo);
  };

  if (wid >= 4) {
    const int lw = w4;
    const u32x4 wsd = mkdesc(p.ws, (unsigned)H_OFF);
    const u32x4 wdr = mkdesc(p.wd, F * D * 2), xr = mkdesc(p.x, L * D * 2);
    const unsigned vwd = (lw * 14 * 32 + (lane >> 3)) * 1024 + cb * 128 + (((lane & 7) ^ (((lane >> 4) & 3) << 1)) << 4);
    const int k0 = (2 * cj + 4 * hh) & 7, gam = NOG ? 0 : (rt + ct) & 1;
    u32x4 ctv;
    u64 cfl;
    auto ct_load = [&]() {
      const int b = blockIdx.x;
      ctv = ld16_async(mkdesc((b & 1) ? p.cim : p.cre, D * P * 2),
                       ((b >> 5) * 64 + ((b >> 1) & 3) * 16 + n) * 1024 + ((b >> 3) & 3) * 256 + lw * 64 + g * 16, 0);
    };
    auto ct_store = [&]() { st16_async(wsd, (unsigned)CT_OFF + (blockIdx.x * 4 + lw) * 1024 + lane * 16, ctv); };
    auto ct_flag = [&]() { st_flag(wsr, lane == 0 ? (blockIdx.x * 4 + lw) * FS : OOR, FC_OFF, tag); };
    const unsigned cfo = FC_OFF + ((cb * 4 + lw) * 32 + (lane & 31)) * FS;
    auto ct_ask = [&]() { cfl = ld8_async(wsd, cfo); };
    auto ct_check = [&]() { PIN(cfl); if (!__all(cfl == tag)) poll_flags(wsr, cfo, 0, tag); };
    u32x4 cf[32];
    const unsigned vcf = (cb * 4 + lw) * 32768 + lane * 16;
    auto ldc = [&](int a, int b) {
#pragma unroll
      for (int f = a; f < b; f++) cf[f] = ld16_async(wsd, vcf, (unsigned)CT_OFF + f * 1024);
    };
    auto wpiece = [&](int i, int k) { dma16(wdr, wslot(i < 9 ? i : i - 9) + lw * 12288 + k * 1024, vwd, (gstep(i) * 32 + k * 8) * 1024); };
    auto wround = [&](int r) {
#pragma unroll
      for (int q = 0; q < 12; q++) wpiece(r * 3 + (q >> 2), q & 3);
    };
    const u32x4 gr = mkdesc(p.wg, D * F * 2), ur = mkdesc(p.wu, D * F * 2);
    auto pre_1 = [&]() {
      const unsigned v = (((k0 + 2) & 7) * 64 + idx * 4 + lw) * (F * 2) + col0 * 256 + (lane < ncol * 16 ? lane : 0) * 16;
      dma16(gr, C_DUMP, v, 0);
      dma16(ur, C_DUMP, v, 0);
    };
    auto pre_w = [&]() { touch(wdr, C_DUMP, (lane < 14 ? lane : 0) * 128, (blockIdx.x * 4 + lw) * 1792); };
    auto loader = [&](auto piece, auto np_, auto na_, auto nb_) {
      constexpr int NP = decltype(np_)::value, NA = decltype(na_)::value, NB = decltype(nb_)::value;
      auto pieces = [&](int s, int a, int b) {
        const int sg = ((s ^ gam) + k0) & 7;
#pragma unroll
        for (int k = a; k < b; k++) piece(s, sg, k);
      };
      LST(27); pieces(0, 0, 1); LST(28); pieces(0, 1, NP); LST(29); ct_load(); pre_1(); if (!PWL) pre_w(); pieces(1, 0, S1A); LST(0); WAITV(4 - PWL + S1A); LST(1); BARRIER(); LST(2);
      tag = *(LAS u64*)(lds + C_TAG);
      pieces(1, S1A, NP); pieces(2, 0, S2A); LST(3); WAITV(S2A); LST(4); BARRIER(); LST(5);
      ct_store(); pieces(2, S2A, NP); pieces(3, 0, S3A); LST(6); WAITV(S3A); ct_flag(); LST(7); BARRIER(); LST(8);
      pieces(3, S3A, NP);
      pieces(4, 0, NP); LST(9); WAITV(NP); if (PWL) pre_w(); LST(10); BARRIER(); LST(11);
      if (NA) ct_ask();
      pieces(5, 0, NP); LST(12); WAITV(NP);
      if (NA) ct_check();
      ldc(0, NA); LST(13); BARRIER(); LST(14);
      pieces(6, 0, NP); ldc(NA, 2 * NA); LST(15); WAITV(NP + 2 * NA); LST(16); BARRIER(); LST(17);
      if (!NA) ct_ask();
      pieces(7, 0, NP); ldc(2 * NA, 3 * NA); LST(18); WAITV(NP + 2 * NA); LST(19); BARRIER(); LST(20);
      if (!NA) ct_check();
      wround(0); ldc(3 * NA, 3 * NA + NB); LST(21); WAITV(NA + 12 + NB); LST(22); BARRIER(); LST(23);
      wround(1); ldc(3 * NA + NB, 3 * NA + 2 * NB); LST(25); BARRIER(); LST(26);
      WAITV(NB);
      lds_set(lds + C_F0 + lw * 4);
    };
    if (role == 0) {
      const int r0 = min(rt * 160, L - 160);
      const int xrow = 8 * lw + (lane >> 3);
      const unsigned vx = (r0 + xrow) * 1024 + (((lane & 7) ^ ((xrow >> 1) & 7)) << 4);
      const int krow = 4 * lw + (lane >> 4);
      const int swz = (krow & 3) | (((krow >> 3) & 1) << 2);
      const unsigned vw = krow * (F * 2) + ct * 256 + (((lane & 15) ^ (swz << 1)) << 4);
      loader([&](int s, int sg, int k) {
        const unsigned b = (s % 3) * SLOT + lw * 1024;
        if (k < 5) dma16(xr, b + k * 4096, vx, sg * 128 + k * 32768);
        else { const int i = (k - 5) >> 1; dma16(((k - 5) & 1) ? ur : gr, b + 20480 + ((k - 5) & 1) * 16384 + i * 4096, vw, (sg * 64 + i * 16) * (F * 2)); }
      }, std::integral_constant<int, 13>{}, std::integral_constant<int, GNA>{}, std::integral_constant<int, GNB>{});
      STAMP(1);
      ldc(3 * GNA + 2 * GNB, 32);
      wround(2);
      WAITV(12);
    } else {
      if (role == 1) {
        const int r0 = rt * 256;
        const u32x4 br = mkdesc(p.bre, P * D * 2), bi = mkdesc(p.bim, P * D * 2);
        const int ii = 8 * (lw & 1) + (lane >> 3);
        const int csw = ((lane & 7) ^ ((ii >> 1) & 7)) << 4;
        const unsigned vx = (r0 + 32 * (ii >> 2) + 16 * (ii & 1) + ((ii >> 1) & 1) + 2 * (lw >> 1)) * 1024 + csw;
        const unsigned vb = (ct * 64 + 8 * lw + (lane >> 3)) * 1024 + csw;
        loader([&](int s, int sg, int k) {
          const unsigned b = (s % 3) * SLOT + lw * 1024;
          if (k < 4) dma16((k & 1) ? bi : br, b + 32768 + (k & 1) * 8192 + (k >> 1) * 4096, vb, sg * 128 + (k >> 1) * 32768);
          else { const int i = k - 4; dma16(xr, b + i * 4096, vx, sg * 128 + (128 * (i >> 2) + 4 * (i & 3)) * 1024); }
        }, std::integral_constant<int, 12>{}, std::integral_constant<int, 0>{}, std::integral_constant<int, SNB>{});
        wround(2);
        WAITV(12);
      } else {
        ct_load();
        BARRIER();
        tag = *(LAS u64*)(lds + C_TAG);
        WAITV(0); ct_store(); WAITV(0); ct_flag();
        pre_1(); pre_w();
        wround(0); wround(1); wround(2);
        WAITV(12);
        lds_set(lds + C_F0 + lw * 4);
        poll_flags(wsr, cfo, 0, tag);
        ldc(0, 2 * SNB);
        WAITV(0);
        if (myid == 255 && lw == 3) {
#pragma unroll 1
          for (int k = 0; k < 4; k++) poll_flags(wsr, (k * 64 + lane) * 4 * FS, FC_OFF, tag);
          st_flag(wsr, lane == 0 ? (unsigned)EPOCH_OFF : OOR, 0, tag);
        }
      }
      ldc(2 * SNB, 32);
      WAITV(0);
    }
    const unsigned fs = lane < 16 ? FA_OFF + glu_flags()
                                  : FB_OFF + ((182 + (rb >> 2) * 8 + lw * 2 + ((lane >> 1) & 1)) * 4 + (rb & 2) + (lane & 1)) * FS;
    vh[0] = rb * 4 * HM + (56 + lw * 8) * 1024 + lane * 16;
    set_vh();
    LAS char* pb[4];
#pragma unroll
    for (int k = 0; k < 4; k++) {
      const int mt = n >> 2;
      pb[k] = lds + (mt == 0 ? wslot(5) : wslot(6) + (mt - 1) * 4096) + lw * 12288 + g * 256 + ((((n & 3) ^ g) | ((k ^ mt) << 2)) << 4);
      PIN(pb[k]);
    }
    STAMP(2);
    lds_wait(lds + C_F1 + lw * 4);
    STAMP(3);
    {
      u64 miss = ~0ull;
      bool sent = false;
      auto look = [&]() {
        cfl = ld8_async(wsd, fs);
        WAITV(0);
        PIN(cfl);
        miss = __ballot(cfl != tag);
        if (!sent && (miss & 0xffffull) == 0) { sent = true; lds_set(lds + C_FA + lw * 4); }
      };
#pragma unroll
      for (int i = 9; i < 14; i++) {
#pragma unroll
        for (int k = 0; k < 4; k++) wpiece(i, k);
        if (!sent || i == 13) look();
        if (i == 9) lds_set(lds + C_F2 + lw * 4);
      }
      lds_set(lds + C_F2 + lw * 4, 2u);
      STAMP(6);
#pragma unroll 1
      for (unsigned spins = 0; miss != 0 && spins < SPIN_CAP; spins++) {
        __builtin_amdgcn_s_sleep(PSL);
        look();
      }
      if (!sent) lds_set(lds + C_FA + lw * 4);
      asm volatile("" ::: "memory");
    }
    STAMP(7);
    bf16x8 hf[3][4];
#pragma unroll
    for (int k = 0; k < 3; k++)
#pragma unroll
      for (int m = 0; m < 4; m++) hf[k][m] = ldh(k, m);
#pragma unroll
    for (int f = 0; f < 32; f++) PIN(cf[f]);
#pragma unroll
    for (int c = 0; c < 4; c++)
#pragma unroll
      for (int m = 0; m < 4; m++) acc2[c][m] = (f32x4){0.f, 0.f, 0.f, 0.f};
    SCHED();
#pragma unroll
    for (int k = 0; k < 8; k++) {
      if (k == 3) ld_final();
      if (k == 6) prep_final();
      if (k == 7) lds_wait(lds + C_F3 + lw * 4);
#pragma unroll
      for (int m = 0; m < 4; m++) {
#pragma unroll
        for (int c = 0; c < 4; c++)
          acc2[c][m] = __builtin_amdgcn_mfma_f32_16x16x32_bf16(__builtin_bit_cast(bf16x8, cf[c * 8 + (k & 1) * 4 + (k >> 1)]), hf[k % 3][m], acc2[c][m], 0, 0, 0);
        if (k + 3 < 8) hf[k % 3][m] = ldh(k + 3, m);
        if (k == 7 && m > 0) {
#pragma unroll
          for (int c = 0; c < 4; c++) *(LAS f32x4*)(pb[(m - 1) ^ c] + c * 1024) = acc2[c][m - 1];
        }
        SCHED();
      }
    }
    STAMP(8);
#pragma unroll
    for (int c = 0; c < 4; c++) *(LAS f32x4*)(pb[3 ^ c] + c * 1024) = acc2[c][3];
  } else {
    const int wr = wid >> 1, wc = wid & 1;
    const int chg = (ct * 64 + wc * 32 + (lane & 31)) & (P - 1);
    const u16 c_ldt = p.ldt[chg], c_lre = p.lre[chg], c_lim = p.lim[chg];
    if (wid >= 2 && idx * 2 + (wid & 1) < CODE_KB) {
      const u32x4 cd = mkdesc((const void*)(__builtin_amdgcn_s_getpc() & ~255ull), CODE_KB * 1024);
      dma16(cd, C_DUMP, lane * 16, (idx * 2 + (wid & 1)) * 1024);
    }
    if (wid == 0) {
      u64 w0;
      asm volatile("s_load_dwordx2 %0, %1, 0x0\n\ts_waitcnt lgkmcnt(0)" : "=s"(w0) : "s"(p.ws + EPOCH_OFF) : "memory");
      *(LAS u64*)(lds + C_TAG + lane * 8) = lane == 0 ? w0 + TAG_STEP : 0ull;
    }
    unsigned vfo = lane == 0 ? (myid * 4 + wid) * FS : OOR;
    unsigned vfi = glu_flags();
    vh[0] = rb * 4 * HM + wid * 14 * 1024 + lane * 16;
    LAS char* ra0 = lds + wid * 12288 + (4 * g + (n >> 2)) * 128 + (((2 * g + (n >> 3)) & 3) << 5) + (n & 3) * 8;
    PIN(vfo); PIN(vfi); PIN(vh[0]); PIN(ra0);
    bf16x8 wf[6][4];
    LAS char* ra[3][4];
    auto ldw = [&](int i, int c) -> bf16x8 {
      const int k = i < 9 ? i : i - 9;
      LAS char* a = ra[k / 3][c] + (k % 3) * 4096;
      return ldtr(a, a + 2048);
    };
    auto grab_w = [&]() {
#pragma unroll
      for (int c = 0; c < 4; c++) {
        ra[1][c] = (LAS char*)((unsigned)(size_t)ra0 ^ (c << 5));
        ra[0][c] = ra[1][c] + 2 * SLOT;
        ra[2][c] = ra[1][c] + SLOT;
      }
      lds_wait(lds + C_F0 + wid * 4);
#pragma unroll
      for (int i = 0; i < 5; i++)
#pragma unroll
        for (int c = 0; c < 4; c++) wf[i][c] = ldw(i, c);
      asm volatile("" ::: "memory");
      lds_set(lds + C_F1 + wid * 4);
    };
    if (role == 0) {
      const int mt0 = (min(rt * 160, L - 160) >> 4) + wr * 5;
      unsigned vs[5];
#pragma unroll
      for (int m = 0; m < 5; m++) { vs[m] = mt0 + m < rt * 10 ? OOR : (mt0 + m) * HM + (ct * 4 + wc * 2) * 1024 + lane * 16; PIN(vs[m]); }
      f32x4 acc[8][5];
      STAMP(1);
      main_loop<true, 5>(lds, wr, wc, lane, acc);
      STAMP(2);
      tag = *(LAS u64*)(lds + C_TAG);
#pragma unroll
      for (int h = 0; h < 2; h++) {
        if (h == 1) { SCHED(); STAMP(13); grab_w(); STAMP(14); SCHED(); }
#pragma unroll
        for (int m = 0; m < 5; m++) {
          if (h == 1 && m == PUBA) { WAITV(PUBA); st_flag(wsr, vfo, FA_OFF, tag); STAMP(3); }
          u32x4 o;
#pragma unroll
          for (int e = 0; e < 4; e++) {
            const int c = 2 * h + (e >> 1);
            const f32x2 gv_ = (e & 1) ? hi2(acc[c][m]) : lo2(acc[c][m]);
            const f32x2 uv = (e & 1) ? hi2(acc[4 + c][m]) : lo2(acc[4 + c][m]);
            f32x2 t = gv_ * -1.4426950408889634f;
            t = (f32x2){__builtin_amdgcn_exp2f(t.x), __builtin_amdgcn_exp2f(t.y)} + 1.0f;
            t = (f32x2){__builtin_amdgcn_rcpf(t.x), __builtin_amdgcn_rcpf(t.y)} * uv;
            o[e] = pack2(t.x, t.y);
          }
          __builtin_amdgcn_raw_buffer_store_b128(o, hr, vs[m] + h * 1024, 0, SC1);
        }
      }
    } else {
      if (role == 1) {
        float lr[2], li[2], cr[2], ci[2], sr[2], si[2], mr[2], mi[2], wr_[2], wi_[2], qr, qi, zr, zi;
        {
          float a2, a3;
          ssm_consts(c_ldt, c_lre, c_lim, qr, qi, a2, a3);
          share2(qr, lr); share2(qi, li); share2(2.f * a2, cr); share2(2.f * a3, ci);
          csq(qr, qi); csq(qr, qi); csq(qr, qi); csq(qr, qi);
          share2(qr, sr); share2(qi, si);
          csq(qr, qi);
          share2(qr, mr); share2(qi, mi);
          csq(qr, qi); csq(qr, qi);
          zr = qr; zi = qi;
          csq(zr, zi); csq(zr, zi); csq(zr, zi);
          if (rt * 2 + wr <= 8) { zr = 0.f; zi = 0.f; }
#pragma unroll
          for (int u = 0; u < 2; u++) {
            float ar = mr[u], ai = mi[u];
            csq(ar, ai);
            const float br_ = (g & 1) ? mr[u] : 1.f, bi_ = (g & 1) ? mi[u] : 0.f;
            wr_[u] = (g & 2) ? ar * br_ - ai * bi_ : br_;
            wi_[u] = (g & 2) ? ar * bi_ + ai * br_ : bi_;
            PIN(mr[u]); PIN(mi[u]); PIN(lr[u]); PIN(li[u]); PIN(cr[u]); PIN(ci[u]); PIN(sr[u]); PIN(si[u]); PIN(wr_[u]); PIN(wi_[u]);
          }
          PIN(qr); PIN(qi); PIN(zr); PIN(zi);
        }
        const int ck = rt * 2 + wr;
        unsigned veo = lane < 32 ? (unsigned)E_OFF + (ck * 512 + chg) * 16 : OOR;
        const int c0 = ck - 1 - 8 * (lane >> 5);
        int vei = (c0 * 512 + chg) * 16, nval = min(max(c0 + 1, 0), 8);
        const unsigned tb = (wid >> 1) * 2 * SLOT + 49152 + (wid & 1) * 2048;
        LAS char* twp = lds + tb + n * 64 + ((g ^ (((n >> 1) & 1) | ((((n >> 2) ^ (n >> 3)) & 1) << 1))) << 4);
        LAS char* trp[2];
#pragma unroll
        for (int t = 0; t < 2; t++)
          trp[t] = lds + tb + (8 * g + (n >> 2)) * 64 + (((((n >> 1) ^ (n >> 3)) & 1) | (((g ^ t) & 1) << 1)) << 4) + (n & 1) * 8;
        unsigned vso[8];
#pragma unroll
        for (int k = 0; k < 8; k++) { vso[k] = (rt * 16 + wr * 8 + k) * HM + (56 + (ct * 2 + wc) * 2) * 1024 + lane * 16; PIN(vso[k]); }
        PIN(veo); PIN(vei); PIN(nval); PIN(twp); PIN(trp[0]); PIN(trp[1]);
        f32x4 acc[8][4];
        STAMP(1);
        main_loop<false, 4>(lds, wr, wc, lane, acc);
        STAMP(2);
        tag = *(LAS u64*)(lds + C_TAG);
        f32x2 er[2], ei[2];
#pragma unroll
        for (int u = 0; u < 2; u++) { er[u] = lo2(acc[0][u]); ei[u] = lo2(acc[0][2 + u]); }
#pragma unroll
        for (int st = 1; st < 16; st++)
#pragma unroll
          for (int u = 0; u < 2; u++) {
            const f32x2 l2 = {lr[u], lr[u]}, i2 = {li[u], li[u]};
            const f32x2 vr = (st & 1) ? hi2(acc[st >> 1][u]) : lo2(acc[st >> 1][u]);
            const f32x2 vi = (st & 1) ? hi2(acc[st >> 1][2 + u]) : lo2(acc[st >> 1][2 + u]);
            const f32x2 t = fma2(-i2, ei[u], fma2(l2, er[u], vr));
            ei[u] = fma2(i2, er[u], fma2(l2, ei[u], vi));
            er[u] = t;
          }
        float xs[2][4], ys[2][4];
#pragma unroll
        for (int u = 0; u < 2; u++) {
          const float a = fmaf(-si[u], ei[u].x, fmaf(sr[u], er[u].x, er[u].y)), b = fmaf(si[u], er[u].x, fmaf(sr[u], ei[u].x, ei[u].y));
          float a2[2], b2[2];
          share2(a, a2); share2(b, b2);
#pragma unroll
          for (int k = 0; k < 2; k++) {
            const auto ta = __builtin_amdgcn_permlane32_swap(__float_as_uint(a2[k]), __float_as_uint(a2[k]), false, false);
            const auto tb_ = __builtin_amdgcn_permlane32_swap(__float_as_uint(b2[k]), __float_as_uint(b2[k]), false, false);
            xs[u][k] = __uint_as_float(ta[0]); xs[u][2 + k] = __uint_as_float(ta[1]);
            ys[u][k] = __uint_as_float(tb_[0]); ys[u][2 + k] = __uint_as_float(tb_[1]);
          }
#pragma unroll
          for (int k = 1; k < 4; k++) {
            xs[u][k] = fmaf(-mi[u], ys[u][k - 1], fmaf(mr[u], xs[u][k - 1], xs[u][k]));
            ys[u][k] = fmaf(mi[u], xs[u][k - 1], fmaf(mr[u], ys[u][k - 1], ys[u][k]));
            PIN(xs[u][k]); PIN(ys[u][k]);
          }
        }
        const unsigned tg = (unsigned)tag;
        __builtin_amdgcn_raw_buffer_store_b128((u32x4){__float_as_uint(g ? xs[1][3] : xs[0][3]), tg, __float_as_uint(g ? ys[1][3] : ys[0][3]), tg},
                                               wsr, veo, 0, SC1);
        STAMP(3);
        SCHED();
        float pr[2], pi[2];
#pragma unroll
        for (int u = 0; u < 2; u++) {
          pr[u] = g == 0 ? 0.f : (g == 1 ? xs[u][0] : (g == 2 ? xs[u][1] : xs[u][2]));
          pi[u] = g == 0 ? 0.f : (g == 1 ? ys[u][0] : (g == 2 ? ys[u][1] : ys[u][2]));
          PIN(pr[u]); PIN(pi[u]);
        }
        const rsrc_t erc = mkrsrc(p.ws + E_OFF, (unsigned)E_BYTES);
        u32x4 v[8];
        auto ask = [&]() {
#pragma unroll
          for (int d = 0; d < 8; d++) v[d] = __builtin_amdgcn_raw_buffer_load_b128(erc, vei - d * 8192, 0, SC1);
        };
#pragma unroll
        for (int u = 0; u < 2; u++) {
          if (u == 1) { SCHED(); ask(); SCHED(); }
          PIN(cr[u]);
#pragma unroll
          for (int m = 0; m < 8; m++) {
            const f32x4 a = acc[m][u], b = acc[m][2 + u];
            acc[m][u] = cr[u] * a - ci[u] * b;
            acc[m][2 + u] = cr[u] * b + ci[u] * a;
            PIN(acc[m][u]); PIN(acc[m][2 + u]);
          }
        }
#pragma unroll 1
        for (unsigned spins = 0; spins < SPIN_CAP; spins++) {
          int cnt = 0;
#pragma unroll
          for (int d = 0; d < 8; d++) cnt += ((v[d][1] ^ tg) | (v[d][3] ^ tg)) == 0u;
          if (__all(cnt >= nval)) break;
          asm volatile("" ::: "memory");
          __builtin_amdgcn_s_sleep(ESL);
          ask();
        }
        float kr[2], ki[2];
        {
          float xr_ = __uint_as_float(v[7][0]), xi_ = __uint_as_float(v[7][2]);
#pragma unroll
          for (int d = 6; d >= 0; d--) {
            const float t = fmaf(-qi, xi_, fmaf(qr, xr_, __uint_as_float(v[d][0])));
            xi_ = fmaf(qi, xr_, fmaf(qr, xi_, __uint_as_float(v[d][2])));
            xr_ = t;
            PIN(xr_); PIN(xi_);
          }
          const auto ta = __builtin_amdgcn_permlane32_swap(__float_as_uint(xr_), __float_as_uint(xr_), false, false);
          const auto tb_ = __builtin_amdgcn_permlane32_swap(__float_as_uint(xi_), __float_as_uint(xi_), false, false);
          const float fr = __uint_as_float(ta[1]), fi = __uint_as_float(tb_[1]);
          share2(fmaf(-zi, fi, fmaf(zr, fr, __uint_as_float(ta[0]))), kr);
          share2(fmaf(zi, fr, fmaf(zr, fi, __uint_as_float(tb_[0]))), ki);
        }
        STAMP(4);
        f32x2 hr_[2], hi_[2];
#pragma unroll
        for (int u = 0; u < 2; u++) {
          f32x2 a, b;
          a.x = fmaf(-wi_[u], ki[u], fmaf(wr_[u], kr[u], pr[u])); b.x = fmaf(wi_[u], kr[u], fmaf(wr_[u], ki[u], pi[u]));
          a.y = fmaf(-si[u], b.x, fmaf(sr[u], a.x, er[u].x)); b.y = fmaf(si[u], a.x, fmaf(sr[u], b.x, ei[u].x));
          PIN(a); PIN(b);
          hr_[u] = cr[u] * a - ci[u] * b; hi_[u] = cr[u] * b + ci[u] * a;
        }
        bf16x8 tv[4];
#pragma unroll
        for (int qt = 0; qt < 5; qt++) {
          u32x4 o[2][2];
          if (qt < 4) {
#pragma unroll
            for (int mm = 0; mm < 2; mm++)
#pragma unroll
              for (int u = 0; u < 2; u++) {
                const int m = qt * 2 + mm;
                const f32x2 l2 = {lr[u], lr[u]}, i2 = {li[u], li[u]};
                const f32x2 r0_ = fma2(-i2, hi_[u], fma2(l2, hr_[u], lo2(acc[m][u])));
                const f32x2 i0_ = fma2(i2, hr_[u], fma2(l2, hi_[u], lo2(acc[m][2 + u])));
                hr_[u] = fma2(-i2, i0_, fma2(l2, r0_, hi2(acc[m][u])));
                hi_[u] = fma2(i2, r0_, fma2(l2, i0_, hi2(acc[m][2 + u])));
                o[0][u][mm] = pack2(r0_.x, hr_[u].x); o[0][u][2 + mm] = pack2(r0_.y, hr_[u].y);
                o[1][u][mm] = pack2(-i0_.x, -hi_[u].x); o[1][u][2 + mm] = pack2(-i0_.y, -hi_[u].y);
              }
          }
          SCHED();
          if (qt > 0) {
#pragma unroll
            for (int k = 0; k < 4; k++) __builtin_amdgcn_raw_buffer_store_b128(__builtin_bit_cast(u32x4, tv[k]), hr, vso[(k & 1) * 4 + qt - 1] + (k >> 1) * 1024, 0, SC1);
          }
          if (qt < 4) {
#pragma unroll
            for (int part = 0; part < 2; part++) {
              asm volatile("" ::: "memory");
              *(LAS u32x4*)twp = o[part][0];
              *(LAS u32x4*)(twp + 1024) = o[part][1];
              asm volatile("" ::: "memory");
#pragma unroll
              for (int rho = 0; rho < 2; rho++) tv[part * 2 + rho] = ldtr(trp[rho], trp[rho ^ 1] + 256);
            }
          }
          SCHED();
        }
        STAMP(13);
      } else {
        BARRIER();
        tag = *(LAS u64*)(lds + C_TAG);
        if (role == 2) {
          const u32x4 gwv = *(const u32x4*)(p.gw + lane * 8);
          f32x2 gwf[4];
#pragma unroll
          for (int e = 0; e < 4; e++) gwf[e] = (f32x2){__uint_as_float(gwv[e] << 16), __uint_as_float(gwv[e] & 0xffff0000u)};
          const bf16x2 ones = {(__bf16)1.0f, (__bf16)1.0f};
#pragma unroll 1
          for (int it = 0; it < 4; it++) {
            const int r0 = rt * 256 + wid * 64 + it * 16;
            u32x4 xw[16];
#pragma unroll
            for (int r = 0; r < 16; r++) xw[r] = *(const u32x4*)(p.x + (size_t)(r0 + r) * D + lane * 8);
            float s[16];
#pragma unroll
            for (int r = 0; r < 16; r++) {
              s[r] = 0.f;
#pragma unroll
              for (int e = 0; e < 4; e++) {
                const unsigned w = xw[r][e];
                const f32x2 pr = (f32x2){__uint_as_float(w << 16), __uint_as_float(w & 0xffff0000u)} * gwf[e];
                const bf16x2 pb = {(__bf16)pr.x, (__bf16)pr.y};
                s[r] = __builtin_amdgcn_fdot2_f32_bf16(pb, ones, s[r], false);
              }
            }
#pragma unroll
            for (int r = 0; r < 8; r++) {
              const auto t = __builtin_amdgcn_permlane32_swap(__float_as_uint(s[r]), __float_as_uint(s[r + 8]), false, false);
              s[r] = __uint_as_float(t[0]) + __uint_as_float(t[1]);
            }
#pragma unroll
            for (int r = 0; r < 4; r++) {
              const auto t = __builtin_amdgcn_permlane16_swap(__float_as_uint(s[r]), __float_as_uint(s[r + 4]), false, false);
              s[r] = __uint_as_float(t[0]) + __uint_as_float(t[1]);
            }
#pragma unroll
            for (int r = 0; r < 4; r++) {
              s[r] += __uint_as_float(__builtin_amdgcn_update_dpp(0, __float_as_uint(s[r]), 0x128, 0xf, 0xf, false));
              s[r] += __uint_as_float(__builtin_amdgcn_update_dpp(0, __float_as_uint(s[r]), 0x124, 0xf, 0xf, false));
              s[r] += __uint_as_float(__builtin_amdgcn_update_dpp(0, __float_as_uint(s[r]), 0x122, 0xf, 0xf, false));
              s[r] += __uint_as_float(__builtin_amdgcn_update_dpp(0, __float_as_uint(s[r]), 0x121, 0xf, 0xf, false));
            }
            const float sv = (n & 2) ? ((n & 1) ? s[3] : s[2]) : ((n & 1) ? s[1] : s[0]);
            __builtin_amdgcn_raw_buffer_store_b32(__float_as_uint(rbf(sigmoidf_(rbf(sv)))), wsr,
                                                  n < 4 ? (unsigned)G_OFF + (r0 + n + 4 * (g & 1) + 8 * (g >> 1)) * 4 : OOR, 0, SC1);
          }
          WAITV(0);
          st_flag(wsr, vfo, FA_OFF, tag);
        }
      }
      grab_w();
    }
    STAMP(5);
    set_vh();
    const bool early = __builtin_amdgcn_readfirstlane(__hip_atomic_load((LAS unsigned*)(lds + C_FA + wid * 4), __ATOMIC_RELAXED, __HIP_MEMORY_SCOPE_WORKGROUP));
    if (!early) {
      WAITV(0);
      st_flag(wsr, vfo, FB_OFF, tag);
      lds_wait(lds + C_FA + wid * 4);
    }
    asm volatile("" ::: "memory");
    STAMP(7);
    bf16x8 hf[4][4];
#pragma unroll
    for (int k = 0; k < 4; k++)
#pragma unroll
      for (int m = 0; m < 4; m++) hf[k][m] = ldh(gstep(k), m);
    if (early) { WAITV(16); st_flag(wsr, vfo, FB_OFF, tag); }
    STAMP(6);
#pragma unroll
    for (int c = 0; c < 4; c++)
#pragma unroll
      for (int m = 0; m < 4; m++) acc2[c][m] = (f32x4){0.f, 0.f, 0.f, 0.f};
    LAS char* pw[4];
#pragma unroll
    for (int c = 0; c < 4; c++) { pw[c] = lds + wid * 12288 + c * 1024 + g * 256 + ((n ^ (4 * c + g)) << 4); PIN(pw[c]); }
    SCHED();
    u64 fbv = 0;
#pragma unroll
    for (int k = 0; k < 14; k++) {
      if (k == 2) fbv = ld_flag(wsr, vfi, FB_OFF);
      if (k == 3) { if (!__all(fbv == tag)) poll_flags(wsr, vfi, FB_OFF, tag); asm volatile("" ::: "memory"); STAMP(12); }
      if (k == 5) lds_wait(lds + C_F2 + wid * 4);
      if (k == 8) lds_wait(lds + C_F2 + wid * 4, 2u);
      if (k == 9) lds_set(lds + C_F3 + wid * 4);
      if (k == 7) ld_final();
      if (k == 11) prep_final();
#pragma unroll
      for (int m = 0; m < 4; m++) {
        if (k + 1 >= 5 && k + 1 < 14) wf[(k + 1) % 6][m] = ldw(k + 1, m);
#pragma unroll
        for (int c = 0; c < 4; c++) acc2[c][m] = __builtin_amdgcn_mfma_f32_16x16x32_bf16(wf[k % 6][c], hf[k & 3][m], acc2[c][m], 0, 0, 0);
        if (k + 4 < 14) hf[k & 3][m] = ldh(gstep(k + 4), m);
        if (k == 13 && m > 0) {
#pragma unroll
          for (int c = 0; c < 4; c++) *(LAS f32x4*)(pw[c] + wslot(m - 1)) = acc2[c][m - 1];
        }
        SCHED();
      }
    }
    STAMP(8);
#pragma unroll
    for (int c = 0; c < 4; c++) *(LAS f32x4*)(pw[c] + wslot(3)) = acc2[c][3];
  }
  BARRIER();
  STAMP(9);
  {
    const rsrc_t orc = mkrsrc(p.out, L * D * 4);
#pragma unroll
    for (int u = 0; u < 2; u++) {
      f32x4 b = *(LAS f32x4*)sb[u], a = sk[u] + *(LAS f32x4*)sa[u];
#pragma unroll
      for (int w = 1; w < 4; w++) {
        b += *(LAS f32x4*)(sb[u] + w * 12288);
        a += *(LAS f32x4*)(sa[u] + w * 12288);
      }
      const f32x4 o = gv[u] * a + omg[u] * b;
      __builtin_amdgcn_raw_buffer_store_b128(__builtin_bit_cast(u32x4, o), orc, vo + u * 8192, 0, OAUX);
    }
  }
  STAMP(10);
}

extern "C" void kernel_launch(void* const* d_in, const int* in_sizes, int n_in, void* d_out, int out_size, void* d_ws,
                              size_t ws_size, hipStream_t stream) {
  Params p;
  p.x = (const u16*)d_in[0]; p.lre = (const u16*)d_in[1]; p.lim = (const u16*)d_in[2];
  p.bre = (const u16*)d_in[3]; p.bim = (const u16*)d_in[4]; p.cre = (const u16*)d_in[5]; p.cim = (const u16*)d_in[6];
  p.ldt = (const u16*)d_in[7]; p.dsk = (const u16*)d_in[8];
  p.wg = (const u16*)d_in[10]; p.wu = (const u16*)d_in[11]; p.wd = (const u16*)d_in[12]; p.gw = (const u16*)d_in[13];
  p.ws = (char*)d_ws; p.out = (float*)d_out;
  (void)hipFuncSetAttribute((const void*)k_fused, hipFuncAttributeMaxDynamicSharedMemorySize, LDS_BYTES);
  k_fused<<<256, 512, LDS_BYTES, stream>>>(p);
}
```

```cpp
#include <hip/hip_runtime.h>
#include <stdint.h>

typedef unsigned short u16;
typedef unsigned long long u64;
typedef __bf16 bf16x8 __attribute__((ext_vector_type(8)));
typedef __bf16 bf16x4 __attribute__((ext_vector_type(4)));
typedef __bf16 bf16x2 __attribute__((ext_vector_type(2)));
typedef float f32x4 __attribute__((ext_vector_type(4)));
typedef float f32x2 __attribute__((ext_vector_type(2)));
typedef unsigned u32x4 __attribute__((ext_vector_type(4)));
typedef unsigned u32x2 __attribute__((ext_vector_type(2)));
#define LAS __attribute__((address_space(3)))
#define GAS __attribute__((address_space(1)))
#define RLX_AGENT __ATOMIC_RELAXED, __HIP_MEMORY_SCOPE_AGENT
typedef GAS unsigned gu32;
typedef GAS u64 gu64;
typedef __amdgpu_buffer_rsrc_t rsrc_t;

constexpr int L = 2048, D = 512, P = 512, F = 1792;
constexpr int KS2 = 88;
constexpr int SLOT = 53248;
constexpr int LDS_CTL = 3 * SLOT;
constexpr int LDS_BYTES = 163840;
constexpr int SC1 = 16;
constexpr size_t EPOCH_OFF = 0;
constexpr size_t G_OFF = 65536;
constexpr size_t E_OFF = 131072, E_BYTES = 16 * 512 * 16;
#define FS 128
constexpr unsigned FA_OFF = 262144, FB_OFF = FA_OFF + 1024 * FS, FC_OFF = FB_OFF + 1024 * FS;
constexpr size_t CT_OFF = 1u << 20, CT_BYTES = 1u << 20;
constexpr int HM = KS2 * 1024;
constexpr size_t H_OFF = CT_OFF + CT_BYTES, H_BYTES = (size_t)128 * HM;
constexpr u64 TAG_STEP = 0x9E3779B97F4A7C15ull;
#define OAUX 18
#define PSL 2
#define MAP2 1
#define ESL 4
#define DMAUX ""
#define PWL 1
#define NOG 0
#define PREB 1
#define HOLD 0
#define NA 3
#define NB 4
#define ASKD 8
#define PUBA 3
#define S1A 2
#define S2A 6
#define S3A 9
#define CHKB 3
constexpr int CODE_KB = 36;
constexpr unsigned OOR = 0x40000000u;
constexpr unsigned SPIN_CAP = 1u << 18;

#define BARRIER() asm volatile("s_waitcnt lgkmcnt(0)\n\ts_barrier" ::: "memory")
#define WAITV(n) asm volatile("s_waitcnt vmcnt(%0)" ::"i"(n) : "memory")
#define WAITL0() asm volatile("s_waitcnt lgkmcnt(0)" ::: "memory")
#define SCHED() __builtin_amdgcn_sched_barrier(0)
#define PIN(x) asm volatile("" : "+v"(x))

#define STAMP0() do {} while (0)
#define STAMP(id) do {} while (0)
#define LST(id) do {} while (0)

__device__ __forceinline__ float bf2f(u16 v) { return __uint_as_float(((unsigned)v) << 16); }
__device__ __forceinline__ float rbf(float f) { return (float)(__bf16)f; }
__device__ __forceinline__ unsigned pack2(float a, float b) {
  bf16x2 v = {(__bf16)a, (__bf16)b};
  return __builtin_bit_cast(unsigned, v);
}
__device__ __forceinline__ rsrc_t mkrsrc(const void* p, unsigned bytes) {
  return __builtin_amdgcn_make_buffer_rsrc((void*)p, 0, bytes, 0x00020000);
}
__device__ __forceinline__ u32x4 mkdesc(const void* p, unsigned bytes) {
  const u64 a = (u64)p;
  return (u32x4){(unsigned)a, (unsigned)(a >> 32) & 0xffffu, bytes, 0x00020000u};
}
__device__ __forceinline__ void dma16(u32x4 d, unsigned lds_dst, unsigned voff, unsigned soff) {
  asm volatile("s_mov_b32 m0, %2\n\ts_nop 3\n\tbuffer_load_dwordx4 %0, %1, %3 offen" DMAUX " lds" ::"v"(voff), "s"(d), "s"(lds_dst), "s"(soff) : "memory");
}
__device__ __forceinline__ u32x4 ld16_async(u32x4 d, unsigned voff, unsigned soff) {
  u32x4 r;
  asm volatile("s_nop 4\n\tbuffer_load_dwordx4 %0, %1, %2, %3 offen sc1" : "=v"(r) : "v"(voff), "s"(d), "s"(soff) : "memory");
  return r;
}
__device__ __forceinline__ u64 ld8_async(u32x4 d, unsigned voff) {
  u64 r;
  asm volatile("s_nop 4\n\tbuffer_load_dwordx2 %0, %1, %2, 0 offen sc1" : "=v"(r) : "v"(voff), "s"(d) : "memory");
  return r;
}
__device__ __forceinline__ unsigned ld2_async(const void* base, unsigned voff) {
  unsigned r;
  asm volatile("s_nop 4\n\tglobal_load_ushort %0, %1, %2" : "=v"(r) : "v"(voff), "s"(base) : "memory");
  return r;
}
__device__ __forceinline__ void st16_async(u32x4 d, unsigned voff, u32x4 v) {
  asm volatile("s_nop 4\n\tbuffer_store_dwordx4 %0, %1, %2, 0 offen sc1\n\ts_nop 1" ::"v"(v), "v"(voff), "s"(d) : "memory");
}
__device__ __forceinline__ void lds_set(LAS char* p, unsigned v = 1u) { __hip_atomic_store((LAS unsigned*)p, v, __ATOMIC_RELAXED, __HIP_MEMORY_SCOPE_WORKGROUP); }
__device__ __forceinline__ void touch(u32x4 d, unsigned lds_dst, unsigned voff, unsigned soff) {
  asm volatile("s_mov_b32 m0, %2\n\ts_nop 3\n\tbuffer_load_dword %0, %1, %3 offen lds" ::"v"(voff), "s"(d), "s"(lds_dst), "s"(soff) : "memory");
}
__device__ __forceinline__ void lds_wait(LAS char* p, unsigned want = 1u) {
#pragma unroll 1
  for (unsigned spins = 0; spins < SPIN_CAP * 8; spins++) {
    if ((unsigned)__builtin_amdgcn_readfirstlane(__hip_atomic_load((LAS unsigned*)p, __ATOMIC_RELAXED, __HIP_MEMORY_SCOPE_WORKGROUP)) >= want) break;
    __builtin_amdgcn_s_sleep(1);
  }
  asm volatile("" ::: "memory");
}
__device__ __forceinline__ bf16x8 ld128(LAS char* p) { return *(LAS bf16x8*)p; }
__device__ __forceinline__ bf16x8 ldtr(LAS char* p0, LAS char* p1) {
  bf16x4 a = __builtin_amdgcn_ds_read_tr16_b64_v4bf16((LAS bf16x4*)p0);
  bf16x4 b = __builtin_amdgcn_ds_read_tr16_b64_v4bf16((LAS bf16x4*)p1);
  return __builtin_shufflevector(a, b, 0, 1, 2, 3, 4, 5, 6, 7);
}
__device__ __forceinline__ float sigmoidf_(float g) {
  return __builtin_amdgcn_rcpf(1.0f + __builtin_amdgcn_exp2f(-1.4426950408889634f * g));
}
__device__ __forceinline__ f32x2 fma2(f32x2 a, f32x2 b, f32x2 c) { return __builtin_elementwise_fma(a, b, c); }
__device__ __forceinline__ f32x2 lo2(f32x4 v) { return __builtin_shufflevector(v, v, 0, 1); }
__device__ __forceinline__ f32x2 hi2(f32x4 v) { return __builtin_shufflevector(v, v, 2, 3); }
__device__ __forceinline__ void csq(float& r, float& i) { const float t = r * r - i * i; i = 2.f * r * i; r = t; }

struct Params {
  const u16 *x, *wg, *wu, *bre, *bim, *ldt, *lre, *lim, *cre, *cim, *dsk, *wd, *gw;
  char* ws;
  float* out;
};

__device__ __forceinline__ void ssm_consts(u16 ldt, u16 lre, u16 lim, float& lr, float& li, float& cr, float& ci) {
  const float dt = rbf(expf(bf2f(ldt)));
  const float a = bf2f(lre), b = bf2f(lim);
  const float m = expf(dt * a);
  float s, c;
  sincosf(dt * b, &s, &c);
  lr = m * c;
  li = m * s;
  const float nr = lr - 1.0f, ni = li, rden = 1.0f / (a * a + b * b);
  cr = (nr * a + ni * b) * rden;
  ci = (ni * a - nr * b) * rden;
}
__device__ __forceinline__ void share2(float v, float (&o)[2]) {
  const auto t = __builtin_amdgcn_permlane16_swap(__float_as_uint(v), __float_as_uint(v), false, false);
  o[0] = __uint_as_float(t[0]); o[1] = __uint_as_float(t[1]);
}

__device__ __forceinline__ u64 ld_flag(rsrc_t r, unsigned voff, unsigned soff) {
  return __builtin_bit_cast(u64, __builtin_amdgcn_raw_buffer_load_b64(r, voff, soff, SC1));
}
__device__ __forceinline__ void st_flag(rsrc_t r, unsigned voff, unsigned soff, u64 tag) {
  __builtin_amdgcn_raw_buffer_store_b64(__builtin_bit_cast(u32x2, tag), r, voff, soff, SC1);
}
__device__ __forceinline__ void poll_flags(rsrc_t r, unsigned voff, unsigned soff, u64 tag) {
#pragma unroll 1
  for (unsigned spins = 0; spins < SPIN_CAP; spins++) {
    asm volatile("" ::: "memory");
    if (__all(ld_flag(r, voff, soff) == tag)) break;
    __builtin_amdgcn_s_sleep(PSL);
  }
  asm volatile("" ::: "memory");
}

template <bool GLU, int NR>
__device__ __forceinline__ void main_loop(LAS char* lds, int wr, int wc, int lane, f32x4 (&acc)[8][NR]) {
  const int i = lane & 15, g = lane >> 4;
  const int sw = (i >> 1) & 7;
  LAS char *resA[2], *rolA[4];
  if constexpr (GLU) {
    resA[0] = lds + (wr * 80 + i) * 128 + ((g ^ sw) << 4);
    resA[1] = lds + (wr * 80 + i) * 128 + ((g ^ sw ^ 4) << 4);
    const int q = (lane & 15) >> 2, pp = lane & 3;
    const int swz = q | ((g & 1) << 2);
#pragma unroll
    for (int c = 0; c < 4; c++) rolA[c] = lds + 20480 + (8 * g + q) * 256 + (((wc * 4 + c) ^ swz) << 5) + pp * 8;
  } else {
    resA[0] = lds + 32768 + (wc * 32 + i) * 128 + ((g ^ sw) << 4);
    resA[1] = lds + 32768 + (wc * 32 + i) * 128 + ((g ^ sw ^ 4) << 4);
    rolA[0] = lds + (wr * 128 + i) * 128 + ((g ^ sw) << 4);
    rolA[1] = lds + (wr * 128 + i) * 128 + ((g ^ sw ^ 4) << 4);
    rolA[2] = rolA[3] = lds;
  }
  auto ld_res = [&](int ks, int r) -> bf16x8 {
    if constexpr (GLU) return ld128(resA[ks] + r * 2048);
    else return ld128(resA[ks] + (r & 1) * 2048 + (r >> 1) * 8192);
  };
  auto ld_rol = [&](int j) -> bf16x8 {
    const int ks = j >> 3, c = j & 7;
    if constexpr (GLU) {
      LAS char* b = rolA[c & 3] + (c >> 2) * 16384 + ks * 8192;
      return ldtr(b, b + 1024);
    } else {
      return ld128(rolA[ks] + c * 2048);
    }
  };
#pragma unroll
  for (int c = 0; c < 8; c++)
#pragma unroll
    for (int r = 0; r < NR; r++) { acc[c][r] = (f32x4){0.f, 0.f, 0.f, 0.f}; PIN(acc[c][r]); }
  bf16x8 res[2][NR], rol[5];
  WAITV(0);
  BARRIER();
  rol[0] = ld_rol(0); SCHED(); res[0][0] = ld_res(0, 0); SCHED(); res[0][1] = ld_res(0, 1); SCHED();
  rol[1] = ld_rol(1); SCHED(); res[0][2] = ld_res(0, 2); SCHED(); res[0][3] = ld_res(0, 3); SCHED();
  rol[2] = ld_rol(2); SCHED();
  if constexpr (NR > 4) res[0][NR - 1] = ld_res(0, NR - 1);
  SCHED();
  int slot = 0;
#pragma unroll 1
  for (unsigned s = 0; s < 8; s++) {
#pragma unroll
    for (int j = 0; j < 16; j++) {
      int nds = 0;
      constexpr int RD = GLU ? 2 : 1;
      if (j == 12) {
        const int delta = (slot == 2) ? -2 * SLOT : SLOT;
        slot = (slot == 2) ? 0 : slot + 1;
        resA[0] += delta; resA[1] += delta;
        rolA[0] += delta; rolA[1] += delta;
        PIN(resA[0]); PIN(resA[1]); PIN(rolA[0]); PIN(rolA[1]);
        if constexpr (GLU) { rolA[2] += delta; rolA[3] += delta; PIN(rolA[2]); PIN(rolA[3]); }
      }
      if (j == 13) {
        BARRIER();
        SCHED();
      }
      if (j != 12) { rol[(j + 3) & 3] = ld_rol((j + 3) & 15); nds += RD; }
      if (j == 10) { rol[4] = ld_rol(15); nds += RD; }
      if (j >= 2 && j < 2 + NR) { res[1][j - 2] = ld_res(1, j - 2); nds++; }
      if (j == 13) { res[0][0] = ld_res(0, 0); res[0][1] = ld_res(0, 1); nds += 2; }
      if (j == 14) { res[0][2] = ld_res(0, 2); res[0][3] = ld_res(0, 3); nds += 2; }
      if (j == 15 && NR > 4) { res[0][NR - 1] = ld_res(0, NR - 1); nds++; }
#pragma unroll
      for (int r = 0; r < NR; r++)
        acc[j & 7][r] = __builtin_amdgcn_mfma_f32_16x16x32_bf16(rol[j == 15 ? 4 : (j & 3)], res[j >> 3][r], acc[j & 7][r], 0, 0, 0);
#pragma unroll
      for (int r = 0; r < NR; r++) {
        __builtin_amdgcn_sched_group_barrier(0x8, 1, 0);
        if (j == 12) __builtin_amdgcn_sched_group_barrier(0x2, 2, 0);
        if (r < nds) __builtin_amdgcn_sched_group_barrier(0x100, 1, 0);
      }
      SCHED();
    }
  }
}

__device__ __forceinline__ constexpr int wslot(int i) { return (i / 3 == 0 ? 2 : (i / 3 == 1 ? 0 : 1)) * SLOT + (i % 3) * 4096; }
__device__ __forceinline__ constexpr int gstep(int i) { return i < 7 ? 2 * i : 2 * i - 13; }
constexpr int C_TAG = LDS_CTL, C_F0 = LDS_CTL + 16, C_F1 = LDS_CTL + 32, C_F2 = LDS_CTL + 48, C_F3 = LDS_CTL + 64, C_FA = LDS_CTL + 80;
constexpr int C_DUMP = LDS_CTL + 1024;

__global__ __launch_bounds__(512, 2) void k_fused(Params p) {
  extern __shared__ __attribute__((aligned(16))) char smem[];
  LAS char* lds = (LAS char*)smem;
  const int tid = threadIdx.x, lane = tid & 63;
  STAMP0();
  const int wid = __builtin_amdgcn_readfirstlane(tid >> 6);
  const int xg = blockIdx.x & 7, idx = blockIdx.x >> 3;
  const int hh = xg & 1, cj = xg >> 1, odd = cj & 1;
  const int ncol = 4 - odd, col0 = (cj >> 1) * 7 + odd * 4;
  const int e_ = idx - 4 - odd * 8, ec = (e_ * 43) >> 8, em = e_ - ncol * 6, es = em - (hh ? ncol - 2 : 2);
  const bool ssm = e_ < 0;
  const int role = ssm ? 1 : (es < 0 ? 0 : 2 + odd);
  const int rt = ssm ? hh * 4 + (idx & 3) : (em < 0 ? hh * 7 + e_ - ec * 6 : (es < 0 ? 6 : (cj + hh) * 2 + es));
  const int ct = ssm ? (cj >> 1) * 4 + odd + (idx >> 2) : col0 + (em < 0 ? ec : hh * 2 + em);
  const int w4 = wid & 3;
  const int n = lane & 15, g = lane >> 4;
  const int k0 = (2 * cj + 4 * hh) & 7, gam = (rt + ct) & 1;
  const unsigned okb = role < 2 ? ~0u : 0u;
  const u32x4 xr = mkdesc(p.x, L * D * 2 & okb);
  const u32x4 w0r = mkdesc(ssm ? p.bre : p.wg, (ssm ? P * D * 2 : D * F * 2) & okb), w1r = mkdesc(ssm ? p.bim : p.wu, (ssm ? P * D * 2 : D * F * 2) & okb);
  unsigned vx, vw;
  {
    const int r8 = 8 * w4 + (lane >> 3), ii = r8 & 15;
    const unsigned csw = ((lane & 7) ^ (ii >> 1)) << 4;
    const int xrow = ssm ? rt * 256 + 32 * (ii >> 2) + 16 * (ii & 1) + ((ii >> 1) & 1) + (w4 & 2) : min(rt * 160, L - 160) + r8;
    vx = xrow * 1024 + csw;
    vw = ssm ? (ct * 64 + r8) * 1024 + csw : (4 * w4 + g) * (F * 2) + ct * 256 + ((n ^ ((g | ((w4 & 2) << 1)) << 1)) << 4);
  }
  const unsigned stx = ssm ? 4096 : 32768, stw = ssm ? 32768 : 16 * F * 2, sgw = ssm ? 128 : 64 * F * 2;
  const unsigned lw0 = ssm ? 32768 : 20480, lw1 = ssm ? 40960 : 36864;
  auto pieces = [&](int s, int a, int b) {
    const unsigned sg = ((s ^ gam) + k0) & 7, lb = (s % 3) * SLOT + w4 * 1024;
    auto px = [&](int i) { dma16(xr, lb + i * 4096, vx, sg * 128 + (i >> 2) * 131072 + (i & 3) * stx); };
    auto pw = [&](int k) { dma16((k & 1) ? w1r : w0r, lb + ((k & 1) ? lw1 : lw0) + (k >> 1) * 4096, vw, sg * sgw + (k >> 1) * stw); };
#pragma unroll
    for (int k = a; k < (b < 9 ? b : 9); k++) { if (k < 5) px(k); else pw(k - 5); }
    if (b > 9) {
      if (ssm) { px(5); px(6); px(7); }
      else { pw(4); pw(5); pw(6); pw(7); }
    }
  };
  unsigned c_ldt, c_lre, c_lim;
  const int chg = (ct * 64 + (wid & 1) * 32 + (lane & 31)) & (P - 1);
  if (wid < 4) {
    if (ssm) { c_ldt = ld2_async(p.ldt, chg * 2); c_lre = ld2_async(p.lre, chg * 2); c_lim = ld2_async(p.lim, chg * 2); }
    pieces(0, 0, 6);
  } else {
    pieces(0, 6, 10);
  }
  const int myid = role == 0 ? rt * 14 + ct : (ssm ? 182 + rt * 8 + ct : (role == 2 ? 246 + rt : 254 + (cj >> 1)));
  const int rb = MAP2 ? (xg >> 1) * 8 + (idx >> 2) : xg * 4 + (idx >> 3), cb = MAP2 ? (xg & 1) * 4 + (idx & 3) : idx & 7;
  const rsrc_t hr = mkrsrc(p.ws + H_OFF, (unsigned)H_BYTES), wsr = mkrsrc(p.ws, (unsigned)H_OFF);
  f32x4 acc2[4][4];
  u64 tag;
  unsigned vh[4];
  auto ldh = [&](int k, int m) -> bf16x8 {
    return __builtin_bit_cast(bf16x8, __builtin_amdgcn_raw_buffer_load_b128(hr, vh[m] + (k & 3) * 1024, (k & ~3) * 1024, SC1));
  };
  auto set_vh = [&]() { vh[1] = vh[0] + HM; vh[2] = vh[0] + 2 * HM; vh[3] = vh[0] + 3 * HM; PIN(vh[1]); PIN(vh[2]); PIN(vh[3]); };
  const unsigned gflag = (246 * 4 + rb) * FS;
  auto glu_flags = [&]() -> unsigned {
    const int l14 = lane < 14 ? lane : 0, j = l14 >= 7 ? l14 - 7 : l14, pr = 7 * w4 + j;
    const int mt = rb * 4 + (l14 >= 7 ? 3 : 0), om = mt < 120 ? mt / 5 : (mt < 123 ? 24 : 25);
    return lane == 14 ? gflag : (((om >> 1) * 14 + (pr >> 1)) * 4 + (om & 1) * 2 + (pr & 1)) * FS;
  };
  float gv[2];
  u32x2 xv[2], dv;
  auto ld_final = [&]() {
    int ln = lane;
    PIN(ln);
    const int t = rb * 64 + wid * 8 + (ln >> 4), dcol = cb * 64 + (ln & 15) * 4;
    const rsrc_t xr_ = mkrsrc(p.x, L * D * 2);
#pragma unroll
    for (int u = 0; u < 2; u++) {
      gv[u] = __uint_as_float(__builtin_amdgcn_raw_buffer_load_b32(wsr, t * 4 + u * 16, (unsigned)G_OFF, SC1));
      xv[u] = __builtin_amdgcn_raw_buffer_load_b64(xr_, t * 1024 + dcol * 2, u * 4096, 0);
    }
    dv = *(const u32x2*)(p.dsk + dcol);
  };

  LAS char *sb[2], *sa[2];
  f32x4 sk[2];
  float omg[2];
  unsigned vo;
  auto prep_final = [&]() {
    int ln = lane;
    PIN(ln);
    const int nn = ln & 15, mt = wid >> 1;
    const int ob = mt < 3 ? wslot(0) + mt * 4096 : wslot(3), oa = mt == 0 ? wslot(5) : wslot(6) + (mt - 1) * 4096;
    vo = ((rb * 64 + wid * 8 + (ln >> 4)) * D + cb * 64 + nn * 4) * 4;
#pragma unroll
    for (int u = 0; u < 2; u++) {
      const int sl = ((wid & 1) * 8 + u * 4 + (ln >> 4)) ^ nn;
      sb[u] = lds + ob + nn * 256 + (sl << 4);
      sa[u] = lds + oa + nn * 256 + ((sl ^ (4 * mt)) << 4);
      omg[u] = rbf(1.0f - gv[u]);
#pragma unroll
      for (int e = 0; e < 4; e++) {
        const unsigned xw = xv[u][e >> 1], dw = dv[e >> 1];
        const float xf = __uint_as_float((e & 1) ? (xw & 0xffff0000u) : (xw << 16));
        const float df = __uint_as_float((e & 1) ? (dw & 0xffff0000u) : (dw << 16));
        sk[u][e] = rbf(xf * df);
      }
      PIN(sb[u]); PIN(sa[u]); PIN(omg[u]); PIN(sk[u]);
    }
    PIN(vo);
  };

  if (wid >= 4) {
    const int lw = w4;
    const u32x4 wsd = mkdesc(p.ws, (unsigned)H_OFF);
    const u32x4 wdr = mkdesc(p.wd, F * D * 2);
    const unsigned vwd = (lw * 14 * 32 + (lane >> 3)) * 1024 + cb * 128 + (((lane & 7) ^ (((lane >> 4) & 3) << 1)) << 4);
    u32x4 ctv;
    u64 cfl;
    auto ct_load = [&]() {
      const int b = blockIdx.x;
      ctv = ld16_async(mkdesc((b & 1) ? p.cim : p.cre, D * P * 2),
                       ((b >> 5) * 64 + ((b >> 1) & 3) * 16 + n) * 1024 + ((b >> 3) & 3) * 256 + lw * 64 + g * 16, 0);
    };
    auto ct_store = [&]() { st16_async(wsd, (unsigned)CT_OFF + (blockIdx.x * 4 + lw) * 1024 + lane * 16, ctv); };
    auto ct_flag = [&]() { st_flag(wsr, lane == 0 ? (blockIdx.x * 4 + lw) * FS : OOR, FC_OFF, tag); };
    const unsigned cfo = FC_OFF + ((cb * 4 + lw) * 32 + (lane & 31)) * FS;
    auto ct_ask = [&]() { cfl = ld8_async(wsd, cfo); };
    auto ct_check = [&]() { PIN(cfl); if (!__all(cfl == tag)) poll_flags(wsr, cfo, 0, tag); };
    u32x4 cf[32];
    const unsigned vcf = (cb * 4 + lw) * 32768 + lane * 16;
    auto ldc = [&](int a, int b) {
#pragma unroll
      for (int f = a; f < b; f++) cf[f] = ld16_async(wsd, vcf, (unsigned)CT_OFF + f * 1024);
    };
    auto wpiece = [&](int i, int k) { dma16(wdr, wslot(i < 9 ? i : i - 9) + lw * 12288 + k * 1024, vwd, (gstep(i) * 32 + k * 8) * 1024); };
    auto wround = [&](int r) {
#pragma unroll
      for (int q = 0; q < 12; q++) wpiece(r * 3 + (q >> 2), q & 3);
    };
    const u32x4 gr = mkdesc(p.wg, D * F * 2), ur = mkdesc(p.wu, D * F * 2);
    auto pre_1 = [&]() {
      const unsigned v = (((k0 + 2) & 7) * 64 + idx * 4 + lw) * (F * 2) + col0 * 256 + (lane < ncol * 16 ? lane : 0) * 16;
      dma16(gr, C_DUMP, v, 0);
      dma16(ur, C_DUMP, v, 0);
    };
    auto pre_w = [&]() { touch(wdr, C_DUMP, (lane < 14 ? lane : 0) * 128, (blockIdx.x * 4 + lw) * 1792); };
    constexpr int NP = 12, ALL = 10;
    if (role < 2) {
      if (PREB && ssm) {
        const unsigned v = (ct * 64 + (idx & 3) * 16 + lw * 4 + g) * 1024 + ((k0 + 2) & 7) * 128 + n * 16;
        dma16(w0r, C_DUMP, v, 0);
        dma16(w1r, C_DUMP, v, 0);
        ct_load(); pre_1(); pieces(1, 0, S1A); LST(0); WAITV(5 + S1A);
      } else {
        ct_load(); pre_1(); pieces(1, 0, S1A); LST(0); WAITV(3 + S1A);
      }
      LST(1); BARRIER(); LST(2);
      tag = *(LAS u64*)(lds + C_TAG);
      pieces(1, S1A, ALL); pieces(2, 0, S2A); LST(3); WAITV(S2A); LST(4); BARRIER(); LST(5);
      ct_store(); pieces(2, S2A, ALL); pieces(3, 0, S3A); LST(6); WAITV(S3A); ct_flag(); LST(7); BARRIER(); LST(8);
      pieces(3, S3A, ALL);
      pieces(4, 0, ALL); LST(9); WAITV(NP); LST(10); BARRIER(); LST(11);
      if (!ssm) ct_ask();
      pieces(5, 0, ALL); LST(12); WAITV(NP);
      if (!ssm) { ct_check(); ldc(0, NA); }
      pre_w();
      LST(13); BARRIER(); LST(14);
      pieces(6, 0, ALL); LST(15);
      if (!ssm) { ldc(NA, 2 * NA); WAITV(NP + 2 + 2 * NA); } else WAITV(NP + 1);
      LST(16); BARRIER(); LST(17);
      if (ssm) ct_ask();
      pieces(7, 0, ALL); LST(18);
      if (!ssm) { ldc(2 * NA, 3 * NA); WAITV(NP + 1 + 2 * NA); } else WAITV(NP);
      LST(19); BARRIER(); LST(20);
      if (ssm) ct_check();
      wround(0); ldc(3 * NA, 3 * NA + NB); LST(21); WAITV(12 + NB); LST(22); BARRIER(); LST(23);
      wround(1); ldc(3 * NA + NB, 3 * NA + 2 * NB); LST(25); BARRIER(); LST(26);
      if (ssm) ldc(0, 3 * NA);
      WAITV(0);
    } else {
      ct_load();
      BARRIER();
      tag = *(LAS u64*)(lds + C_TAG);
      WAITV(0); ct_store(); WAITV(0); ct_flag();
      pre_1(); pre_w();
      wround(0); wround(1);
      poll_flags(wsr, cfo, 0, tag);
      ldc(0, 3 * NA + 2 * NB);
      WAITV(0);
      if (myid == 255 && lw == 3) {
#pragma unroll 1
        for (int k = 0; k < 4; k++) poll_flags(wsr, (k * 64 + lane) * 4 * FS, FC_OFF, tag);
        st_flag(wsr, lane == 0 ? (unsigned)EPOCH_OFF : OOR, 0, tag);
      }
    }
    lds_set(lds + C_F0 + lw * 4);
    STAMP(1);
    if (HOLD && role == 0) lds_wait(lds + C_FA + lw * 4);
    wround(2);
    ldc(3 * NA + 2 * NB, 32);
    const unsigned fs = FB_OFF + (lane == 4 ? gflag : ((182 + (rb >> 2) * 8 + lw * 2 + ((lane >> 1) & 1)) * 4 + (rb & 2) + (lane & 1)) * FS);
    vh[0] = rb * 4 * HM + (56 + lw * 8) * 1024 + lane * 16;
    set_vh();
    LAS char* pb[4];
#pragma unroll
    for (int k = 0; k < 4; k++) {
      const int mt = n >> 2;
      pb[k] = lds + (mt == 0 ? wslot(5) : wslot(6) + (mt - 1) * 4096) + lw * 12288 + g * 256 + ((((n & 3) ^ g) | ((k ^ mt) << 2)) << 4);
      PIN(pb[k]);
    }
    STAMP(2);
    lds_wait(lds + C_F1 + lw * 4);
    STAMP(3);
#pragma unroll
    for (int q = 0; q < 20; q++) {
      if (q == 20 - ASKD) cfl = ld8_async(wsd, fs);
      wpiece(9 + (q >> 2), q & 3);
    }
    WAITV(ASKD);
    lds_set(lds + C_F2 + lw * 4);
    STAMP(6);
    PIN(cfl);
    if (!__all(cfl == tag)) { WAITV(0); lds_set(lds + C_F2 + lw * 4, 2u); poll_flags(wsr, fs, 0, tag); }
    asm volatile("" ::: "memory");
    STAMP(7);
    bf16x8 hf[3][4];
#pragma unroll
    for (int k = 0; k < 3; k++)
#pragma unroll
      for (int m = 0; m < 4; m++) hf[k][m] = ldh(k, m);
    WAITV(12);
    lds_set(lds + C_F2 + lw * 4, 2u);
#pragma unroll
    for (int f = 0; f < 32; f++) PIN(cf[f]);
#pragma unroll
    for (int c = 0; c < 4; c++)
#pragma unroll
      for (int m = 0; m < 4; m++) acc2[c][m] = (f32x4){0.f, 0.f, 0.f, 0.f};
    SCHED();
#pragma unroll
    for (int k = 0; k < 8; k++) {
      if (k == 3) ld_final();
      if (k == 6) prep_final();
      if (k == 7) lds_wait(lds + C_F3 + lw * 4);
#pragma unroll
      for (int m = 0; m < 4; m++) {
#pragma unroll
        for (int c = 0; c < 4; c++)
          acc2[c][m] = __builtin_amdgcn_mfma_f32_16x16x32_bf16(__builtin_bit_cast(bf16x8, cf[c * 8 + (k & 1) * 4 + (k >> 1)]), hf[k % 3][m], acc2[c][m], 0, 0, 0);
        if (k + 3 < 8) hf[k % 3][m] = ldh(k + 3, m);
        if (k == 7 && m > 0) {
#pragma unroll
          for (int c = 0; c < 4; c++) *(LAS f32x4*)(pb[(m - 1) ^ c] + c * 1024) = acc2[c][m - 1];
        }
        SCHED();
      }
    }
    STAMP(8);
#pragma unroll
    for (int c = 0; c < 4; c++) *(LAS f32x4*)(pb[3 ^ c] + c * 1024) = acc2[c][3];
  } else {
    const int wr = wid >> 1, wc = wid & 1;
    if (wid >= 2 && idx * 2 + (wid & 1) < CODE_KB) {
      const u32x4 cd = mkdesc((const void*)(__builtin_amdgcn_s_getpc() & ~255ull), CODE_KB * 1024);
      dma16(cd, C_DUMP, lane * 16, (idx * 2 + (wid & 1)) * 1024);
    }
    if (wid == 0) {
      u64 w0;
      asm volatile("s_load_dwordx2 %0, %1, 0x0\n\ts_waitcnt lgkmcnt(0)" : "=s"(w0) : "s"(p.ws + EPOCH_OFF) : "memory");
      *(LAS u64*)(lds + C_TAG + lane * 8) = lane == 0 ? w0 + TAG_STEP : 0ull;
    }
    unsigned vfo = lane == 0 ? (myid * 4 + wid) * FS : OOR;
    unsigned vfi = glu_flags();
    vh[0] = rb * 4 * HM + wid * 14 * 1024 + lane * 16;
    LAS char* ra0 = lds + wid * 12288 + (4 * g + (n >> 2)) * 128 + (((2 * g + (n >> 3)) & 3) << 5) + (n & 3) * 8;
    PIN(vfo); PIN(vfi); PIN(vh[0]); PIN(ra0);
    bf16x8 wf[6][4];
    LAS char* ra[3][4];
    auto ldw = [&](int i, int c) -> bf16x8 {
      const int k = i < 9 ? i : i - 9;
      LAS char* a = ra[k / 3][c] + (k % 3) * 4096;
      return ldtr(a, a + 2048);
    };
    auto grab_w = [&]() {
#pragma unroll
      for (int c = 0; c < 4; c++) {
        ra[1][c] = (LAS char*)((unsigned)(size_t)ra0 ^ (c << 5));
        ra[0][c] = ra[1][c] + 2 * SLOT;
        ra[2][c] = ra[1][c] + SLOT;
      }
      lds_wait(lds + C_F0 + wid * 4);
#pragma unroll
      for (int i = 0; i < 5; i++)
#pragma unroll
        for (int c = 0; c < 4; c++) wf[i][c] = ldw(i, c);
      asm volatile("" ::: "memory");
      lds_set(lds + C_F1 + wid * 4);
    };
    if (role == 0) {
      const int mt0 = (min(rt * 160, L - 160) >> 4) + wr * 5;
      unsigned vs[5];
#pragma unroll
      for (int m = 0; m < 5; m++) { vs[m] = mt0 + m < rt * 10 ? OOR : (mt0 + m) * HM + (ct * 4 + wc * 2) * 1024 + lane * 16; PIN(vs[m]); }
      f32x4 acc[8][5];
      STAMP(1);
      main_loop<true, 5>(lds, wr, wc, lane, acc);
      STAMP(2);
      tag = *(LAS u64*)(lds + C_TAG);
#pragma unroll
      for (int h = 0; h < 2; h++) {
        if (h == 1) { SCHED(); STAMP(13); grab_w(); STAMP(14); SCHED(); }
#pragma unroll
        for (int m = 0; m < 5; m++) {
          if (HOLD && HOLD <= 10 && h * 5 + m == HOLD) lds_set(lds + C_FA + wid * 4);
          if (h == 1 && m == PUBA) { WAITV(PUBA); st_flag(wsr, vfo, FA_OFF, tag); STAMP(3); }
          u32x4 o;
#pragma unroll
          for (int e = 0; e < 4; e++) {
            const int c = 2 * h + (e >> 1);
            const f32x2 gv_ = (e & 1) ? hi2(acc[c][m]) : lo2(acc[c][m]);
            const f32x2 uv = (e & 1) ? hi2(acc[4 + c][m]) : lo2(acc[4 + c][m]);
            f32x2 t = gv_ * -1.4426950408889634f;
            t = (f32x2){__builtin_amdgcn_exp2f(t.x), __builtin_amdgcn_exp2f(t.y)} + 1.0f;
            t = (f32x2){__builtin_amdgcn_rcpf(t.x), __builtin_amdgcn_rcpf(t.y)} * uv;
            o[e] = pack2(t.x, t.y);
          }
          __builtin_amdgcn_raw_buffer_store_b128(o, hr, vs[m] + h * 1024, 0, SC1);
        }
      }
    } else {
      if (role == 1) {
        float lr[2], li[2], cr[2], ci[2], sr[2], si[2], mr[2], mi[2], wr_[2], wi_[2], qr, qi, zr, zi;
        {
          float a2, a3;
          WAITV(6);
          PIN(c_ldt); PIN(c_lre); PIN(c_lim);
          ssm_consts(c_ldt, c_lre, c_lim, qr, qi, a2, a3);
          share2(qr, lr); share2(qi, li); share2(2.f * a2, cr); share2(2.f * a3, ci);
          csq(qr, qi); csq(qr, qi); csq(qr, qi); csq(qr, qi);
          share2(qr, sr); share2(qi, si);
          csq(qr, qi);
          share2(qr, mr); share2(qi, mi);
          csq(qr, qi); csq(qr, qi);
          zr = qr; zi = qi;
          csq(zr, zi); csq(zr, zi); csq(zr, zi);
          if (rt * 2 + wr <= 8) { zr = 0.f; zi = 0.f; }
#pragma unroll
          for (int u = 0; u < 2; u++) {
            float ar = mr[u], ai = mi[u];
            csq(ar, ai);
            const float br_ = (g & 1) ? mr[u] : 1.f, bi_ = (g & 1) ? mi[u] : 0.f;
            wr_[u] = (g & 2) ? ar * br_ - ai * bi_ : br_;
            wi_[u] = (g & 2) ? ar * bi_ + ai * br_ : bi_;
            PIN(mr[u]); PIN(mi[u]); PIN(lr[u]); PIN(li[u]); PIN(cr[u]); PIN(ci[u]); PIN(sr[u]); PIN(si[u]); PIN(wr_[u]); PIN(wi_[u]);
          }
          PIN(qr); PIN(qi); PIN(zr); PIN(zi);
        }
        const int ck = rt * 2 + wr;
        unsigned veo = lane < 32 ? (unsigned)E_OFF + (ck * 512 + chg) * 16 : OOR;
        const int c0 = ck - 1 - 8 * (lane >> 5);
        int vei = (c0 * 512 + chg) * 16, nval = min(max(c0 + 1, 0), 8);
        const unsigned tb = (wid >> 1) * 2 * SLOT + 49152 + (wid & 1) * 2048;
        LAS char* twp = lds + tb + n * 64 + ((g ^ (((n >> 1) & 1) | ((((n >> 2) ^ (n >> 3)) & 1) << 1))) << 4);
        LAS char* trp[2];
#pragma unroll
        for (int t = 0; t < 2; t++)
          trp[t] = lds + tb + (8 * g + (n >> 2)) * 64 + (((((n >> 1) ^ (n >> 3)) & 1) | (((g ^ t) & 1) << 1)) << 4) + (n & 1) * 8;
        unsigned vso[8];
#pragma unroll
        for (int k = 0; k < 8; k++) { vso[k] = (rt * 16 + wr * 8 + k) * HM + (56 + (ct * 2 + wc) * 2) * 1024 + lane * 16; PIN(vso[k]); }
        PIN(veo); PIN(vei); PIN(nval); PIN(twp); PIN(trp[0]); PIN(trp[1]);
        f32x4 acc[8][4];
        STAMP(1);
        main_loop<false, 4>(lds, wr, wc, lane, acc);
        STAMP(2);
        tag = *(LAS u64*)(lds + C_TAG);
        f32x2 er[2], ei[2];
#pragma unroll
        for (int u = 0; u < 2; u++) { er[u] = lo2(acc[0][u]); ei[u] = lo2(acc[0][2 + u]); }
#pragma unroll
        for (int st = 1; st < 16; st++)
#pragma unroll
          for (int u = 0; u < 2; u++) {
            const f32x2 l2 = {lr[u], lr[u]}, i2 = {li[u], li[u]};
            const f32x2 vr = (st & 1) ? hi2(acc[st >> 1][u]) : lo2(acc[st >> 1][u]);
            const f32x2 vi = (st & 1) ? hi2(acc[st >> 1][2 + u]) : lo2(acc[st >> 1][2 + u]);
            const f32x2 t = fma2(-i2, ei[u], fma2(l2, er[u], vr));
            ei[u] = fma2(i2, er[u], fma2(l2, ei[u], vi));
            er[u] = t;
          }
        float xs[2][4], ys[2][4];
#pragma unroll
        for (int u = 0; u < 2; u++) {
          const float a = fmaf(-si[u], ei[u].x, fmaf(sr[u], er[u].x, er[u].y)), b = fmaf(si[u], er[u].x, fmaf(sr[u], ei[u].x, ei[u].y));
          float a2[2], b2[2];
          share2(a, a2); share2(b, b2);
#pragma unroll
          for (int k = 0; k < 2; k++) {
            const auto ta = __builtin_amdgcn_permlane32_swap(__float_as_uint(a2[k]), __float_as_uint(a2[k]), false, false);
            const auto tb_ = __builtin_amdgcn_permlane32_swap(__float_as_uint(b2[k]), __float_as_uint(b2[k]), false, false);
            xs[u][k] = __uint_as_float(ta[0]); xs[u][2 + k] = __uint_as_float(ta[1]);
            ys[u][k] = __uint_as_float(tb_[0]); ys[u][2 + k] = __uint_as_float(tb_[1]);
          }
#pragma unroll
          for (int k = 1; k < 4; k++) {
            xs[u][k] = fmaf(-mi[u], ys[u][k - 1], fmaf(mr[u], xs[u][k - 1], xs[u][k]));
            ys[u][k] = fmaf(mi[u], xs[u][k - 1], fmaf(mr[u], ys[u][k - 1], ys[u][k]));
            PIN(xs[u][k]); PIN(ys[u][k]);
          }
        }
        const unsigned tg = (unsigned)tag;
        __builtin_amdgcn_raw_buffer_store_b128((u32x4){__float_as_uint(g ? xs[1][3] : xs[0][3]), tg, __float_as_uint(g ? ys[1][3] : ys[0][3]), tg},
                                               wsr, veo, 0, SC1);
        STAMP(3);
        SCHED();
        float pr[2], pi[2];
#pragma unroll
        for (int u = 0; u < 2; u++) {
          pr[u] = g == 0 ? 0.f : (g == 1 ? xs[u][0] : (g == 2 ? xs[u][1] : xs[u][2]));
          pi[u] = g == 0 ? 0.f : (g == 1 ? ys[u][0] : (g == 2 ? ys[u][1] : ys[u][2]));
          PIN(pr[u]); PIN(pi[u]);
        }
        const rsrc_t erc = mkrsrc(p.ws + E_OFF, (unsigned)E_BYTES);
        u32x4 v[8];
        auto ask = [&]() {
#pragma unroll
          for (int d = 0; d < 8; d++) v[d] = __builtin_amdgcn_raw_buffer_load_b128(erc, vei - d * 8192, 0, SC1);
        };
#pragma unroll
        for (int u = 0; u < 2; u++) {
          if (u == 1) { SCHED(); ask(); SCHED(); }
          PIN(cr[u]);
#pragma unroll
          for (int m = 0; m < 8; m++) {
            const f32x4 a = acc[m][u], b = acc[m][2 + u];
            acc[m][u] = cr[u] * a - ci[u] * b;
            acc[m][2 + u] = cr[u] * b + ci[u] * a;
            PIN(acc[m][u]); PIN(acc[m][2 + u]);
          }
        }
#pragma unroll 1
        for (unsigned spins = 0; spins < SPIN_CAP; spins++) {
          int cnt = 0;
#pragma unroll
          for (int d = 0; d < 8; d++) cnt += ((v[d][1] ^ tg) | (v[d][3] ^ tg)) == 0u;
          if (__all(cnt >= nval)) break;
          asm volatile("" ::: "memory");
          __builtin_amdgcn_s_sleep(ESL);
          ask();
        }
        float kr[2], ki[2];
        {
          float xr_ = __uint_as_float(v[7][0]), xi_ = __uint_as_float(v[7][2]);
#pragma unroll
          for (int d = 6; d >= 0; d--) {
            const float t = fmaf(-qi, xi_, fmaf(qr, xr_, __uint_as_float(v[d][0])));
            xi_ = fmaf(qi, xr_, fmaf(qr, xi_, __uint_as_float(v[d][2])));
            xr_ = t;
            PIN(xr_); PIN(xi_);
          }
          const auto ta = __builtin_amdgcn_permlane32_swap(__float_as_uint(xr_), __float_as_uint(xr_), false, false);
          const auto tb_ = __builtin_amdgcn_permlane32_swap(__float_as_uint(xi_), __float_as_uint(xi_), false, false);
          const float fr = __uint_as_float(ta[1]), fi = __uint_as_float(tb_[1]);
          share2(fmaf(-zi, fi, fmaf(zr, fr, __uint_as_float(ta[0]))), kr);
          share2(fmaf(zi, fr, fmaf(zr, fi, __uint_as_float(tb_[0]))), ki);
        }
        STAMP(4);
        f32x2 hr_[2], hi_[2];
#pragma unroll
        for (int u = 0; u < 2; u++) {
          f32x2 a, b;
          a.x = fmaf(-wi_[u], ki[u], fmaf(wr_[u], kr[u], pr[u])); b.x = fmaf(wi_[u], kr[u], fmaf(wr_[u], ki[u], pi[u]));
          a.y = fmaf(-si[u], b.x, fmaf(sr[u], a.x, er[u].x)); b.y = fmaf(si[u], a.x, fmaf(sr[u], b.x, ei[u].x));
          PIN(a); PIN(b);
          hr_[u] = cr[u] * a - ci[u] * b; hi_[u] = cr[u] * b + ci[u] * a;
        }
        bf16x8 tv[4];
#pragma unroll
        for (int qt = 0; qt < 5; qt++) {
          u32x4 o[2][2];
          if (qt < 4) {
#pragma unroll
            for (int mm = 0; mm < 2; mm++)
#pragma unroll
              for (int u = 0; u < 2; u++) {
                const int m = qt * 2 + mm;
                const f32x2 l2 = {lr[u], lr[u]}, i2 = {li[u], li[u]};
                const f32x2 r0_ = fma2(-i2, hi_[u], fma2(l2, hr_[u], lo2(acc[m][u])));
                const f32x2 i0_ = fma2(i2, hr_[u], fma2(l2, hi_[u], lo2(acc[m][2 + u])));
                hr_[u] = fma2(-i2, i0_, fma2(l2, r0_, hi2(acc[m][u])));
                hi_[u] = fma2(i2, r0_, fma2(l2, i0_, hi2(acc[m][2 + u])));
                o[0][u][mm] = pack2(r0_.x, hr_[u].x); o[0][u][2 + mm] = pack2(r0_.y, hr_[u].y);
                o[1][u][mm] = pack2(-i0_.x, -hi_[u].x); o[1][u][2 + mm] = pack2(-i0_.y, -hi_[u].y);
              }
          }
          SCHED();
          if (qt > 0) {
#pragma unroll
            for (int k = 0; k < 4; k++) __builtin_amdgcn_raw_buffer_store_b128(__builtin_bit_cast(u32x4, tv[k]), hr, vso[(k & 1) * 4 + qt - 1] + (k >> 1) * 1024, 0, SC1);
          }
          if (qt < 4) {
#pragma unroll
            for (int part = 0; part < 2; part++) {
              asm volatile("" ::: "memory");
              *(LAS u32x4*)twp = o[part][0];
              *(LAS u32x4*)(twp + 1024) = o[part][1];
              asm volatile("" ::: "memory");
#pragma unroll
              for (int rho = 0; rho < 2; rho++) tv[part * 2 + rho] = ldtr(trp[rho], trp[rho ^ 1] + 256);
            }
          }
          SCHED();
        }
        STAMP(13);
      } else {
        WAITV(0);
        BARRIER();
        tag = *(LAS u64*)(lds + C_TAG);
        if (role == 2) {
          const u32x4 gwv = *(const u32x4*)(p.gw + lane * 8);
          f32x2 gwf[4];
#pragma unroll
          for (int e = 0; e < 4; e++) gwf[e] = (f32x2){__uint_as_float(gwv[e] << 16), __uint_as_float(gwv[e] & 0xffff0000u)};
          const bf16x2 ones = {(__bf16)1.0f, (__bf16)1.0f};
#pragma unroll 1
          for (int it = 0; it < 4; it++) {
            const int r0 = rt * 256 + wid * 64 + it * 16;
            u32x4 xw[16];
#pragma unroll
            for (int r = 0; r < 16; r++) xw[r] = *(const u32x4*)(p.x + (size_t)(r0 + r) * D + lane * 8);
            float s[16];
#pragma unroll
            for (int r = 0; r < 16; r++) {
              s[r] = 0.f;
#pragma unroll
              for (int e = 0; e < 4; e++) {
                const unsigned w = xw[r][e];
                const f32x2 pr = (f32x2){__uint_as_float(w << 16), __uint_as_float(w & 0xffff0000u)} * gwf[e];
                const bf16x2 pb = {(__bf16)pr.x, (__bf16)pr.y};
                s[r] = __builtin_amdgcn_fdot2_f32_bf16(pb, ones, s[r], false);
              }
            }
#pragma unroll
            for (int r = 0; r < 8; r++) {
              const auto t = __builtin_amdgcn_permlane32_swap(__float_as_uint(s[r]), __float_as_uint(s[r + 8]), false, false);
              s[r] = __uint_as_float(t[0]) + __uint_as_float(t[1]);
            }
#pragma unroll
            for (int r = 0; r < 4; r++) {
              const auto t = __builtin_amdgcn_permlane16_swap(__float_as_uint(s[r]), __float_as_uint(s[r + 4]), false, false);
              s[r] = __uint_as_float(t[0]) + __uint_as_float(t[1]);
            }
#pragma unroll
            for (int r = 0; r < 4; r++) {
              s[r] += __uint_as_float(__builtin_amdgcn_update_dpp(0, __float_as_uint(s[r]), 0x128, 0xf, 0xf, false));
              s[r] += __uint_as_float(__builtin_amdgcn_update_dpp(0, __float_as_uint(s[r]), 0x124, 0xf, 0xf, false));
              s[r] += __uint_as_float(__builtin_amdgcn_update_dpp(0, __float_as_uint(s[r]), 0x122, 0xf, 0xf, false));
              s[r] += __uint_as_float(__builtin_amdgcn_update_dpp(0, __float_as_uint(s[r]), 0x121, 0xf, 0xf, false));
            }
            const float sv = (n & 2) ? ((n & 1) ? s[3] : s[2]) : ((n & 1) ? s[1] : s[0]);
            __builtin_amdgcn_raw_buffer_store_b32(__float_as_uint(rbf(sigmoidf_(rbf(sv)))), wsr,
                                                  n < 4 ? (unsigned)G_OFF + (r0 + n + 4 * (g & 1) + 8 * (g >> 1)) * 4 : OOR, 0, SC1);
          }
          WAITV(0);
          st_flag(wsr, vfo, FA_OFF, tag);
        }
      }
      grab_w();
    }
    STAMP(5);
    {
      if (HOLD == 10) lds_set(lds + C_FA + wid * 4);
      const u64 fv = ld_flag(wsr, vfi, FA_OFF);
      set_vh();
      const bool ok = __all(fv == tag);
      WAITV(0);
      st_flag(wsr, vfo, FB_OFF, tag);
      if (HOLD == 11) lds_set(lds + C_FA + wid * 4);
      STAMP(6);
      if (!ok) poll_flags(wsr, vfi, FA_OFF, tag);
      asm volatile("" ::: "memory");
    }
    STAMP(7);
    bf16x8 hf[4][4];
#pragma unroll
    for (int k = 0; k < 4; k++)
#pragma unroll
      for (int m = 0; m < 4; m++) hf[k][m] = ldh(gstep(k), m);
#pragma unroll
    for (int c = 0; c < 4; c++)
#pragma unroll
      for (int m = 0; m < 4; m++) acc2[c][m] = (f32x4){0.f, 0.f, 0.f, 0.f};
    LAS char* pw[4];
#pragma unroll
    for (int c = 0; c < 4; c++) { pw[c] = lds + wid * 12288 + c * 1024 + g * 256 + ((n ^ (4 * c + g)) << 4); PIN(pw[c]); }
    SCHED();
    u64 fbv = 0;
#pragma unroll
    for (int k = 0; k < 14; k++) {
      if (k == 2) fbv = ld_flag(wsr, vfi, FB_OFF);
      if (k == 3) { if (!__all(fbv == tag)) poll_flags(wsr, vfi, FB_OFF, tag); asm volatile("" ::: "memory"); STAMP(12); }
      if (k == 5) lds_wait(lds + C_F2 + wid * 4);
      if (k == 8) lds_wait(lds + C_F2 + wid * 4, 2u);
      if (k == 9) lds_set(lds + C_F3 + wid * 4);
      if (k == 7) ld_final();
      if (k == 11) prep_final();
#pragma unroll
      for (int m = 0; m < 4; m++) {
        if (k + 1 >= 5 && k + 1 < 14) wf[(k + 1) % 6][m] = ldw(k + 1, m);
#pragma unroll
        for (int c = 0; c < 4; c++) acc2[c][m] = __builtin_amdgcn_mfma_f32_16x16x32_bf16(wf[k % 6][c], hf[k & 3][m], acc2[c][m], 0, 0, 0);
        if (k + 4 < 14) hf[k & 3][m] = ldh(gstep(k + 4), m);
        if (k == 13 && m > 0) {
#pragma unroll
          for (int c = 0; c < 4; c++) *(LAS f32x4*)(pw[c] + wslot(m - 1)) = acc2[c][m - 1];
        }
        SCHED();
      }
    }
    STAMP(8);
#pragma unroll
    for (int c = 0; c < 4; c++) *(LAS f32x4*)(pw[c] + wslot(3)) = acc2[c][3];
  }
  BARRIER();
  STAMP(9);
  {
    const rsrc_t orc = mkrsrc(p.out, L * D * 4);
#pragma unroll
    for (int u = 0; u < 2; u++) {
      f32x4 b = *(LAS f32x4*)sb[u], a = sk[u] + *(LAS f32x4*)sa[u];
#pragma unroll
      for (int w = 1; w < 4; w++) {
        b += *(LAS f32x4*)(sb[u] + w * 12288);
        a += *(LAS f32x4*)(sa[u] + w * 12288);
      }
      const f32x4 o = gv[u] * a + omg[u] * b;
      __builtin_amdgcn_raw_buffer_store_b128(__builtin_bit_cast(u32x4, o), orc, vo + u * 8192, 0, OAUX);
    }
  }
  STAMP(10);
}

extern "C" void kernel_launch(void* const* d_in, const int* in_sizes, int n_in, void* d_out, int out_size, void* d_ws,
                              size_t ws_size, hipStream_t stream) {
  Params p;
  p.x = (const u16*)d_in[0]; p.lre = (const u16*)d_in[1]; p.lim = (const u16*)d_in[2];
  p.bre = (const u16*)d_in[3]; p.bim = (const u16*)d_in[4]; p.cre = (const u16*)d_in[5]; p.cim = (const u16*)d_in[6];
  p.ldt = (const u16*)d_in[7]; p.dsk = (const u16*)d_in[8];
  p.wg = (const u16*)d_in[10]; p.wu = (const u16*)d_in[11]; p.wd = (const u16*)d_in[12]; p.gw = (const u16*)d_in[13];
  p.ws = (char*)d_ws; p.out = (float*)d_out;
  (void)hipFuncSetAttribute((const void*)k_fused, hipFuncAttributeMaxDynamicSharedMemorySize, LDS_BYTES);
  k_fused<<<256, 512, LDS_BYTES, stream>>>(p);
}
```

```cpp
#include <hip/hip_runtime.h>
#include <stdint.h>

typedef unsigned short u16;
typedef unsigned long long u64;
typedef __bf16 bf16x8 __attribute__((ext_vector_type(8)));
typedef __bf16 bf16x4 __attribute__((ext_vector_type(4)));
typedef __bf16 bf16x2 __attribute__((ext_vector_type(2)));
typedef float f32x4 __attribute__((ext_vector_type(4)));
typedef float f32x2 __attribute__((ext_vector_type(2)));
typedef unsigned u32x4 __attribute__((ext_vector_type(4)));
typedef unsigned u32x2 __attribute__((ext_vector_type(2)));
#define LAS __attribute__((address_space(3)))
#define GAS __attribute__((address_space(1)))
#define RLX_AGENT __ATOMIC_RELAXED, __HIP_MEMORY_SCOPE_AGENT
typedef GAS unsigned gu32;
typedef GAS u64 gu64;
typedef __amdgpu_buffer_rsrc_t rsrc_t;

constexpr int L = 2048, D = 512, P = 512, F = 1792;
constexpr int KS2 = 88;
constexpr int SLOT = 53248;
constexpr int LDS_CTL = 3 * SLOT;
constexpr int LDS_BYTES = 163840;
constexpr int SC1 = 16;
constexpr size_t EPOCH_OFF = 0;
constexpr size_t G_OFF = 65536;
constexpr size_t E_OFF = 131072, E_BYTES = 16 * 512 * 16;
#define FS 128
constexpr unsigned FA_OFF = 262144, FB_OFF = FA_OFF + 1024 * FS, FC_OFF = FB_OFF + 1024 * FS;
constexpr size_t CT_OFF = 1u << 20, CT_BYTES = 1u << 20;
constexpr int HM = KS2 * 1024;
constexpr size_t H_OFF = CT_OFF + CT_BYTES, H_BYTES = (size_t)128 * HM;
constexpr u64 TAG_STEP = 0x9E3779B97F4A7C15ull;
#define OAUX 18
#define PSL 2
#define MAP2 1
#define ESL 4
#define DMAUX ""
#define PWL 1
#define NOG 0
#define PREB 1
#define HOLD 0
#define GBR 1
#define GPR 1
#define NA 3
#define NB 4
#define ASKD 8
#define PUBA 3
#define S1A 2
#define S2A 6
#define S3A 9
#define CHKB 3
constexpr unsigned OOR = 0x40000000u;
constexpr unsigned SPIN_CAP = 1u << 18;

#define BARRIER() asm volatile("s_waitcnt lgkmcnt(0)\n\ts_barrier" ::: "memory")
#define WAITV(n) asm volatile("s_waitcnt vmcnt(%0)" ::"i"(n) : "memory")
#define WAITL0() asm volatile("s_waitcnt lgkmcnt(0)" ::: "memory")
#define SCHED() __builtin_amdgcn_sched_barrier(0)
#define PIN(x) asm volatile("" : "+v"(x))

#define STAMP0() do {} while (0)
#define STAMP(id) do {} while (0)
#define LST(id) do {} while (0)

__device__ __forceinline__ float bf2f(u16 v) { return __uint_as_float(((unsigned)v) << 16); }
__device__ __forceinline__ float rbf(float f) { return (float)(__bf16)f; }
__device__ __forceinline__ unsigned pack2(float a, float b) {
  bf16x2 v = {(__bf16)a, (__bf16)b};
  return __builtin_bit_cast(unsigned, v);
}
__device__ __forceinline__ rsrc_t mkrsrc(const void* p, unsigned bytes) {
  return __builtin_amdgcn_make_buffer_rsrc((void*)p, 0, bytes, 0x00020000);
}
__device__ __forceinline__ u32x4 mkdesc(const void* p, unsigned bytes) {
  const u64 a = (u64)p;
  return (u32x4){(unsigned)a, (unsigned)(a >> 32) & 0xffffu, bytes, 0x00020000u};
}
__device__ __forceinline__ void dma16(u32x4 d, unsigned lds_dst, unsigned voff, unsigned soff) {
  asm volatile("s_mov_b32 m0, %2\n\ts_nop 3\n\tbuffer_load_dwordx4 %0, %1, %3 offen" DMAUX " lds" ::"v"(voff), "s"(d), "s"(lds_dst), "s"(soff) : "memory");
}
__device__ __forceinline__ u32x4 ld16_async(u32x4 d, unsigned voff, unsigned soff) {
  u32x4 r;
  asm volatile("s_nop 4\n\tbuffer_load_dwordx4 %0, %1, %2, %3 offen sc1" : "=v"(r) : "v"(voff), "s"(d), "s"(soff) : "memory");
  return r;
}
__device__ __forceinline__ u64 ld8_async(u32x4 d, unsigned voff) {
  u64 r;
  asm volatile("s_nop 4\n\tbuffer_load_dwordx2 %0, %1, %2, 0 offen sc1" : "=v"(r) : "v"(voff), "s"(d) : "memory");
  return r;
}
__device__ __forceinline__ unsigned ld2_async(const void* base, unsigned voff) {
  unsigned r;
  asm volatile("s_nop 4\n\tglobal_load_ushort %0, %1, %2" : "=v"(r) : "v"(voff), "s"(base) : "memory");
  return r;
}
__device__ __forceinline__ void st16_async(u32x4 d, unsigned voff, u32x4 v) {
  asm volatile("s_nop 4\n\tbuffer_store_dwordx4 %0, %1, %2, 0 offen sc1\n\ts_nop 1" ::"v"(v), "v"(voff), "s"(d) : "memory");
}
__device__ __forceinline__ void lds_set(LAS char* p, unsigned v = 1u) { __hip_atomic_store((LAS unsigned*)p, v, __ATOMIC_RELAXED, __HIP_MEMORY_SCOPE_WORKGROUP); }
__device__ __forceinline__ void touch(u32x4 d, unsigned lds_dst, unsigned voff, unsigned soff) {
  asm volatile("s_mov_b32 m0, %2\n\ts_nop 3\n\tbuffer_load_dword %0, %1, %3 offen lds" ::"v"(voff), "s"(d), "s"(lds_dst), "s"(soff) : "memory");
}
__device__ __forceinline__ void lds_wait(LAS char* p, unsigned want = 1u) {
#pragma unroll 1
  for (unsigned spins = 0; spins < SPIN_CAP * 8; spins++) {
    if ((unsigned)__builtin_amdgcn_readfirstlane(__hip_atomic_load((LAS unsigned*)p, __ATOMIC_RELAXED, __HIP_MEMORY_SCOPE_WORKGROUP)) >= want) break;
    __builtin_amdgcn_s_sleep(1);
  }
  asm volatile("" ::: "memory");
}
__device__ __forceinline__ bf16x8 ld128(LAS char* p) { return *(LAS bf16x8*)p; }
__device__ __forceinline__ bf16x8 ldtr(LAS char* p0, LAS char* p1) {
  bf16x4 a = __builtin_amdgcn_ds_read_tr16_b64_v4bf16((LAS bf16x4*)p0);
  bf16x4 b = __builtin_amdgcn_ds_read_tr16_b64_v4bf16((LAS bf16x4*)p1);
  return __builtin_shufflevector(a, b, 0, 1, 2, 3, 4, 5, 6, 7);
}
__device__ __forceinline__ float sigmoidf_(float g) {
  return __builtin_amdgcn_rcpf(1.0f + __builtin_amdgcn_exp2f(-1.4426950408889634f * g));
}
__device__ __forceinline__ f32x2 fma2(f32x2 a, f32x2 b, f32x2 c) { return __builtin_elementwise_fma(a, b, c); }
__device__ __forceinline__ f32x2 lo2(f32x4 v) { return __builtin_shufflevector(v, v, 0, 1); }
__device__ __forceinline__ f32x2 hi2(f32x4 v) { return __builtin_shufflevector(v, v, 2, 3); }
__device__ __forceinline__ void csq(float& r, float& i) { const float t = r * r - i * i; i = 2.f * r * i; r = t; }

struct Params {
  const u16 *x, *wg, *wu, *bre, *bim, *ldt, *lre, *lim, *cre, *cim, *dsk, *wd, *gw;
  char* ws;
  float* out;
};

__device__ __forceinline__ void ssm_consts(u16 ldt, u16 lre, u16 lim, float& lr, float& li, float& cr, float& ci) {
  const float dt = rbf(expf(bf2f(ldt)));
  const float a = bf2f(lre), b = bf2f(lim);
  const float m = expf(dt * a);
  float s, c;
  sincosf(dt * b, &s, &c);
  lr = m * c;
  li = m * s;
  const float nr = lr - 1.0f, ni = li, rden = 1.0f / (a * a + b * b);
  cr = (nr * a + ni * b) * rden;
  ci = (ni * a - nr * b) * rden;
}
__device__ __forceinline__ void share2(float v, float (&o)[2]) {
  const auto t = __builtin_amdgcn_permlane16_swap(__float_as_uint(v), __float_as_uint(v), false, false);
  o[0] = __uint_as_float(t[0]); o[1] = __uint_as_float(t[1]);
}

__device__ __forceinline__ u64 ld_flag(rsrc_t r, unsigned voff, unsigned soff) {
  return __builtin_bit_cast(u64, __builtin_amdgcn_raw_buffer_load_b64(r, voff, soff, SC1));
}
__device__ __forceinline__ void st_flag(rsrc_t r, unsigned voff, unsigned soff, u64 tag) {
  __builtin_amdgcn_raw_buffer_store_b64(__builtin_bit_cast(u32x2, tag), r, voff, soff, SC1);
}
__device__ __forceinline__ void poll_flags(rsrc_t r, unsigned voff, unsigned soff, u64 tag) {
#pragma unroll 1
  for (unsigned spins = 0; spins < SPIN_CAP; spins++) {
    asm volatile("" ::: "memory");
    if (__all(ld_flag(r, voff, soff) == tag)) break;
    __builtin_amdgcn_s_sleep(PSL);
  }
  asm volatile("" ::: "memory");
}

template <bool GLU, int NR>
__device__ __forceinline__ void main_loop(LAS char* lds, int wr, int wc, int lane, f32x4 (&acc)[8][NR]) {
  const int i = lane & 15, g = lane >> 4;
  const int sw = (i >> 1) & 7;
  LAS char *resA[2], *rolA[4];
  if constexpr (GLU) {
    resA[0] = lds + (wr * 80 + i) * 128 + ((g ^ sw) << 4);
    resA[1] = lds + (wr * 80 + i) * 128 + ((g ^ sw ^ 4) << 4);
    const int q = (lane & 15) >> 2, pp = lane & 3;
    const int swz = q | ((g & 1) << 2);
#pragma unroll
    for (int c = 0; c < 4; c++) rolA[c] = lds + 20480 + (8 * g + q) * 256 + (((wc * 4 + c) ^ swz) << 5) + pp * 8;
  } else {
    resA[0] = lds + 32768 + (wc * 32 + i) * 128 + ((g ^ sw) << 4);
    resA[1] = lds + 32768 + (wc * 32 + i) * 128 + ((g ^ sw ^ 4) << 4);
    rolA[0] = lds + (wr * 128 + i) * 128 + ((g ^ sw) << 4);
    rolA[1] = lds + (wr * 128 + i) * 128 + ((g ^ sw ^ 4) << 4);
    rolA[2] = rolA[3] = lds;
  }
  auto ld_res = [&](int ks, int r) -> bf16x8 {
    if constexpr (GLU) return ld128(resA[ks] + r * 2048);
    else return ld128(resA[ks] + (r & 1) * 2048 + (r >> 1) * 8192);
  };
  auto ld_rol = [&](int j) -> bf16x8 {
    const int ks = j >> 3, c = j & 7;
    if constexpr (GLU) {
      LAS char* b = rolA[c & 3] + (c >> 2) * 16384 + ks * 8192;
      return ldtr(b, b + 1024);
    } else {
      return ld128(rolA[ks] + c * 2048);
    }
  };
#pragma unroll
  for (int c = 0; c < 8; c++)
#pragma unroll
    for (int r = 0; r < NR; r++) { acc[c][r] = (f32x4){0.f, 0.f, 0.f, 0.f}; PIN(acc[c][r]); }
  bf16x8 res[2][NR], rol[5];
  WAITV(0);
  BARRIER();
  rol[0] = ld_rol(0); SCHED(); res[0][0] = ld_res(0, 0); SCHED(); res[0][1] = ld_res(0, 1); SCHED();
  rol[1] = ld_rol(1); SCHED(); res[0][2] = ld_res(0, 2); SCHED(); res[0][3] = ld_res(0, 3); SCHED();
  rol[2] = ld_rol(2); SCHED();
  if constexpr (NR > 4) res[0][NR - 1] = ld_res(0, NR - 1);
  SCHED();
  int slot = 0;
#pragma unroll 1
  for (unsigned s = 0; s < 8; s++) {
#pragma unroll
    for (int j = 0; j < 16; j++) {
      int nds = 0;
      constexpr int RD = GLU ? 2 : 1;
      if (j == 12) {
        const int delta = (slot == 2) ? -2 * SLOT : SLOT;
        slot = (slot == 2) ? 0 : slot + 1;
        resA[0] += delta; resA[1] += delta;
        rolA[0] += delta; rolA[1] += delta;
        PIN(resA[0]); PIN(resA[1]); PIN(rolA[0]); PIN(rolA[1]);
        if constexpr (GLU) { rolA[2] += delta; rolA[3] += delta; PIN(rolA[2]); PIN(rolA[3]); }
      }
      if (j == 13) {
        BARRIER();
        SCHED();
      }
      if (j != 12) { rol[(j + 3) & 3] = ld_rol((j + 3) & 15); nds += RD; }
      if (j == 10) { rol[4] = ld_rol(15); nds += RD; }
      if (j >= 2 && j < 2 + NR) { res[1][j - 2] = ld_res(1, j - 2); nds++; }
      if (j == 13) { res[0][0] = ld_res(0, 0); res[0][1] = ld_res(0, 1); nds += 2; }
      if (j == 14) { res[0][2] = ld_res(0, 2); res[0][3] = ld_res(0, 3); nds += 2; }
      if (j == 15 && NR > 4) { res[0][NR - 1] = ld_res(0, NR - 1); nds++; }
#pragma unroll
      for (int r = 0; r < NR; r++)
        acc[j & 7][r] = __builtin_amdgcn_mfma_f32_16x16x32_bf16(rol[j == 15 ? 4 : (j & 3)], res[j >> 3][r], acc[j & 7][r], 0, 0, 0);
#pragma unroll
      for (int r = 0; r < NR; r++) {
        __builtin_amdgcn_sched_group_barrier(0x8, 1, 0);
        if (j == 12) __builtin_amdgcn_sched_group_barrier(0x2, 2, 0);
        if (r < nds) __builtin_amdgcn_sched_group_barrier(0x100, 1, 0);
      }
      SCHED();
    }
  }
}

__device__ __forceinline__ constexpr int wslot(int i) { return (i / 3 == 0 ? 2 : (i / 3 == 1 ? 0 : 1)) * SLOT + (i % 3) * 4096; }
__device__ __forceinline__ constexpr int gstep(int i) { return i < 7 ? 2 * i : 2 * i - 13; }
constexpr int C_TAG = LDS_CTL, C_F0 = LDS_CTL + 16, C_F1 = LDS_CTL + 32, C_F2 = LDS_CTL + 48, C_F3 = LDS_CTL + 64, C_FA = LDS_CTL + 80;
constexpr int C_DUMP = LDS_CTL + 1024, C_GW = LDS_CTL + 2048;

__global__ __launch_bounds__(512, 2) void k_fused(Params p) {
  extern __shared__ __attribute__((aligned(16))) char smem[];
  LAS char* lds = (LAS char*)smem;
  const int tid = threadIdx.x, lane = tid & 63;
  STAMP0();
  const int wid = __builtin_amdgcn_readfirstlane(tid >> 6);
  const int xg = blockIdx.x & 7, idx = blockIdx.x >> 3;
  {
    u64 pc;
    unsigned len;
    asm volatile("s_getpc_b64 %0\n.Lgm_code_begin:\n\ts_mov_b32 %1, .Lgm_code_end-.Lgm_code_begin" : "=s"(pc), "=s"(len));
    touch(mkdesc((const void*)pc, len), C_DUMP, lane < 2 ? lane * 128 : OOR, (wid * 32 + idx) * 256);
  }
  const int hh = xg & 1, cj = xg >> 1, odd = cj & 1;
  const int ncol = 4 - odd, col0 = (cj >> 1) * 7 + odd * 4;
  const int e_ = idx - 4 - odd * 8, ec = (e_ * 43) >> 8, em = e_ - ncol * 6, es = em - (hh ? ncol - 2 : 2);
  const bool ssm = e_ < 0;
  const int role = ssm ? 1 : (es < 0 ? 0 : 2);
  const int rt = ssm ? hh * 4 + (idx & 3) : (em < 0 ? hh * 7 + e_ - ec * 6 : 6);
  const int ct = ssm ? (cj >> 1) * 4 + odd + (idx >> 2) : col0 + (em < 0 ? ec : hh * 2 + em);
  const int w4 = wid & 3;
  const int n = lane & 15, g = lane >> 4;
  const int k0 = (2 * cj + 4 * hh) & 7, gam = (rt + ct) & 1;
  const u32x4 xr = mkdesc(p.x, L * D * 2);
  const u32x4 w0r = mkdesc(ssm ? p.bre : p.wg, ssm ? P * D * 2 : D * F * 2), w1r = mkdesc(ssm ? p.bim : p.wu, ssm ? P * D * 2 : D * F * 2);
  unsigned vx, vw;
  {
    const int r8 = 8 * w4 + (lane >> 3), ii = r8 & 15;
    const unsigned csw = ((lane & 7) ^ (ii >> 1)) << 4;
    const int xrow = ssm ? rt * 256 + 32 * (ii >> 2) + 16 * (ii & 1) + ((ii >> 1) & 1) + (w4 & 2) : min(rt * 160, L - 160) + r8;
    vx = xrow * 1024 + csw;
    vw = ssm ? (ct * 64 + r8) * 1024 + csw : (4 * w4 + g) * (F * 2) + ct * 256 + ((n ^ ((g | ((w4 & 2) << 1)) << 1)) << 4);
  }
  const unsigned stx = ssm ? 4096 : 32768, stw = ssm ? 32768 : 16 * F * 2, sgw = ssm ? 128 : 64 * F * 2;
  const unsigned lw0 = ssm ? 32768 : 20480, lw1 = ssm ? 40960 : 36864;
  auto pieces = [&](int s, int a, int b) {
    if (role >= 2) return;
    const unsigned sg = ((s ^ gam) + k0) & 7, lb = (s % 3) * SLOT + w4 * 1024;
    auto px = [&](int i) { dma16(xr, lb + i * 4096, vx, sg * 128 + (i >> 2) * 131072 + (i & 3) * stx); };
    auto pw = [&](int k) { dma16((k & 1) ? w1r : w0r, lb + ((k & 1) ? lw1 : lw0) + (k >> 1) * 4096, vw, sg * sgw + (k >> 1) * stw); };
#pragma unroll
    for (int k = a; k < (b < 9 ? b : 9); k++) { if (k < 5) px(k); else pw(k - 5); }
    if (b > 9) {
      if (ssm) { px(5); px(6); px(7); }
      else { pw(4); pw(5); pw(6); pw(7); }
    }
  };
  unsigned c_ldt, c_lre, c_lim;
  const int chg = (ct * 64 + (wid & 1) * 32 + (lane & 31)) & (P - 1);
  if (wid < 4) {
    if (ssm) { c_ldt = ld2_async(p.ldt, chg * 2); c_lre = ld2_async(p.lre, chg * 2); c_lim = ld2_async(p.lim, chg * 2); }
    pieces(0, 0, 6);
    if (wid == 1) dma16(mkdesc(p.gw, D * 2), C_GW, lane * 16, 0);
  } else {
    pieces(0, 6, 10);
  }
  const int myid = role == 0 ? rt * 14 + ct : (ssm ? 182 + rt * 8 + ct : 246);
  const int rb = MAP2 ? (xg >> 1) * 8 + (idx >> 2) : xg * 4 + (idx >> 3), cb = MAP2 ? (xg & 1) * 4 + (idx & 3) : idx & 7;
  const rsrc_t hr = mkrsrc(p.ws + H_OFF, (unsigned)H_BYTES), wsr = mkrsrc(p.ws, (unsigned)H_OFF);
  f32x4 acc2[4][4];
  u64 tag;
  unsigned vh[4];
  auto ldh = [&](int k, int m) -> bf16x8 {
    return __builtin_bit_cast(bf16x8, __builtin_amdgcn_raw_buffer_load_b128(hr, vh[m] + (k & 3) * 1024, (k & ~3) * 1024, SC1));
  };
  auto set_vh = [&]() { vh[1] = vh[0] + HM; vh[2] = vh[0] + 2 * HM; vh[3] = vh[0] + 3 * HM; PIN(vh[1]); PIN(vh[2]); PIN(vh[3]); };
  auto glu_flags = [&]() -> unsigned {
    const int l14 = lane < 14 ? lane : 0, j = l14 >= 7 ? l14 - 7 : l14, pr = 7 * w4 + j;
    const int mt = rb * 4 + (l14 >= 7 ? 3 : 0), om = mt < 120 ? mt / 5 : (mt < 123 ? 24 : 25);
    return (((om >> 1) * 14 + (pr >> 1)) * 4 + (om & 1) * 2 + (pr & 1)) * FS;
  };
  float gv[2];
  u32x2 gg[2], xv[2], dv;
  unsigned vg;
  auto ld_final = [&]() {
    int ln = lane;
    PIN(ln);
    const int t = rb * 64 + wid * 8 + (ln >> 4), dcol = cb * 64 + (ln & 15) * 4;
    const rsrc_t xr_ = mkrsrc(p.x, L * D * 2);
    vg = t * 8;
#pragma unroll
    for (int u = 0; u < 2; u++) {
      gg[u] = __builtin_amdgcn_raw_buffer_load_b64(wsr, vg + u * 32, (unsigned)G_OFF, SC1);
      xv[u] = __builtin_amdgcn_raw_buffer_load_b64(xr_, t * 1024 + dcol * 2, u * 4096, 0);
    }
    dv = *(const u32x2*)(p.dsk + dcol);
  };

  LAS char *sb[2], *sa[2];
  f32x4 sk[2];
  float omg[2];
  unsigned vo;
  auto prep_final = [&]() {
#pragma unroll 1
    for (unsigned spins = 0; spins < SPIN_CAP && !__all(gg[0][1] == (unsigned)tag && gg[1][1] == (unsigned)tag); spins++) {
      asm volatile("" ::: "memory");
#pragma unroll
      for (int u = 0; u < 2; u++) gg[u] = __builtin_amdgcn_raw_buffer_load_b64(wsr, vg + u * 32, (unsigned)G_OFF, SC1);
    }
    gv[0] = __uint_as_float(gg[0][0]); gv[1] = __uint_as_float(gg[1][0]);
    int ln = lane;
    PIN(ln);
    const int nn = ln & 15, mt = wid >> 1;
    const int ob = mt < 3 ? wslot(0) + mt * 4096 : wslot(3), oa = mt == 0 ? wslot(5) : wslot(6) + (mt - 1) * 4096;
    vo = ((rb * 64 + wid * 8 + (ln >> 4)) * D + cb * 64 + nn * 4) * 4;
#pragma unroll
    for (int u = 0; u < 2; u++) {
      const int sl = ((wid & 1) * 8 + u * 4 + (ln >> 4)) ^ nn;
      sb[u] = lds + ob + nn * 256 + (sl << 4);
      sa[u] = lds + oa + nn * 256 + ((sl ^ (4 * mt)) << 4);
      omg[u] = rbf(1.0f - gv[u]);
#pragma unroll
      for (int e = 0; e < 4; e++) {
        const unsigned xw = xv[u][e >> 1], dw = dv[e >> 1];
        const float xf = __uint_as_float((e & 1) ? (xw & 0xffff0000u) : (xw << 16));
        const float df = __uint_as_float((e & 1) ? (dw & 0xffff0000u) : (dw << 16));
        sk[u][e] = rbf(xf * df);
      }
      PIN(sb[u]); PIN(sa[u]); PIN(omg[u]); PIN(sk[u]);
    }
    PIN(vo);
  };

  if (wid < 4) {
    const int wr = wid >> 1, wc = wid & 1;
    if (wid == 0) {
      u64 w0;
      asm volatile("s_load_dwordx2 %0, %1, 0x0\n\ts_waitcnt lgkmcnt(0)" : "=s"(w0) : "s"(p.ws + EPOCH_OFF) : "memory");
      *(LAS u64*)(lds + C_TAG + lane * 8) = lane == 0 ? w0 + TAG_STEP : 0ull;
    }
    unsigned vfo = lane == 0 && role < 2 ? (myid * 4 + wid) * FS : OOR;
    unsigned vfi = glu_flags();
    vh[0] = rb * 4 * HM + wid * 14 * 1024 + lane * 16;
    LAS char* ra0 = lds + wid * 12288 + (4 * g + (n >> 2)) * 128 + (((2 * g + (n >> 3)) & 3) << 5) + (n & 3) * 8;
    PIN(vfo); PIN(vfi); PIN(vh[0]); PIN(ra0);
    bf16x8 wf[6][4];
    LAS char* ra[3][4];
    auto ldw = [&](int i, int c) -> bf16x8 {
      const int k = i < 9 ? i : i - 9;
      LAS char* a = ra[k / 3][c] + (k % 3) * 4096;
      return ldtr(a, a + 2048);
    };
    auto grab_w = [&]() {
#pragma unroll
      for (int c = 0; c < 4; c++) {
        ra[1][c] = (LAS char*)((unsigned)(size_t)ra0 ^ (c << 5));
        ra[0][c] = ra[1][c] + 2 * SLOT;
        ra[2][c] = ra[1][c] + SLOT;
      }
      lds_wait(lds + C_F0 + wid * 4);
#pragma unroll
      for (int i = 0; i < 5; i++)
#pragma unroll
        for (int c = 0; c < 4; c++) wf[i][c] = ldw(i, c);
      asm volatile("" ::: "memory");
      lds_set(lds + C_F1 + wid * 4);
    };
    if (role == 0) {
      const int mt0 = (min(rt * 160, L - 160) >> 4) + wr * 5;
      unsigned vs[5];
#pragma unroll
      for (int m = 0; m < 5; m++) { vs[m] = mt0 + m < rt * 10 ? OOR : (mt0 + m) * HM + (ct * 4 + wc * 2) * 1024 + lane * 16; PIN(vs[m]); }
      f32x4 acc[8][5];
      STAMP(1);
      main_loop<true, 5>(lds, wr, wc, lane, acc);
      STAMP(2);
      tag = *(LAS u64*)(lds + C_TAG);
#pragma unroll
      for (int h = 0; h < 2; h++) {
        if (h == 1) { SCHED(); STAMP(13); grab_w(); STAMP(14); SCHED(); }
#pragma unroll
        for (int m = 0; m < 5; m++) {
          if (HOLD && HOLD <= 10 && h * 5 + m == HOLD) lds_set(lds + C_FA + wid * 4);
          if (h == 1 && m == PUBA) { WAITV(PUBA); st_flag(wsr, vfo, FA_OFF, tag); STAMP(3); }
          u32x4 o;
#pragma unroll
          for (int e = 0; e < 4; e++) {
            const int c = 2 * h + (e >> 1);
            const f32x2 gv_ = (e & 1) ? hi2(acc[c][m]) : lo2(acc[c][m]);
            const f32x2 uv = (e & 1) ? hi2(acc[4 + c][m]) : lo2(acc[4 + c][m]);
            f32x2 t = gv_ * -1.4426950408889634f;
            t = (f32x2){__builtin_amdgcn_exp2f(t.x), __builtin_amdgcn_exp2f(t.y)} + 1.0f;
            t = (f32x2){__builtin_amdgcn_rcpf(t.x), __builtin_amdgcn_rcpf(t.y)} * uv;
            o[e] = pack2(t.x, t.y);
          }
          __builtin_amdgcn_raw_buffer_store_b128(o, hr, vs[m] + h * 1024, 0, SC1);
        }
      }
    } else {
      if (role == 1) {
        float lr[2], li[2], cr[2], ci[2], sr[2], si[2], mr[2], mi[2], wr_[2], wi_[2], qr, qi, zr, zi;
        {
          float a2, a3;
          WAITV(6);
          PIN(c_ldt); PIN(c_lre); PIN(c_lim);
          ssm_consts(c_ldt, c_lre, c_lim, qr, qi, a2, a3);
          share2(qr, lr); share2(qi, li); share2(2.f * a2, cr); share2(2.f * a3, ci);
          csq(qr, qi); csq(qr, qi); csq(qr, qi); csq(qr, qi);
          share2(qr, sr); share2(qi, si);
          csq(qr, qi);
          share2(qr, mr); share2(qi, mi);
          csq(qr, qi); csq(qr, qi);
          zr = qr; zi = qi;
          csq(zr, zi); csq(zr, zi); csq(zr, zi);
          if (rt * 2 + wr <= 8) { zr = 0.f; zi = 0.f; }
#pragma unroll
          for (int u = 0; u < 2; u++) {
            float ar = mr[u], ai = mi[u];
            csq(ar, ai);
            const float br_ = (g & 1) ? mr[u] : 1.f, bi_ = (g & 1) ? mi[u] : 0.f;
            wr_[u] = (g & 2) ? ar * br_ - ai * bi_ : br_;
            wi_[u] = (g & 2) ? ar * bi_ + ai * br_ : bi_;
            PIN(mr[u]); PIN(mi[u]); PIN(lr[u]); PIN(li[u]); PIN(cr[u]); PIN(ci[u]); PIN(sr[u]); PIN(si[u]); PIN(wr_[u]); PIN(wi_[u]);
          }
          PIN(qr); PIN(qi); PIN(zr); PIN(zi);
        }
        const int ck = rt * 2 + wr;
        unsigned veo = lane < 32 ? (unsigned)E_OFF + (ck * 512 + chg) * 16 : OOR;
        const int c0 = ck - 1 - 8 * (lane >> 5);
        int vei = (c0 * 512 + chg) * 16, nval = min(max(c0 + 1, 0), 8);
        const unsigned tb = (wid >> 1) * 2 * SLOT + 49152 + (wid & 1) * 2048;
        LAS char* twp = lds + tb + n * 64 + ((g ^ (((n >> 1) & 1) | ((((n >> 2) ^ (n >> 3)) & 1) << 1))) << 4);
        LAS char* trp[2];
#pragma unroll
        for (int t = 0; t < 2; t++)
          trp[t] = lds + tb + (8 * g + (n >> 2)) * 64 + (((((n >> 1) ^ (n >> 3)) & 1) | (((g ^ t) & 1) << 1)) << 4) + (n & 1) * 8;
        unsigned vso[8];
#pragma unroll
        for (int k = 0; k < 8; k++) { vso[k] = (rt * 16 + wr * 8 + k) * HM + (56 + (ct * 2 + wc) * 2) * 1024 + lane * 16; PIN(vso[k]); }
        PIN(veo); PIN(vei); PIN(nval); PIN(twp); PIN(trp[0]); PIN(trp[1]);
        f32x4 acc[8][4];
        STAMP(1);
        main_loop<false, 4>(lds, wr, wc, lane, acc);
        STAMP(2);
        tag = *(LAS u64*)(lds + C_TAG);
        f32x2 er[2], ei[2];
#pragma unroll
        for (int u = 0; u < 2; u++) { er[u] = lo2(acc[0][u]); ei[u] = lo2(acc[0][2 + u]); }
#pragma unroll
        for (int st = 1; st < 16; st++)
#pragma unroll
          for (int u = 0; u < 2; u++) {
            const f32x2 l2 = {lr[u], lr[u]}, i2 = {li[u], li[u]};
            const f32x2 vr = (st & 1) ? hi2(acc[st >> 1][u]) : lo2(acc[st >> 1][u]);
            const f32x2 vi = (st & 1) ? hi2(acc[st >> 1][2 + u]) : lo2(acc[st >> 1][2 + u]);
            const f32x2 t = fma2(-i2, ei[u], fma2(l2, er[u], vr));
            ei[u] = fma2(i2, er[u], fma2(l2, ei[u], vi));
            er[u] = t;
          }
        float xs[2][4], ys[2][4];
#pragma unroll
        for (int u = 0; u < 2; u++) {
          const float a = fmaf(-si[u], ei[u].x, fmaf(sr[u], er[u].x, er[u].y)), b = fmaf(si[u], er[u].x, fmaf(sr[u], ei[u].x, ei[u].y));
          float a2[2], b2[2];
          share2(a, a2); share2(b, b2);
#pragma unroll
          for (int k = 0; k < 2; k++) {
            const auto ta = __builtin_amdgcn_permlane32_swap(__float_as_uint(a2[k]), __float_as_uint(a2[k]), false, false);
            const auto tb_ = __builtin_amdgcn_permlane32_swap(__float_as_uint(b2[k]), __float_as_uint(b2[k]), false, false);
            xs[u][k] = __uint_as_float(ta[0]); xs[u][2 + k] = __uint_as_float(ta[1]);
            ys[u][k] = __uint_as_float(tb_[0]); ys[u][2 + k] = __uint_as_float(tb_[1]);
          }
#pragma unroll
          for (int k = 1; k < 4; k++) {
            xs[u][k] = fmaf(-mi[u], ys[u][k - 1], fmaf(mr[u], xs[u][k - 1], xs[u][k]));
            ys[u][k] = fmaf(mi[u], xs[u][k - 1], fmaf(mr[u], ys[u][k - 1], ys[u][k]));
            PIN(xs[u][k]); PIN(ys[u][k]);
          }
        }
        const unsigned tg = (unsigned)tag;
        __builtin_amdgcn_raw_buffer_store_b128((u32x4){__float_as_uint(g ? xs[1][3] : xs[0][3]), tg, __float_as_uint(g ? ys[1][3] : ys[0][3]), tg},
                                               wsr, veo, 0, SC1);
        STAMP(3);
        SCHED();
        float pr[2], pi[2];
#pragma unroll
        for (int u = 0; u < 2; u++) {
          pr[u] = g == 0 ? 0.f : (g == 1 ? xs[u][0] : (g == 2 ? xs[u][1] : xs[u][2]));
          pi[u] = g == 0 ? 0.f : (g == 1 ? ys[u][0] : (g == 2 ? ys[u][1] : ys[u][2]));
          PIN(pr[u]); PIN(pi[u]);
        }
        const rsrc_t erc = mkrsrc(p.ws + E_OFF, (unsigned)E_BYTES);
        u32x4 v[8];
        auto ask = [&]() {
#pragma unroll
          for (int d = 0; d < 8; d++) v[d] = __builtin_amdgcn_raw_buffer_load_b128(erc, vei - d * 8192, 0, SC1);
        };
#pragma unroll
        for (int u = 0; u < 2; u++) {
          if (u == 1) { SCHED(); ask(); SCHED(); }
          PIN(cr[u]);
#pragma unroll
          for (int m = 0; m < 8; m++) {
            const f32x4 a = acc[m][u], b = acc[m][2 + u];
            acc[m][u] = cr[u] * a - ci[u] * b;
            acc[m][2 + u] = cr[u] * b + ci[u] * a;
            PIN(acc[m][u]); PIN(acc[m][2 + u]);
          }
        }
#pragma unroll 1
        for (unsigned spins = 0; spins < SPIN_CAP; spins++) {
          int cnt = 0;
#pragma unroll
          for (int d = 0; d < 8; d++) cnt += ((v[d][1] ^ tg) | (v[d][3] ^ tg)) == 0u;
          if (__all(cnt >= nval)) break;
          asm volatile("" ::: "memory");
          __builtin_amdgcn_s_sleep(ESL);
          ask();
        }
        float kr[2], ki[2];
        {
          float xr_ = __uint_as_float(v[7][0]), xi_ = __uint_as_float(v[7][2]);
#pragma unroll
          for (int d = 6; d >= 0; d--) {
            const float t = fmaf(-qi, xi_, fmaf(qr, xr_, __uint_as_float(v[d][0])));
            xi_ = fmaf(qi, xr_, fmaf(qr, xi_, __uint_as_float(v[d][2])));
            xr_ = t;
            PIN(xr_); PIN(xi_);
          }
          const auto ta = __builtin_amdgcn_permlane32_swap(__float_as_uint(xr_), __float_as_uint(xr_), false, false);
          const auto tb_ = __builtin_amdgcn_permlane32_swap(__float_as_uint(xi_), __float_as_uint(xi_), false, false);
          const float fr = __uint_as_float(ta[1]), fi = __uint_as_float(tb_[1]);
          share2(fmaf(-zi, fi, fmaf(zr, fr, __uint_as_float(ta[0]))), kr);
          share2(fmaf(zi, fr, fmaf(zr, fi, __uint_as_float(tb_[0]))), ki);
        }
        STAMP(4);
        f32x2 hr_[2], hi_[2];
#pragma unroll
        for (int u = 0; u < 2; u++) {
          f32x2 a, b;
          a.x = fmaf(-wi_[u], ki[u], fmaf(wr_[u], kr[u], pr[u])); b.x = fmaf(wi_[u], kr[u], fmaf(wr_[u], ki[u], pi[u]));
          a.y = fmaf(-si[u], b.x, fmaf(sr[u], a.x, er[u].x)); b.y = fmaf(si[u], a.x, fmaf(sr[u], b.x, ei[u].x));
          PIN(a); PIN(b);
          hr_[u] = cr[u] * a - ci[u] * b; hi_[u] = cr[u] * b + ci[u] * a;
        }
        bf16x8 tv[4];
#pragma unroll
        for (int qt = 0; qt < 5; qt++) {
          u32x4 o[2][2];
          if (qt < 4) {
#pragma unroll
            for (int mm = 0; mm < 2; mm++)
#pragma unroll
              for (int u = 0; u < 2; u++) {
                const int m = qt * 2 + mm;
                const f32x2 l2 = {lr[u], lr[u]}, i2 = {li[u], li[u]};
                const f32x2 r0_ = fma2(-i2, hi_[u], fma2(l2, hr_[u], lo2(acc[m][u])));
                const f32x2 i0_ = fma2(i2, hr_[u], fma2(l2, hi_[u], lo2(acc[m][2 + u])));
                hr_[u] = fma2(-i2, i0_, fma2(l2, r0_, hi2(acc[m][u])));
                hi_[u] = fma2(i2, r0_, fma2(l2, i0_, hi2(acc[m][2 + u])));
                o[0][u][mm] = pack2(r0_.x, hr_[u].x); o[0][u][2 + mm] = pack2(r0_.y, hr_[u].y);
                o[1][u][mm] = pack2(-i0_.x, -hi_[u].x); o[1][u][2 + mm] = pack2(-i0_.y, -hi_[u].y);
              }
          }
          SCHED();
          if (qt > 0) {
#pragma unroll
            for (int k = 0; k < 4; k++) __builtin_amdgcn_raw_buffer_store_b128(__builtin_bit_cast(u32x4, tv[k]), hr, vso[(k & 1) * 4 + qt - 1] + (k >> 1) * 1024, 0, SC1);
          }
          if (qt < 4) {
#pragma unroll
            for (int part = 0; part < 2; part++) {
              asm volatile("" ::: "memory");
              *(LAS u32x4*)twp = o[part][0];
              *(LAS u32x4*)(twp + 1024) = o[part][1];
              asm volatile("" ::: "memory");
#pragma unroll
              for (int rho = 0; rho < 2; rho++) tv[part * 2 + rho] = ldtr(trp[rho], trp[rho ^ 1] + 256);
            }
          }
          SCHED();
        }
        STAMP(13);
      } else {
        WAITV(0);
#pragma unroll 1
        for (int k = 0; k < 8; k++) BARRIER();
        tag = *(LAS u64*)(lds + C_TAG);
        BARRIER();
      }
      grab_w();
    }
    STAMP(5);
    {
      if (HOLD == 10) lds_set(lds + C_FA + wid * 4);
      const u64 fv = ld_flag(wsr, vfi, FA_OFF);
      set_vh();
      const bool ok = __all(fv == tag);
      WAITV(0);
      st_flag(wsr, vfo, FB_OFF, tag);
      if (HOLD == 11) lds_set(lds + C_FA + wid * 4);
      STAMP(6);
      if (!ok) poll_flags(wsr, vfi, FA_OFF, tag);
      asm volatile("" ::: "memory");
    }
    STAMP(7);
    bf16x8 hf[4][4];
#pragma unroll
    for (int k = 0; k < 4; k++)
#pragma unroll
      for (int m = 0; m < 4; m++) hf[k][m] = ldh(gstep(k), m);
#pragma unroll
    for (int c = 0; c < 4; c++)
#pragma unroll
      for (int m = 0; m < 4; m++) acc2[c][m] = (f32x4){0.f, 0.f, 0.f, 0.f};
    LAS char* pw[4];
#pragma unroll
    for (int c = 0; c < 4; c++) { pw[c] = lds + wid * 12288 + c * 1024 + g * 256 + ((n ^ (4 * c + g)) << 4); PIN(pw[c]); }
    SCHED();
    u64 fbv = 0;
#pragma unroll
    for (int k = 0; k < 14; k++) {
      if (k == 2) fbv = ld_flag(wsr, vfi, FB_OFF);
      if (k == 3) { if (!__all(fbv == tag)) poll_flags(wsr, vfi, FB_OFF, tag); asm volatile("" ::: "memory"); STAMP(12); }
      if (k == 5) lds_wait(lds + C_F2 + wid * 4);
      if (k == 8) lds_wait(lds + C_F2 + wid * 4, 2u);
      if (k == 9) lds_set(lds + C_F3 + wid * 4);
      if (k == 7) ld_final();
      if (k == 11) prep_final();
#pragma unroll
      for (int m = 0; m < 4; m++) {
        if (k + 1 >= 5 && k + 1 < 14) wf[(k + 1) % 6][m] = ldw(k + 1, m);
#pragma unroll
        for (int c = 0; c < 4; c++) acc2[c][m] = __builtin_amdgcn_mfma_f32_16x16x32_bf16(wf[k % 6][c], hf[k & 3][m], acc2[c][m], 0, 0, 0);
        if (k + 4 < 14) hf[k & 3][m] = ldh(gstep(k + 4), m);
        if (k == 13 && m > 0) {
#pragma unroll
          for (int c = 0; c < 4; c++) *(LAS f32x4*)(pw[c] + wslot(m - 1)) = acc2[c][m - 1];
        }
        SCHED();
      }
    }
    STAMP(8);
#pragma unroll
    for (int c = 0; c < 4; c++) *(LAS f32x4*)(pw[c] + wslot(3)) = acc2[c][3];
  } else {
    const int lw = w4;
    const u32x4 wsd = mkdesc(p.ws, (unsigned)H_OFF);
    const u32x4 wdr = mkdesc(p.wd, F * D * 2);
    const unsigned vwd = (lw * 14 * 32 + (lane >> 3)) * 1024 + cb * 128 + (((lane & 7) ^ (((lane >> 4) & 3) << 1)) << 4);
    u32x4 ctv;
    u64 cfl;
    auto ct_load = [&]() {
      const int b = blockIdx.x;
      ctv = ld16_async(mkdesc((b & 1) ? p.cim : p.cre, D * P * 2),
                       ((b >> 5) * 64 + ((b >> 1) & 3) * 16 + n) * 1024 + ((b >> 3) & 3) * 256 + lw * 64 + g * 16, 0);
    };
    auto ct_store = [&]() { st16_async(wsd, (unsigned)CT_OFF + (blockIdx.x * 4 + lw) * 1024 + lane * 16, ctv); };
    auto ct_flag = [&]() { st_flag(wsr, lane == 0 ? (blockIdx.x * 4 + lw) * FS : OOR, FC_OFF, tag); };
    const unsigned cfo = FC_OFF + ((cb * 4 + lw) * 32 + (lane & 31)) * FS;
    auto ct_ask = [&]() { cfl = ld8_async(wsd, cfo); };
    auto ct_check = [&]() { PIN(cfl); if (!__all(cfl == tag)) poll_flags(wsr, cfo, 0, tag); };
    u32x4 cf[32];
    const unsigned vcf = (cb * 4 + lw) * 32768 + lane * 16;
    auto ldc = [&](int a, int b) {
#pragma unroll
      for (int f = a; f < b; f++) cf[f] = ld16_async(wsd, vcf, (unsigned)CT_OFF + f * 1024);
    };
    auto wpiece = [&](int i, int k) { dma16(wdr, wslot(i < 9 ? i : i - 9) + lw * 12288 + k * 1024, vwd, (gstep(i) * 32 + k * 8) * 1024); };
    auto wround = [&](int r) {
#pragma unroll
      for (int q = 0; q < 12; q++) wpiece(r * 3 + (q >> 2), q & 3);
    };
    const u32x4 gr = mkdesc(p.wg, D * F * 2), ur = mkdesc(p.wu, D * F * 2);
    auto pre_1 = [&]() {
      const unsigned v = (((k0 + 2) & 7) * 64 + idx * 4 + lw) * (F * 2) + col0 * 256 + (lane < ncol * 16 ? lane : 0) * 16;
      dma16(gr, C_DUMP, v, 0);
      dma16(ur, C_DUMP, v, 0);
    };
    auto pre_w = [&]() { touch(wdr, C_DUMP, (lane < 14 ? lane : 0) * 128, (blockIdx.x * 4 + lw) * 1792); };
    constexpr int NP = 12, ALL = 10;
    const bool glu = role == 0;
    const int gi = (ct * 11) >> 5;
    const bool gkeep = glu && gi * 3 == ct;
    if (GPR == 2) __builtin_amdgcn_s_setprio(3);
    LAS char* gxa = lds + (lw + 4 * gi) * 1024 + lane * 16;
    LAS char* gwa = lds + C_GW + (vx & 0x70);
    float gsum = 0.f;
    u32x4 gx, gw;
    auto gate_rd = [&](int s) {
      gx = *(LAS u32x4*)(gxa + (s % 3) * SLOT);
      gw = *(LAS u32x4*)(gwa + (((s ^ gam) + k0) & 7) * 128);
      SCHED();
    };
    auto gate_acc = [&]() {
      if (GBR && !gkeep) return;
      if (GPR == 1) { SCHED(); __builtin_amdgcn_s_setprio(3); SCHED(); }
#pragma unroll
      for (int e = 0; e < 4; e++) {
        const f32x2 pr = (f32x2){__uint_as_float(gx[e] << 16), __uint_as_float(gx[e] & 0xffff0000u)} *
                         (f32x2){__uint_as_float(gw[e] << 16), __uint_as_float(gw[e] & 0xffff0000u)};
        gsum = __builtin_amdgcn_fdot2_f32_bf16((bf16x2){(__bf16)pr.x, (__bf16)pr.y}, (bf16x2){(__bf16)1.0f, (__bf16)1.0f}, gsum, false);
      }
      PIN(gsum);
      if (GPR == 1) { SCHED(); __builtin_amdgcn_s_setprio(0); SCHED(); }
    };
    {
      ct_load(); pre_1();
      pieces(1, 0, S1A);
      if (ssm) {
        const unsigned v = (ct * 64 + (idx & 3) * 16 + lw * 4 + g) * 1024 + ((k0 + 2) & 7) * 128 + n * 16;
        dma16(w0r, C_DUMP, v, 0);
        dma16(w1r, C_DUMP, v, 0);
        WAITV(5 + S1A);
      } else {
        WAITV(3 + S1A);
      }
      LST(1); BARRIER(); LST(2);
      tag = *(LAS u64*)(lds + C_TAG);
      gate_rd(0); pieces(1, S1A, ALL); pieces(2, 0, S2A); gate_acc(); LST(3); WAITV(S2A); LST(4); BARRIER(); LST(5);
      if (role >= 2) WAITV(0);
      gate_rd(1); ct_store(); pieces(2, S2A, ALL); pieces(3, 0, S3A); gate_acc(); LST(6); WAITV(S3A);
      if (role >= 2) WAITV(0);
      ct_flag(); LST(7); BARRIER(); LST(8);
      gate_rd(2); pieces(3, S3A, ALL);
      pieces(4, 0, ALL); gate_acc(); LST(9); WAITV(NP); LST(10); BARRIER(); LST(11);
      if (glu) ct_ask();
      gate_rd(3); pieces(5, 0, ALL); gate_acc(); LST(12); WAITV(NP);
      if (glu) { ct_check(); ldc(0, NA); }
      pre_w();
      LST(13); BARRIER(); LST(14);
      gate_rd(4); pieces(6, 0, ALL); gate_acc(); LST(15);
      if (glu) { ldc(NA, 2 * NA); WAITV(NP + 2 + 2 * NA); } else WAITV(NP + 1);
      LST(16); BARRIER(); LST(17);
      if (!glu) ct_ask();
      gate_rd(5); pieces(7, 0, ALL); gate_acc(); LST(18);
      if (glu) { ldc(2 * NA, 3 * NA); WAITV(NP + 1 + 2 * NA); } else WAITV(NP);
      LST(19); BARRIER(); LST(20);
      if (role >= 2) WAITV(0);
      if (!glu) ct_check();
      gate_rd(6); wround(0); ldc(3 * NA, 3 * NA + NB); gate_acc(); LST(21); WAITV(12 + NB); LST(22); BARRIER(); LST(23);
      gate_rd(7); wround(1); ldc(3 * NA + NB, 3 * NA + 2 * NB); LST(25); BARRIER(); LST(26);
      if (!glu) ldc(0, 3 * NA);
      gate_acc();
      gsum += __uint_as_float(__builtin_amdgcn_update_dpp(0, __float_as_uint(gsum), 0xb1, 0xf, 0xf, false));
      gsum += __uint_as_float(__builtin_amdgcn_update_dpp(0, __float_as_uint(gsum), 0x4e, 0xf, 0xf, false));
      gsum += __uint_as_float(__builtin_amdgcn_update_dpp(0, __float_as_uint(gsum), 0x141, 0xf, 0xf, false));
      WAITV(0);
      {
        const int grow = (vx >> 10) + 32 * gi;
        const bool keep = gkeep && grow >= rt * 160 && (lane & 7) == 0;
        __builtin_amdgcn_raw_buffer_store_b64((u32x2){__float_as_uint(rbf(sigmoidf_(rbf(gsum)))), (unsigned)tag}, wsr,
                                              keep ? grow * 8 : OOR, (unsigned)G_OFF, SC1);
      }
      if (blockIdx.x == 255 && lw == 3) {
#pragma unroll 1
        for (int k = 0; k < 4; k++) poll_flags(wsr, (k * 64 + lane) * 4 * FS, FC_OFF, tag);
        st_flag(wsr, lane == 0 ? (unsigned)EPOCH_OFF : OOR, 0, tag);
      }
    }
    if (GPR == 2) __builtin_amdgcn_s_setprio(0);
    lds_set(lds + C_F0 + lw * 4);
    STAMP(1);
    if (HOLD && role == 0) lds_wait(lds + C_FA + lw * 4);
    wround(2);
    ldc(3 * NA + 2 * NB, 32);
    const unsigned fs = FB_OFF + ((182 + (rb >> 2) * 8 + lw * 2 + ((lane >> 1) & 1)) * 4 + (rb & 2) + (lane & 1)) * FS;
    vh[0] = rb * 4 * HM + (56 + lw * 8) * 1024 + lane * 16;
    set_vh();
    LAS char* pb[4];
#pragma unroll
    for (int k = 0; k < 4; k++) {
      const int mt = n >> 2;
      pb[k] = lds + (mt == 0 ? wslot(5) : wslot(6) + (mt - 1) * 4096) + lw * 12288 + g * 256 + ((((n & 3) ^ g) | ((k ^ mt) << 2)) << 4);
      PIN(pb[k]);
    }
    STAMP(2);
    lds_wait(lds + C_F1 + lw * 4);
    STAMP(3);
#pragma unroll
    for (int q = 0; q < 20; q++) {
      if (q == 20 - ASKD) cfl = ld8_async(wsd, fs);
      wpiece(9 + (q >> 2), q & 3);
    }
    WAITV(ASKD);
    lds_set(lds + C_F2 + lw * 4);
    STAMP(6);
    PIN(cfl);
    if (!__all(cfl == tag)) { WAITV(0); lds_set(lds + C_F2 + lw * 4, 2u); poll_flags(wsr, fs, 0, tag); }
    asm volatile("" ::: "memory");
    STAMP(7);
    bf16x8 hf[3][4];
#pragma unroll
    for (int k = 0; k < 3; k++)
#pragma unroll
      for (int m = 0; m < 4; m++) hf[k][m] = ldh(k, m);
    WAITV(12);
    lds_set(lds + C_F2 + lw * 4, 2u);
#pragma unroll
    for (int f = 0; f < 32; f++) PIN(cf[f]);
#pragma unroll
    for (int c = 0; c < 4; c++)
#pragma unroll
      for (int m = 0; m < 4; m++) acc2[c][m] = (f32x4){0.f, 0.f, 0.f, 0.f};
    SCHED();
#pragma unroll
    for (int k = 0; k < 8; k++) {
      if (k == 3) ld_final();
      if (k == 6) prep_final();
      if (k == 7) lds_wait(lds + C_F3 + lw * 4);
#pragma unroll
      for (int m = 0; m < 4; m++) {
#pragma unroll
        for (int c = 0; c < 4; c++)
          acc2[c][m] = __builtin_amdgcn_mfma_f32_16x16x32_bf16(__builtin_bit_cast(bf16x8, cf[c * 8 + (k & 1) * 4 + (k >> 1)]), hf[k % 3][m], acc2[c][m], 0, 0, 0);
        if (k + 3 < 8) hf[k % 3][m] = ldh(k + 3, m);
        if (k == 7 && m > 0) {
#pragma unroll
          for (int c = 0; c < 4; c++) *(LAS f32x4*)(pb[(m - 1) ^ c] + c * 1024) = acc2[c][m - 1];
        }
        SCHED();
      }
    }
    STAMP(8);
#pragma unroll
    for (int c = 0; c < 4; c++) *(LAS f32x4*)(pb[3 ^ c] + c * 1024) = acc2[c][3];
  }
  BARRIER();
  STAMP(9);
  {
    const rsrc_t orc = mkrsrc(p.out, L * D * 4);
#pragma unroll
    for (int u = 0; u < 2; u++) {
      f32x4 b = *(LAS f32x4*)sb[u], a = sk[u] + *(LAS f32x4*)sa[u];
#pragma unroll
      for (int w = 1; w < 4; w++) {
        b += *(LAS f32x4*)(sb[u] + w * 12288);
        a += *(LAS f32x4*)(sa[u] + w * 12288);
      }
      const f32x4 o = gv[u] * a + omg[u] * b;
      __builtin_amdgcn_raw_buffer_store_b128(__builtin_bit_cast(u32x4, o), orc, vo + u * 8192, 0, OAUX);
    }
  }
  asm volatile(".Lgm_code_end:");
  STAMP(10);
}

extern "C" void kernel_launch(void* const* d_in, const int* in_sizes, int n_in, void* d_out, int out_size, void* d_ws,
                              size_t ws_size, hipStream_t stream) {
  Params p;
  p.x = (const u16*)d_in[0]; p.lre = (const u16*)d_in[1]; p.lim = (const u16*)d_in[2];
  p.bre = (const u16*)d_in[3]; p.bim = (const u16*)d_in[4]; p.cre = (const u16*)d_in[5]; p.cim = (const u16*)d_in[6];
  p.ldt = (const u16*)d_in[7]; p.dsk = (const u16*)d_in[8];
  p.wg = (const u16*)d_in[10]; p.wu = (const u16*)d_in[11]; p.wd = (const u16*)d_in[12]; p.gw = (const u16*)d_in[13];
  p.ws = (char*)d_ws; p.out = (float*)d_out;
  (void)hipFuncSetAttribute((const void*)k_fused, hipFuncAttributeMaxDynamicSharedMemorySize, LDS_BYTES);
  k_fused<<<256, 512, LDS_BYTES, stream>>>(p);
}
```

```cpp
#include <hip/hip_runtime.h>
#include <stdint.h>

typedef unsigned short u16;
typedef unsigned long long u64;
typedef __bf16 bf16x8 __attribute__((ext_vector_type(8)));
typedef __bf16 bf16x4 __attribute__((ext_vector_type(4)));
typedef __bf16 bf16x2 __attribute__((ext_vector_type(2)));
typedef float f32x4 __attribute__((ext_vector_type(4)));
typedef float f32x2 __attribute__((ext_vector_type(2)));
typedef unsigned u32x4 __attribute__((ext_vector_type(4)));
typedef unsigned u32x2 __attribute__((ext_vector_type(2)));
#define LAS __attribute__((address_space(3)))
#define GAS __attribute__((address_space(1)))
#define RLX_AGENT __ATOMIC_RELAXED, __HIP_MEMORY_SCOPE_AGENT
typedef GAS unsigned gu32;
typedef GAS u64 gu64;
typedef __amdgpu_buffer_rsrc_t rsrc_t;

constexpr int L = 2048, D = 512, P = 512, F = 1792;
constexpr int KS2 = 88;
constexpr int SLOT = 53248;
constexpr int LDS_CTL = 3 * SLOT;
constexpr int LDS_BYTES = 163840;
constexpr int SC1 = 16;
constexpr size_t EPOCH_OFF = 0;
constexpr size_t G_OFF = 65536;
constexpr size_t E_OFF = 131072, E_BYTES = 16 * 512 * 16;
#define FS 128
constexpr unsigned FA_OFF = 262144, FB_OFF = FA_OFF + 1024 * FS, FC_OFF = FB_OFF + 1024 * FS;
constexpr size_t CT_OFF = 1u << 20, CT_BYTES = 1u << 20;
constexpr int HM = KS2 * 1024;
constexpr size_t H_OFF = CT_OFF + CT_BYTES, H_BYTES = (size_t)128 * HM;
constexpr u64 TAG_STEP = 0x9E3779B97F4A7C15ull;
#define OAUX 18
#define PSL 2
#define MAP2 1
#define ESL 4
#define DMAUX ""
#define PWL 1
#define NOG 0
#define PREB 1
#define HOLD 10
#define WSB 251658240
#define DH 6
#define EARLYF 1
#define WATCH 0
#define ASKE 1
#define NA 3
#define NB 4
#define ASKD 8
#define PUBA 2
#define S1A 2
#define S2A 6
#define S3A 9
#define DE 12
#define DO 6
#define CHKB ((28 - DO) / 4)
#define SNC 1
#define CPOS 0
constexpr unsigned OOR = 0x40000000u;
constexpr unsigned SPIN_CAP = 1u << 18;

#define BARRIER() asm volatile("s_waitcnt lgkmcnt(0)\n\ts_barrier" ::: "memory")
#define WAITV(n) asm volatile("s_waitcnt vmcnt(%0)" ::"i"(n) : "memory")
#define WAITL0() asm volatile("s_waitcnt lgkmcnt(0)" ::: "memory")
#define SCHED() __builtin_amdgcn_sched_barrier(0)
#define PIN(x) asm volatile("" : "+v"(x))

#define STAMP0() do {} while (0)
#define STAMP(id) do {} while (0)
#define LST(id) do {} while (0)

__device__ __forceinline__ float bf2f(u16 v) { return __uint_as_float(((unsigned)v) << 16); }
__device__ __forceinline__ float rbf(float f) { return (float)(__bf16)f; }
__device__ __forceinline__ unsigned pack2(float a, float b) {
  bf16x2 v = {(__bf16)a, (__bf16)b};
  return __builtin_bit_cast(unsigned, v);
}
__device__ __forceinline__ rsrc_t mkrsrc(const void* p, unsigned bytes) {
  return __builtin_amdgcn_make_buffer_rsrc((void*)p, 0, bytes, 0x00020000);
}
__device__ __forceinline__ u32x4 mkdesc(const void* p, unsigned bytes) {
  const u64 a = (u64)p;
  return (u32x4){(unsigned)a, (unsigned)(a >> 32) & 0xffffu, bytes, 0x00020000u};
}
__device__ __forceinline__ void dma16(u32x4 d, unsigned lds_dst, unsigned voff, unsigned soff) {
  asm volatile("s_mov_b32 m0, %2\n\ts_nop 3\n\tbuffer_load_dwordx4 %0, %1, %3 offen" DMAUX " lds" ::"v"(voff), "s"(d), "s"(lds_dst), "s"(soff) : "memory");
}
__device__ __forceinline__ u32x4 ld16_async(u32x4 d, unsigned voff, unsigned soff) {
  u32x4 r;
  asm volatile("s_nop 4\n\tbuffer_load_dwordx4 %0, %1, %2, %3 offen sc1" : "=v"(r) : "v"(voff), "s"(d), "s"(soff) : "memory");
  return r;
}
__device__ __forceinline__ u64 ld8_async(u32x4 d, unsigned voff) {
  u64 r;
  asm volatile("s_nop 4\n\tbuffer_load_dwordx2 %0, %1, %2, 0 offen sc1" : "=v"(r) : "v"(voff), "s"(d) : "memory");
  return r;
}
__device__ __forceinline__ unsigned ld2_async(const void* base, unsigned voff) {
  unsigned r;
  asm volatile("s_nop 4\n\tglobal_load_ushort %0, %1, %2" : "=v"(r) : "v"(voff), "s"(base) : "memory");
  return r;
}
__device__ __forceinline__ void st16_async(u32x4 d, unsigned voff, u32x4 v) {
  asm volatile("s_nop 4\n\tbuffer_store_dwordx4 %0, %1, %2, 0 offen sc1\n\ts_nop 1" ::"v"(v), "v"(voff), "s"(d) : "memory");
}
__device__ __forceinline__ void lds_set(LAS char* p, unsigned v = 1u) { __hip_atomic_store((LAS unsigned*)p, v, __ATOMIC_RELAXED, __HIP_MEMORY_SCOPE_WORKGROUP); }
__device__ __forceinline__ void touch(u32x4 d, unsigned lds_dst, unsigned voff, unsigned soff) {
  asm volatile("s_mov_b32 m0, %2\n\ts_nop 3\n\tbuffer_load_dword %0, %1, %3 offen lds" ::"v"(voff), "s"(d), "s"(lds_dst), "s"(soff) : "memory");
}
__device__ __forceinline__ void lds_wait(LAS char* p, unsigned want = 1u) {
#pragma unroll 1
  for (unsigned spins = 0; spins < SPIN_CAP * 8; spins++) {
    if ((unsigned)__builtin_amdgcn_readfirstlane(__hip_atomic_load((LAS unsigned*)p, __ATOMIC_RELAXED, __HIP_MEMORY_SCOPE_WORKGROUP)) >= want) break;
    __builtin_amdgcn_s_sleep(1);
  }
  asm volatile("" ::: "memory");
}
__device__ __forceinline__ bf16x8 ld128(LAS char* p) { return *(LAS bf16x8*)p; }
__device__ __forceinline__ bf16x8 ldtr(LAS char* p0, LAS char* p1) {
  bf16x4 a = __builtin_amdgcn_ds_read_tr16_b64_v4bf16((LAS bf16x4*)p0);
  bf16x4 b = __builtin_amdgcn_ds_read_tr16_b64_v4bf16((LAS bf16x4*)p1);
  return __builtin_shufflevector(a, b, 0, 1, 2, 3, 4, 5, 6, 7);
}
__device__ __forceinline__ float sigmoidf_(float g) {
  return __builtin_amdgcn_rcpf(1.0f + __builtin_amdgcn_exp2f(-1.4426950408889634f * g));
}
__device__ __forceinline__ f32x2 fma2(f32x2 a, f32x2 b, f32x2 c) { return __builtin_elementwise_fma(a, b, c); }
__device__ __forceinline__ f32x2 lo2(f32x4 v) { return __builtin_shufflevector(v, v, 0, 1); }
__device__ __forceinline__ f32x2 hi2(f32x4 v) { return __builtin_shufflevector(v, v, 2, 3); }
__device__ __forceinline__ void csq(float& r, float& i) { const float t = r * r - i * i; i = 2.f * r * i; r = t; }

struct Params {
  const u16 *x, *wg, *wu, *bre, *bim, *ldt, *lre, *lim, *cre, *cim, *dsk, *wd, *gw;
  char* ws;
  float* out;
};

__device__ __forceinline__ void ssm_consts(u16 ldt, u16 lre, u16 lim, float& lr, float& li, float& cr, float& ci) {
  const float dt = rbf(expf(bf2f(ldt)));
  const float a = bf2f(lre), b = bf2f(lim);
  const float m = expf(dt * a);
  float s, c;
  sincosf(dt * b, &s, &c);
  lr = m * c;
  li = m * s;
  const float nr = lr - 1.0f, ni = li, rden = 1.0f / (a * a + b * b);
  cr = (nr * a + ni * b) * rden;
  ci = (ni * a - nr * b) * rden;
}
__device__ __forceinline__ void share2(float v, float (&o)[2]) {
  const auto t = __builtin_amdgcn_permlane16_swap(__float_as_uint(v), __float_as_uint(v), false, false);
  o[0] = __uint_as_float(t[0]); o[1] = __uint_as_float(t[1]);
}

__device__ __forceinline__ u64 ld_flag(rsrc_t r, unsigned voff, unsigned soff) {
  return __builtin_bit_cast(u64, __builtin_amdgcn_raw_buffer_load_b64(r, voff, soff, SC1));
}
__device__ __forceinline__ void st_flag(rsrc_t r, unsigned voff, unsigned soff, u64 tag) {
  __builtin_amdgcn_raw_buffer_store_b64(__builtin_bit_cast(u32x2, tag), r, voff, soff, SC1);
}
__device__ __forceinline__ void poll_flags(rsrc_t r, unsigned voff, unsigned soff, u64 tag) {
#pragma unroll 1
  for (unsigned spins = 0; spins < SPIN_CAP; spins++) {
    asm volatile("" ::: "memory");
    if (__all(ld_flag(r, voff, soff) == tag)) break;
    __builtin_amdgcn_s_sleep(PSL);
  }
  asm volatile("" ::: "memory");
}

template <bool GLU, int NR>
__device__ __forceinline__ void main_loop(LAS char* lds, int wr, int wc, int lane, f32x4 (&acc)[8][NR]) {
  const int i = lane & 15, g = lane >> 4;
  const int sw = (i >> 1) & 7;
  LAS char *resA[2], *rolA[4];
  if constexpr (GLU) {
    resA[0] = lds + (wr * 80 + i) * 128 + ((g ^ sw) << 4);
    resA[1] = lds + (wr * 80 + i) * 128 + ((g ^ sw ^ 4) << 4);
    const int q = (lane & 15) >> 2, pp = lane & 3;
    const int swz = q | ((g & 1) << 2);
#pragma unroll
    for (int c = 0; c < 4; c++) rolA[c] = lds + 20480 + (8 * g + q) * 256 + (((wc * 4 + c) ^ swz) << 5) + pp * 8;
  } else {
    resA[0] = lds + 32768 + (wc * 32 + i) * 128 + ((g ^ sw) << 4);
    resA[1] = lds + 32768 + (wc * 32 + i) * 128 + ((g ^ sw ^ 4) << 4);
    rolA[0] = lds + (wr * 128 + i) * 128 + ((g ^ sw) << 4);
    rolA[1] = lds + (wr * 128 + i) * 128 + ((g ^ sw ^ 4) << 4);
    rolA[2] = rolA[3] = lds;
  }
  auto ld_res = [&](int ks, int r) -> bf16x8 {
    if constexpr (GLU) return ld128(resA[ks] + r * 2048);
    else return ld128(resA[ks] + (r & 1) * 2048 + (r >> 1) * 8192);
  };
  auto ld_rol = [&](int j) -> bf16x8 {
    const int ks = j >> 3, c = j & 7;
    if constexpr (GLU) {
      LAS char* b = rolA[c & 3] + (c >> 2) * 16384 + ks * 8192;
      return ldtr(b, b + 1024);
    } else {
      return ld128(rolA[ks] + c * 2048);
    }
  };
#pragma unroll
  for (int c = 0; c < 8; c++)
#pragma unroll
    for (int r = 0; r < NR; r++) { acc[c][r] = (f32x4){0.f, 0.f, 0.f, 0.f}; PIN(acc[c][r]); }
  bf16x8 res[2][NR], rol[5];
  WAITV(0);
  BARRIER();
  rol[0] = ld_rol(0); SCHED(); res[0][0] = ld_res(0, 0); SCHED(); res[0][1] = ld_res(0, 1); SCHED();
  rol[1] = ld_rol(1); SCHED(); res[0][2] = ld_res(0, 2); SCHED(); res[0][3] = ld_res(0, 3); SCHED();
  rol[2] = ld_rol(2); SCHED();
  if constexpr (NR > 4) res[0][NR - 1] = ld_res(0, NR - 1);
  SCHED();
  int slot = 0;
#pragma unroll 1
  for (unsigned s = 0; s < 8; s++) {
#pragma unroll
    for (int j = 0; j < 16; j++) {
      int nds = 0;
      constexpr int RD = GLU ? 2 : 1;
      if (j == 12) {
        const int delta = (slot == 2) ? -2 * SLOT : SLOT;
        slot = (slot == 2) ? 0 : slot + 1;
        resA[0] += delta; resA[1] += delta;
        rolA[0] += delta; rolA[1] += delta;
        PIN(resA[0]); PIN(resA[1]); PIN(rolA[0]); PIN(rolA[1]);
        if constexpr (GLU) { rolA[2] += delta; rolA[3] += delta; PIN(rolA[2]); PIN(rolA[3]); }
      }
      if (j == 13) {
        BARRIER();
        SCHED();
      }
      if (j != 12) { rol[(j + 3) & 3] = ld_rol((j + 3) & 15); nds += RD; }
      if (j == 10) { rol[4] = ld_rol(15); nds += RD; }
      if (j >= 2 && j < 2 + NR) { res[1][j - 2] = ld_res(1, j - 2); nds++; }
      if (j == 13) { res[0][0] = ld_res(0, 0); res[0][1] = ld_res(0, 1); nds += 2; }
      if (j == 14) { res[0][2] = ld_res(0, 2); res[0][3] = ld_res(0, 3); nds += 2; }
      if (j == 15 && NR > 4) { res[0][NR - 1] = ld_res(0, NR - 1); nds++; }
#pragma unroll
      for (int r = 0; r < NR; r++)
        acc[j & 7][r] = __builtin_amdgcn_mfma_f32_16x16x32_bf16(rol[j == 15 ? 4 : (j & 3)], res[j >> 3][r], acc[j & 7][r], 0, 0, 0);
#pragma unroll
      for (int r = 0; r < NR; r++) {
        __builtin_amdgcn_sched_group_barrier(0x8, 1, 0);
        if (j == 12) __builtin_amdgcn_sched_group_barrier(0x2, 2, 0);
        if (r < nds) __builtin_amdgcn_sched_group_barrier(0x100, 1, 0);
      }
      SCHED();
    }
  }
}

__device__ __forceinline__ constexpr int wslot(int i) { return (i / 3 == 0 ? 2 : (i / 3 == 1 ? 0 : 1)) * SLOT + (i % 3) * 4096; }
__device__ __forceinline__ constexpr int gstep(int i) { return i < 7 ? 2 * i : 2 * i - 13; }
constexpr int C_TAG = LDS_CTL, C_F0 = LDS_CTL + 16, C_F1 = LDS_CTL + 32, C_F2 = LDS_CTL + 48, C_F3 = LDS_CTL + 64, C_FA = LDS_CTL + 80, C_FE = LDS_CTL + 96;
constexpr int C_DUMP = LDS_CTL + 1024;

__global__ __launch_bounds__(512, 2) void k_fused(Params p) {
  extern __shared__ __attribute__((aligned(16))) char smem[];
  LAS char* lds = (LAS char*)smem;
  const int tid = threadIdx.x, lane = tid & 63;
  STAMP0();
  const int wid = __builtin_amdgcn_readfirstlane(tid >> 6);
  const int xg = blockIdx.x & 7, idx = blockIdx.x >> 3;
  u64 pc;
  unsigned clen;
  asm volatile("s_getpc_b64 %0\n.Lgm_code_begin:\n\ts_mov_b32 %1, .Lgm_code_end-.Lgm_code_begin" : "=s"(pc), "=s"(clen));
  auto code_touch = [&]() { touch(mkdesc((const void*)pc, clen), C_DUMP, lane < 2 ? lane * 128 : OOR, (wid * 32 + idx) * 256); };
  if (CPOS == 0) code_touch();
  const int hh = xg & 1, cj = xg >> 1, odd = cj & 1;
  const int ncol = 4 - odd, col0 = (cj >> 1) * 7 + odd * 4;
  const int e_ = idx - 4 - odd * 8, ec = (e_ * 43) >> 8, em = e_ - ncol * 6, es = em - (hh ? ncol - 2 : 2);
  const bool ssm = e_ < 0;
  const int role = ssm ? 1 : (es < 0 ? 0 : 2 + odd);
  const int rt = ssm ? hh * 4 + (idx & 3) : (em < 0 ? hh * 7 + e_ - ec * 6 : (es < 0 ? 6 : (cj + hh) * 2 + es));
  const int ct = ssm ? (cj >> 1) * 4 + odd + (idx >> 2) : col0 + (em < 0 ? ec : hh * 2 + em);
  const int w4 = wid & 3;
  const int n = lane & 15, g = lane >> 4;
  const int k0 = (2 * cj + 4 * hh) & 7, gam = (rt + ct) & 1;
  const unsigned okb = role < 2 ? ~0u : 0u;
  const u32x4 xr = mkdesc(p.x, L * D * 2 & okb);
  const u32x4 w0r = mkdesc(ssm ? p.bre : p.wg, (ssm ? P * D * 2 : D * F * 2) & okb), w1r = mkdesc(ssm ? p.bim : p.wu, (ssm ? P * D * 2 : D * F * 2) & okb);
  unsigned vx, vw;
  {
    const int r8 = 8 * w4 + (lane >> 3), ii = r8 & 15;
    const unsigned csw = ((lane & 7) ^ (ii >> 1)) << 4;
    const int xrow = ssm ? rt * 256 + 32 * (ii >> 2) + 16 * (ii & 1) + ((ii >> 1) & 1) + (w4 & 2) : min(rt * 160, L - 160) + r8;
    vx = xrow * 1024 + csw;
    vw = ssm ? (ct * 64 + r8) * 1024 + csw : (4 * w4 + g) * (F * 2) + ct * 256 + ((n ^ ((g | ((w4 & 2) << 1)) << 1)) << 4);
  }
  const unsigned stx = ssm ? 4096 : 32768, stw = ssm ? 32768 : 16 * F * 2, sgw = ssm ? 128 : 64 * F * 2;
  const unsigned lw0 = ssm ? 32768 : 20480, lw1 = ssm ? 40960 : 36864;
  auto pieces = [&](int s, int a, int b) {
    const unsigned sg = ((s ^ gam) + k0) & 7, lb = (s % 3) * SLOT + w4 * 1024;
    auto px = [&](int i) { dma16(xr, lb + i * 4096, vx, sg * 128 + (i >> 2) * 131072 + (i & 3) * stx); };
    auto pw = [&](int k) { dma16((k & 1) ? w1r : w0r, lb + ((k & 1) ? lw1 : lw0) + (k >> 1) * 4096, vw, sg * sgw + (k >> 1) * stw); };
#pragma unroll
    for (int k = a; k < (b < 9 ? b : 9); k++) { if (k < 5) px(k); else pw(k - 5); }
    if (b > 9) {
      if (ssm) { px(5); px(6); px(7); }
      else { pw(4); pw(5); pw(6); pw(7); }
    }
  };
  unsigned c_ldt, c_lre, c_lim;
  const int chg = (ct * 64 + (wid & 1) * 32 + (lane & 31)) & (P - 1);
  if (wid < 4) {
    if (ssm) { c_ldt = ld2_async(p.ldt, chg * 2); c_lre = ld2_async(p.lre, chg * 2); c_lim = ld2_async(p.lim, chg * 2); }
    pieces(0, 0, 6);
    if (CPOS == 1) touch(mkdesc((const void*)pc, clen), C_DUMP, lane < 4 ? lane * 128 : OOR, (wid * 32 + idx) * 512);
  } else {
    pieces(0, 6, 10);
  }
  const int myid = role == 0 ? rt * 14 + ct : (ssm ? 182 + rt * 8 + ct : (role == 2 ? 246 + rt : 254 + (cj >> 1)));
  const int rb = MAP2 ? (xg >> 1) * 8 + (idx >> 2) : xg * 4 + (idx >> 3), cb = MAP2 ? (xg & 1) * 4 + (idx & 3) : idx & 7;
  const rsrc_t hr = mkrsrc(p.ws + H_OFF, (unsigned)H_BYTES), wsr = mkrsrc(p.ws, (unsigned)H_OFF);
  f32x4 acc2[4][4];
  u64 tag;
  unsigned vh[4];
  auto ldh = [&](int k, int m) -> bf16x8 {
    return __builtin_bit_cast(bf16x8, __builtin_amdgcn_raw_buffer_load_b128(hr, vh[m] + (k & 3) * 1024, (k & ~3) * 1024, SC1));
  };
  auto set_vh = [&]() { vh[1] = vh[0] + HM; vh[2] = vh[0] + 2 * HM; vh[3] = vh[0] + 3 * HM; PIN(vh[1]); PIN(vh[2]); PIN(vh[3]); };
  auto glu_flags = [&]() -> unsigned {
    const int l14 = lane < 14 ? lane : 0, j = l14 >= 7 ? l14 - 7 : l14, pr = 7 * w4 + j;
    const int mt = rb * 4 + (l14 >= 7 ? 3 : 0), om = mt < 120 ? mt / 5 : (mt < 123 ? 24 : 25);
    return (((om >> 1) * 14 + (pr >> 1)) * 4 + (om & 1) * 2 + (pr & 1)) * FS;
  };
  float gv[2];
  u32x2 gg[2], xv[2], dv;
  unsigned vg;
  auto ld_final = [&]() {
    int ln = lane;
    PIN(ln);
    const int t = rb * 64 + wid * 8 + (ln >> 4), dcol = cb * 64 + (ln & 15) * 4;
    const rsrc_t xr_ = mkrsrc(p.x, L * D * 2);
    vg = t * 8;
#pragma unroll
    for (int u = 0; u < 2; u++) {
      gg[u] = __builtin_amdgcn_raw_buffer_load_b64(wsr, vg + u * 32, (unsigned)G_OFF, SC1);
      xv[u] = __builtin_amdgcn_raw_buffer_load_b64(xr_, t * 1024 + dcol * 2, u * 4096, 0);
    }
    dv = *(const u32x2*)(p.dsk + dcol);
  };

  LAS char *sb[2], *sa[2];
  f32x4 sk[2];
  float omg[2];
  unsigned vo;
  auto prep_final = [&]() {
#pragma unroll 1
    for (unsigned spins = 0; spins < SPIN_CAP && !__all(gg[0][1] == (unsigned)tag && gg[1][1] == (unsigned)tag); spins++) {
      asm volatile("" ::: "memory");
      __builtin_amdgcn_s_sleep(PSL);
#pragma unroll
      for (int u = 0; u < 2; u++) gg[u] = __builtin_amdgcn_raw_buffer_load_b64(wsr, vg + u * 32, (unsigned)G_OFF, SC1);
    }
    gv[0] = __uint_as_float(gg[0][0]); gv[1] = __uint_as_float(gg[1][0]);
    int ln = lane;
    PIN(ln);
    const int nn = ln & 15, mt = wid >> 1;
    const int ob = mt < 3 ? wslot(0) + mt * 4096 : wslot(3), oa = mt == 0 ? wslot(5) : wslot(6) + (mt - 1) * 4096;
    vo = ((rb * 64 + wid * 8 + (ln >> 4)) * D + cb * 64 + nn * 4) * 4;
#pragma unroll
    for (int u = 0; u < 2; u++) {
      const int sl = ((wid & 1) * 8 + u * 4 + (ln >> 4)) ^ nn;
      sb[u] = lds + ob + nn * 256 + (sl << 4);
      sa[u] = lds + oa + nn * 256 + ((sl ^ (4 * mt)) << 4);
      omg[u] = rbf(1.0f - gv[u]);
#pragma unroll
      for (int e = 0; e < 4; e++) {
        const unsigned xw = xv[u][e >> 1], dw = dv[e >> 1];
        const float xf = __uint_as_float((e & 1) ? (xw & 0xffff0000u) : (xw << 16));
        const float df = __uint_as_float((e & 1) ? (dw & 0xffff0000u) : (dw << 16));
        sk[u][e] = rbf(xf * df);
      }
      PIN(sb[u]); PIN(sa[u]); PIN(omg[u]); PIN(sk[u]);
    }
    PIN(vo);
  };

  if (wid >= 4) {
    const int lw = w4;
    const u32x4 wsd = mkdesc(p.ws, (unsigned)H_OFF);
    const u32x4 wdr = mkdesc(p.wd, F * D * 2);
    const unsigned vwd = (lw * 14 * 32 + (lane >> 3)) * 1024 + cb * 128 + (((lane & 7) ^ (((lane >> 4) & 3) << 1)) << 4);
    u32x4 ctv;
    u64 cfl;
    auto ct_load = [&]() {
      const int b = blockIdx.x;
      ctv = ld16_async(mkdesc((b & 1) ? p.cim : p.cre, D * P * 2),
                       ((b >> 5) * 64 + ((b >> 1) & 3) * 16 + n) * 1024 + ((b >> 3) & 3) * 256 + lw * 64 + g * 16, 0);
    };
    auto ct_store = [&]() { st16_async(wsd, (unsigned)CT_OFF + (blockIdx.x * 4 + lw) * 1024 + lane * 16, ctv); };
    auto ct_flag = [&]() { st_flag(wsr, lane == 0 ? (blockIdx.x * 4 + lw) * FS : OOR, FC_OFF, tag); };
    const unsigned cfo = FC_OFF + ((cb * 4 + lw) * 32 + (lane & 31)) * FS;
    auto ct_ask = [&]() { cfl = ld8_async(wsd, cfo); };
    auto ct_check = [&]() { PIN(cfl); if (!__all(cfl == tag)) poll_flags(wsr, cfo, 0, tag); };
    u32x4 cf[32];
    const unsigned vcf = (cb * 4 + lw) * 32768 + lane * 16;
    auto ldc = [&](int a, int b) {
#pragma unroll
      for (int f = a; f < b; f++) cf[f] = ld16_async(wsd, vcf, (unsigned)CT_OFF + f * 1024);
    };
    auto wpiece = [&](int i, int k) { dma16(wdr, wslot(i < 9 ? i : i - 9) + lw * 12288 + k * 1024, vwd, (gstep(i) * 32 + k * 8) * 1024); };
    auto wround = [&](int r) {
#pragma unroll
      for (int q = 0; q < 12; q++) wpiece(r * 3 + (q >> 2), q & 3);
    };
    const u32x4 gr = mkdesc(p.wg, D * F * 2), ur = mkdesc(p.wu, D * F * 2);
    auto pre_1 = [&]() {
      const unsigned v = (((k0 + 2) & 7) * 64 + idx * 4 + lw) * (F * 2) + col0 * 256 + (lane < ncol * 16 ? lane : 0) * 16;
      dma16(gr, C_DUMP, v, 0);
      dma16(ur, C_DUMP, v, 0);
    };
    auto pre_w = [&]() { touch(wdr, C_DUMP, (lane < 14 ? lane : 0) * 128, (blockIdx.x * 4 + lw) * 1792); };
    constexpr int NP = 12, ALL = 10;
    if (role < 2) {
      if (PREB && ssm) {
        const unsigned v = (ct * 64 + (idx & 3) * 16 + lw * 4 + g) * 1024 + ((k0 + 2) & 7) * 128 + n * 16;
        dma16(w0r, C_DUMP, v, 0);
        dma16(w1r, C_DUMP, v, 0);
        ct_load(); pre_1(); pieces(1, 0, S1A); LST(0); WAITV(5 + S1A);
      } else {
        ct_load(); pre_1(); pieces(1, 0, S1A); LST(0); WAITV(3 + S1A);
      }
      LST(1); BARRIER(); LST(2);
      tag = *(LAS u64*)(lds + C_TAG);
      pieces(1, S1A, ALL); pieces(2, 0, S2A); LST(3); WAITV(S2A); LST(4); BARRIER(); LST(5);
      ct_store(); pieces(2, S2A, ALL); pieces(3, 0, S3A); LST(6); WAITV(S3A); ct_flag(); LST(7); BARRIER(); LST(8);
      pieces(3, S3A, ALL);
      pieces(4, 0, ALL); LST(9); WAITV(NP); LST(10); BARRIER(); LST(11);
      if (!ssm) ct_ask();
      pieces(5, 0, ALL); LST(12); WAITV(NP);
      if (!ssm) { ct_check(); ldc(0, NA); }
      pre_w();
      LST(13); BARRIER(); LST(14);
      pieces(6, 0, ALL); LST(15);
      if (!ssm) { ldc(NA, 2 * NA); WAITV(NP + 2 + 2 * NA); } else WAITV(NP + 1);
      LST(16); BARRIER(); LST(17);
      if (ssm && !SNC) ct_ask();
      pieces(7, 0, ALL); LST(18);
      if (!ssm) { ldc(2 * NA, 3 * NA); WAITV(NP + 1 + 2 * NA); } else WAITV(NP);
      LST(19); BARRIER(); LST(20);
      if (ssm && !SNC) ct_check();
      wround(0);
      if (!(SNC && ssm)) { ldc(3 * NA, 3 * NA + NB); WAITV(12 + NB); } else { WAITV(12); ct_ask(); }
      LST(22); BARRIER(); LST(23);
      wround(1);
      if (!(SNC && ssm)) ldc(3 * NA + NB, 3 * NA + 2 * NB);
      LST(25); BARRIER(); LST(26);
      if (ssm) { if (SNC) { WAITV(12); ct_check(); ldc(0, 3 * NA + 2 * NB); } else ldc(0, 3 * NA); }
      WAITV(0);
    } else {
      ct_load();
      BARRIER();
      tag = *(LAS u64*)(lds + C_TAG);
      WAITV(0); ct_store(); WAITV(0); ct_flag();
      pre_1(); pre_w();
      wround(0); wround(1);
      poll_flags(wsr, cfo, 0, tag);
      ldc(0, 3 * NA + 2 * NB);
      WAITV(0);
      if (myid == 255 && lw == 3) {
#pragma unroll 1
        for (int k = 0; k < 4; k++) poll_flags(wsr, (k * 64 + lane) * 4 * FS, FC_OFF, tag);
        st_flag(wsr, lane == 0 ? (unsigned)EPOCH_OFF : OOR, 0, tag);
      }
    }
    lds_set(lds + C_F0 + lw * 4);
    STAMP(1);
    if (role == 0) {
      if (WATCH) {
        const unsigned fa = glu_flags();
        lds_wait(lds + C_F1 + lw * 4);
        __builtin_amdgcn_s_sleep(WATCH);
#pragma unroll 1
        for (unsigned spins = 0; spins < SPIN_CAP; spins++) {
          asm volatile("" ::: "memory");
          if (__all(ld_flag(wsr, fa, FA_OFF) == tag)) { lds_set(lds + C_FE + lw * 4); break; }
          if (__builtin_amdgcn_readfirstlane(__hip_atomic_load((LAS unsigned*)(lds + C_FA + lw * 4), __ATOMIC_RELAXED, __HIP_MEMORY_SCOPE_WORKGROUP))) break;
        }
      }
      lds_wait(lds + C_FA + lw * 4);
    }
    wround(2);
    ldc(3 * NA + 2 * NB, 32);
    const unsigned fs = FB_OFF + ((182 + (rb >> 2) * 8 + lw * 2 + ((lane >> 1) & 1)) * 4 + (rb & 2) + (lane & 1)) * FS;
    vh[0] = rb * 4 * HM + (56 + lw * 8) * 1024 + lane * 16;
    set_vh();
    LAS char* pb[4];
#pragma unroll
    for (int k = 0; k < 4; k++) {
      const int mt = n >> 2;
      pb[k] = lds + (mt == 0 ? wslot(5) : wslot(6) + (mt - 1) * 4096) + lw * 12288 + g * 256 + ((((n & 3) ^ g) | ((k ^ mt) << 2)) << 4);
      PIN(pb[k]);
    }
    if (EARLYF) ld_final();
    STAMP(2);
    lds_wait(lds + C_F1 + lw * 4);
    STAMP(3);
#pragma unroll
    for (int q = 0; q < 20; q++) {
      if (q == 20 - ASKD) cfl = ld8_async(wsd, fs);
      wpiece(9 + (q >> 2), q & 3);
    }
    WAITV(ASKD);
    lds_set(lds + C_F2 + lw * 4);
    STAMP(6);
    PIN(cfl);
    if (!__all(cfl == tag)) { WAITV(0); lds_set(lds + C_F2 + lw * 4, 2u); poll_flags(wsr, fs, 0, tag); }
    asm volatile("" ::: "memory");
    STAMP(7);
    bf16x8 hf[32];
#pragma unroll
    for (int q = 0; q < DH; q++) hf[q] = ldh(q >> 2, q & 3);
    WAITV(DH);
    lds_set(lds + C_F2 + lw * 4, 2u);
    if (EARLYF) { SCHED(); prep_final(); }
#pragma unroll
    for (int f = 0; f < 32; f++) PIN(cf[f]);
#pragma unroll
    for (int c = 0; c < 4; c++)
#pragma unroll
      for (int m = 0; m < 4; m++) acc2[c][m] = (f32x4){0.f, 0.f, 0.f, 0.f};
    SCHED();
#pragma unroll
    for (int k = 0; k < 8; k++) {
      if (!EARLYF && k == 3) ld_final();
      if (!EARLYF && k == 6) prep_final();
      if (k == 7) lds_wait(lds + C_F3 + lw * 4);
#pragma unroll
      for (int m = 0; m < 4; m++) {
#pragma unroll
        for (int c = 0; c < 4; c++)
          acc2[c][m] = __builtin_amdgcn_mfma_f32_16x16x32_bf16(__builtin_bit_cast(bf16x8, cf[c * 8 + (k & 1) * 4 + (k >> 1)]), hf[k * 4 + m], acc2[c][m], 0, 0, 0);
        if (k * 4 + m + DH < 32) hf[k * 4 + m + DH] = ldh((k * 4 + m + DH) >> 2, (m + DH) & 3);
        if (k == 7 && m > 0) {
#pragma unroll
          for (int c = 0; c < 4; c++) *(LAS f32x4*)(pb[(m - 1) ^ c] + c * 1024) = acc2[c][m - 1];
        }
        SCHED();
      }
    }
    STAMP(8);
#pragma unroll
    for (int c = 0; c < 4; c++) *(LAS f32x4*)(pb[3 ^ c] + c * 1024) = acc2[c][3];
  } else {
    const int wr = wid >> 1, wc = wid & 1;
    if (wid == 0) {
      u64 w0;
      asm volatile("s_load_dwordx2 %0, %1, 0x0\n\ts_waitcnt lgkmcnt(0)" : "=s"(w0) : "s"(p.ws + EPOCH_OFF) : "memory");
      *(LAS u64*)(lds + C_TAG + lane * 8) = lane == 0 ? w0 + TAG_STEP : 0ull;
    }
    unsigned vfo = lane == 0 && role < 2 ? (myid * 4 + wid) * FS : OOR;
    unsigned vfi = glu_flags();
    vh[0] = rb * 4 * HM + wid * 14 * 1024 + lane * 16;
    LAS char* ra0 = lds + wid * 12288 + (4 * g + (n >> 2)) * 128 + (((2 * g + (n >> 3)) & 3) << 5) + (n & 3) * 8;
    PIN(vfo); PIN(vfi); PIN(vh[0]); PIN(ra0);
    bf16x8 wf[6][4];
    LAS char* ra[3][4];
    auto ldw = [&](int i, int c) -> bf16x8 {
      const int k = i < 9 ? i : i - 9;
      LAS char* a = ra[k / 3][c] + (k % 3) * 4096;
      return ldtr(a, a + 2048);
    };
    auto grab_w = [&]() {
#pragma unroll
      for (int c = 0; c < 4; c++) {
        ra[1][c] = (LAS char*)((unsigned)(size_t)ra0 ^ (c << 5));
        ra[0][c] = ra[1][c] + 2 * SLOT;
        ra[2][c] = ra[1][c] + SLOT;
      }
      lds_wait(lds + C_F0 + wid * 4);
#pragma unroll
      for (int i = 0; i < 5; i++)
#pragma unroll
        for (int c = 0; c < 4; c++) wf[i][c] = ldw(i, c);
      asm volatile("" ::: "memory");
      lds_set(lds + C_F1 + wid * 4);
    };
    if (role == 0) {
      const int mt0 = (min(rt * 160, L - 160) >> 4) + wr * 5;
      unsigned vs[5];
#pragma unroll
      for (int m = 0; m < 5; m++) { vs[m] = mt0 + m < rt * 10 ? OOR : (mt0 + m) * HM + (ct * 4 + wc * 2) * 1024 + lane * 16; PIN(vs[m]); }
      f32x4 acc[8][5];
      STAMP(1);
      main_loop<true, 5>(lds, wr, wc, lane, acc);
      STAMP(2);
      tag = *(LAS u64*)(lds + C_TAG);
#pragma unroll
      for (int h = 0; h < 2; h++) {
        if (h == 1) { SCHED(); STAMP(13); grab_w(); STAMP(14); SCHED(); }
#pragma unroll
        for (int m = 0; m < 5; m++) {
          if (h == 1 && m == PUBA) { WAITV(PUBA); st_flag(wsr, vfo, FA_OFF, tag); STAMP(3); }
          u32x4 o;
#pragma unroll
          for (int e = 0; e < 4; e++) {
            const int c = 2 * h + (e >> 1);
            const f32x2 gv_ = (e & 1) ? hi2(acc[c][m]) : lo2(acc[c][m]);
            const f32x2 uv = (e & 1) ? hi2(acc[4 + c][m]) : lo2(acc[4 + c][m]);
            f32x2 t = gv_ * -1.4426950408889634f;
            t = (f32x2){__builtin_amdgcn_exp2f(t.x), __builtin_amdgcn_exp2f(t.y)} + 1.0f;
            t = (f32x2){__builtin_amdgcn_rcpf(t.x), __builtin_amdgcn_rcpf(t.y)} * uv;
            o[e] = pack2(t.x, t.y);
          }
          __builtin_amdgcn_raw_buffer_store_b128(o, hr, vs[m] + h * 1024, 0, SC1);
        }
      }
    } else {
      if (role == 1) {
        float lr[2], li[2], cr[2], ci[2], sr[2], si[2], mr[2], mi[2], wr_[2], wi_[2], qr, qi, zr, zi;
        {
          float a2, a3;
          WAITV(6);
          PIN(c_ldt); PIN(c_lre); PIN(c_lim);
          ssm_consts(c_ldt, c_lre, c_lim, qr, qi, a2, a3);
          share2(qr, lr); share2(qi, li); share2(2.f * a2, cr); share2(2.f * a3, ci);
          csq(qr, qi); csq(qr, qi); csq(qr, qi); csq(qr, qi);
          share2(qr, sr); share2(qi, si);
          csq(qr, qi);
          share2(qr, mr); share2(qi, mi);
          csq(qr, qi); csq(qr, qi);
          zr = qr; zi = qi;
          csq(zr, zi); csq(zr, zi); csq(zr, zi);
          if (rt * 2 + wr <= 8) { zr = 0.f; zi = 0.f; }
#pragma unroll
          for (int u = 0; u < 2; u++) {
            float ar = mr[u], ai = mi[u];
            csq(ar, ai);
            const float br_ = (g & 1) ? mr[u] : 1.f, bi_ = (g & 1) ? mi[u] : 0.f;
            wr_[u] = (g & 2) ? ar * br_ - ai * bi_ : br_;
            wi_[u] = (g & 2) ? ar * bi_ + ai * br_ : bi_;
            PIN(mr[u]); PIN(mi[u]); PIN(lr[u]); PIN(li[u]); PIN(cr[u]); PIN(ci[u]); PIN(sr[u]); PIN(si[u]); PIN(wr_[u]); PIN(wi_[u]);
          }
          PIN(qr); PIN(qi); PIN(zr); PIN(zi);
        }
        const int ck = rt * 2 + wr;
        unsigned veo = lane < 32 ? (unsigned)E_OFF + (ck * 512 + chg) * 16 : OOR;
        const int c0 = ck - 1 - 8 * (lane >> 5);
        int vei = (c0 * 512 + chg) * 16, nval = min(max(c0 + 1, 0), 8);
        const unsigned tb = (wid >> 1) * 2 * SLOT + 49152 + (wid & 1) * 2048;
        LAS char* twp = lds + tb + n * 64 + ((g ^ (((n >> 1) & 1) | ((((n >> 2) ^ (n >> 3)) & 1) << 1))) << 4);
        LAS char* trp[2];
#pragma unroll
        for (int t = 0; t < 2; t++)
          trp[t] = lds + tb + (8 * g + (n >> 2)) * 64 + (((((n >> 1) ^ (n >> 3)) & 1) | (((g ^ t) & 1) << 1)) << 4) + (n & 1) * 8;
        unsigned vso[8];
#pragma unroll
        for (int k = 0; k < 8; k++) { vso[k] = (rt * 16 + wr * 8 + k) * HM + (56 + (ct * 2 + wc) * 2) * 1024 + lane * 16; PIN(vso[k]); }
        PIN(veo); PIN(vei); PIN(nval); PIN(twp); PIN(trp[0]); PIN(trp[1]);
        f32x4 acc[8][4];
        STAMP(1);
        main_loop<false, 4>(lds, wr, wc, lane, acc);
        STAMP(2);
        tag = *(LAS u64*)(lds + C_TAG);
        f32x2 er[2], ei[2];
#pragma unroll
        for (int u = 0; u < 2; u++) { er[u] = lo2(acc[0][u]); ei[u] = lo2(acc[0][2 + u]); }
#pragma unroll
        for (int st = 1; st < 16; st++)
#pragma unroll
          for (int u = 0; u < 2; u++) {
            const f32x2 l2 = {lr[u], lr[u]}, i2 = {li[u], li[u]};
            const f32x2 vr = (st & 1) ? hi2(acc[st >> 1][u]) : lo2(acc[st >> 1][u]);
            const f32x2 vi = (st & 1) ? hi2(acc[st >> 1][2 + u]) : lo2(acc[st >> 1][2 + u]);
            const f32x2 t = fma2(-i2, ei[u], fma2(l2, er[u], vr));
            ei[u] = fma2(i2, er[u], fma2(l2, ei[u], vi));
            er[u] = t;
          }
        float xs[2][4], ys[2][4];
#pragma unroll
        for (int u = 0; u < 2; u++) {
          const float a = fmaf(-si[u], ei[u].x, fmaf(sr[u], er[u].x, er[u].y)), b = fmaf(si[u], er[u].x, fmaf(sr[u], ei[u].x, ei[u].y));
          float a2[2], b2[2];
          share2(a, a2); share2(b, b2);
#pragma unroll
          for (int k = 0; k < 2; k++) {
            const auto ta = __builtin_amdgcn_permlane32_swap(__float_as_uint(a2[k]), __float_as_uint(a2[k]), false, false);
            const auto tb_ = __builtin_amdgcn_permlane32_swap(__float_as_uint(b2[k]), __float_as_uint(b2[k]), false, false);
            xs[u][k] = __uint_as_float(ta[0]); xs[u][2 + k] = __uint_as_float(ta[1]);
            ys[u][k] = __uint_as_float(tb_[0]); ys[u][2 + k] = __uint_as_float(tb_[1]);
          }
#pragma unroll
          for (int k = 1; k < 4; k++) {
            xs[u][k] = fmaf(-mi[u], ys[u][k - 1], fmaf(mr[u], xs[u][k - 1], xs[u][k]));
            ys[u][k] = fmaf(mi[u], xs[u][k - 1], fmaf(mr[u], ys[u][k - 1], ys[u][k]));
            PIN(xs[u][k]); PIN(ys[u][k]);
          }
        }
        const unsigned tg = (unsigned)tag;
        __builtin_amdgcn_raw_buffer_store_b128((u32x4){__float_as_uint(g ? xs[1][3] : xs[0][3]), tg, __float_as_uint(g ? ys[1][3] : ys[0][3]), tg},
                                               wsr, veo, 0, SC1);
        STAMP(3);
        SCHED();
        float pr[2], pi[2];
#pragma unroll
        for (int u = 0; u < 2; u++) {
          pr[u] = g == 0 ? 0.f : (g == 1 ? xs[u][0] : (g == 2 ? xs[u][1] : xs[u][2]));
          pi[u] = g == 0 ? 0.f : (g == 1 ? ys[u][0] : (g == 2 ? ys[u][1] : ys[u][2]));
          PIN(pr[u]); PIN(pi[u]);
        }
        const rsrc_t erc = mkrsrc(p.ws + E_OFF, (unsigned)E_BYTES);
        u32x4 v[8];
        auto ask = [&]() {
#pragma unroll
          for (int d = 0; d < 8; d++) v[d] = __builtin_amdgcn_raw_buffer_load_b128(erc, vei - d * 8192, 0, SC1);
        };
#pragma unroll
        for (int u = 0; u < 2; u++) {
          if (u == ASKE) { SCHED(); ask(); SCHED(); }
          PIN(cr[u]);
#pragma unroll
          for (int m = 0; m < 8; m++) {
            const f32x4 a = acc[m][u], b = acc[m][2 + u];
            acc[m][u] = cr[u] * a - ci[u] * b;
            acc[m][2 + u] = cr[u] * b + ci[u] * a;
            PIN(acc[m][u]); PIN(acc[m][2 + u]);
          }
        }
        if (ASKE >= 2) { __builtin_amdgcn_s_sleep(ASKE - 2); ask(); }
#pragma unroll 1
        for (unsigned spins = 0; spins < SPIN_CAP; spins++) {
          int cnt = 0;
#pragma unroll
          for (int d = 0; d < 8; d++) cnt += ((v[d][1] ^ tg) | (v[d][3] ^ tg)) == 0u;
          if (__all(cnt >= nval)) break;
          asm volatile("" ::: "memory");
          __builtin_amdgcn_s_sleep(ESL);
          ask();
        }
        float kr[2], ki[2];
        {
          float xr_ = __uint_as_float(v[7][0]), xi_ = __uint_as_float(v[7][2]);
#pragma unroll
          for (int d = 6; d >= 0; d--) {
            const float t = fmaf(-qi, xi_, fmaf(qr, xr_, __uint_as_float(v[d][0])));
            xi_ = fmaf(qi, xr_, fmaf(qr, xi_, __uint_as_float(v[d][2])));
            xr_ = t;
            PIN(xr_); PIN(xi_);
          }
          const auto ta = __builtin_amdgcn_permlane32_swap(__float_as_uint(xr_), __float_as_uint(xr_), false, false);
          const auto tb_ = __builtin_amdgcn_permlane32_swap(__float_as_uint(xi_), __float_as_uint(xi_), false, false);
          const float fr = __uint_as_float(ta[1]), fi = __uint_as_float(tb_[1]);
          share2(fmaf(-zi, fi, fmaf(zr, fr, __uint_as_float(ta[0]))), kr);
          share2(fmaf(zi, fr, fmaf(zr, fi, __uint_as_float(tb_[0]))), ki);
        }
        STAMP(4);
        f32x2 hr_[2], hi_[2];
#pragma unroll
        for (int u = 0; u < 2; u++) {
          f32x2 a, b;
          a.x = fmaf(-wi_[u], ki[u], fmaf(wr_[u], kr[u], pr[u])); b.x = fmaf(wi_[u], kr[u], fmaf(wr_[u], ki[u], pi[u]));
          a.y = fmaf(-si[u], b.x, fmaf(sr[u], a.x, er[u].x)); b.y = fmaf(si[u], a.x, fmaf(sr[u], b.x, ei[u].x));
          PIN(a); PIN(b);
          hr_[u] = cr[u] * a - ci[u] * b; hi_[u] = cr[u] * b + ci[u] * a;
        }
        bf16x8 tv[4];
#pragma unroll
        for (int qt = 0; qt < 5; qt++) {
          u32x4 o[2][2];
          if (qt < 4) {
#pragma unroll
            for (int mm = 0; mm < 2; mm++)
#pragma unroll
              for (int u = 0; u < 2; u++) {
                const int m = qt * 2 + mm;
                const f32x2 l2 = {lr[u], lr[u]}, i2 = {li[u], li[u]};
                const f32x2 r0_ = fma2(-i2, hi_[u], fma2(l2, hr_[u], lo2(acc[m][u])));
                const f32x2 i0_ = fma2(i2, hr_[u], fma2(l2, hi_[u], lo2(acc[m][2 + u])));
                hr_[u] = fma2(-i2, i0_, fma2(l2, r0_, hi2(acc[m][u])));
                hi_[u] = fma2(i2, r0_, fma2(l2, i0_, hi2(acc[m][2 + u])));
                o[0][u][mm] = pack2(r0_.x, hr_[u].x); o[0][u][2 + mm] = pack2(r0_.y, hr_[u].y);
                o[1][u][mm] = pack2(-i0_.x, -hi_[u].x); o[1][u][2 + mm] = pack2(-i0_.y, -hi_[u].y);
              }
          }
          SCHED();
          if (qt > 0) {
#pragma unroll
            for (int k = 0; k < 4; k++) __builtin_amdgcn_raw_buffer_store_b128(__builtin_bit_cast(u32x4, tv[k]), hr, vso[(k & 1) * 4 + qt - 1] + (k >> 1) * 1024, 0, SC1);
          }
          if (qt < 4) {
#pragma unroll
            for (int part = 0; part < 2; part++) {
              asm volatile("" ::: "memory");
              *(LAS u32x4*)twp = o[part][0];
              *(LAS u32x4*)(twp + 1024) = o[part][1];
              asm volatile("" ::: "memory");
#pragma unroll
              for (int rho = 0; rho < 2; rho++) tv[part * 2 + rho] = ldtr(trp[rho], trp[rho ^ 1] + 256);
            }
          }
          SCHED();
        }
        STAMP(13);
      } else {
        WAITV(0);
        BARRIER();
        tag = *(LAS u64*)(lds + C_TAG);
        if (role == 2) {
          const u32x4 gwv = *(const u32x4*)(p.gw + lane * 8);
          f32x2 gwf[4];
#pragma unroll
          for (int e = 0; e < 4; e++) gwf[e] = (f32x2){__uint_as_float(gwv[e] << 16), __uint_as_float(gwv[e] & 0xffff0000u)};
          const bf16x2 ones = {(__bf16)1.0f, (__bf16)1.0f};
#pragma unroll 1
          for (int it = 0; it < 4; it++) {
            const int r0 = rt * 256 + wid * 64 + it * 16;
            u32x4 xw[16];
#pragma unroll
            for (int r = 0; r < 16; r++) xw[r] = *(const u32x4*)(p.x + (size_t)(r0 + r) * D + lane * 8);
            float s[16];
#pragma unroll
            for (int r = 0; r < 16; r++) {
              s[r] = 0.f;
#pragma unroll
              for (int e = 0; e < 4; e++) {
                const unsigned w = xw[r][e];
                const f32x2 pr = (f32x2){__uint_as_float(w << 16), __uint_as_float(w & 0xffff0000u)} * gwf[e];
                const bf16x2 pb = {(__bf16)pr.x, (__bf16)pr.y};
                s[r] = __builtin_amdgcn_fdot2_f32_bf16(pb, ones, s[r], false);
              }
            }
#pragma unroll
            for (int r = 0; r < 8; r++) {
              const auto t = __builtin_amdgcn_permlane32_swap(__float_as_uint(s[r]), __float_as_uint(s[r + 8]), false, false);
              s[r] = __uint_as_float(t[0]) + __uint_as_float(t[1]);
            }
#pragma unroll
            for (int r = 0; r < 4; r++) {
              const auto t = __builtin_amdgcn_permlane16_swap(__float_as_uint(s[r]), __float_as_uint(s[r + 4]), false, false);
              s[r] = __uint_as_float(t[0]) + __uint_as_float(t[1]);
            }
#pragma unroll
            for (int r = 0; r < 4; r++) {
              s[r] += __uint_as_float(__builtin_amdgcn_update_dpp(0, __float_as_uint(s[r]), 0x128, 0xf, 0xf, false));
              s[r] += __uint_as_float(__builtin_amdgcn_update_dpp(0, __float_as_uint(s[r]), 0x124, 0xf, 0xf, false));
              s[r] += __uint_as_float(__builtin_amdgcn_update_dpp(0, __float_as_uint(s[r]), 0x122, 0xf, 0xf, false));
              s[r] += __uint_as_float(__builtin_amdgcn_update_dpp(0, __float_as_uint(s[r]), 0x121, 0xf, 0xf, false));
            }
            const float sv = (n & 2) ? ((n & 1) ? s[3] : s[2]) : ((n & 1) ? s[1] : s[0]);
            __builtin_amdgcn_raw_buffer_store_b64((u32x2){__float_as_uint(rbf(sigmoidf_(rbf(sv)))), (unsigned)tag}, wsr,
                                                  n < 4 ? (r0 + n + 4 * (g & 1) + 8 * (g >> 1)) * 8 : OOR, (unsigned)G_OFF, SC1);
          }
        }
      }
      grab_w();
    }
    STAMP(5);
    const bool early = WATCH && __builtin_amdgcn_readfirstlane(__hip_atomic_load((LAS unsigned*)(lds + C_FE + wid * 4), __ATOMIC_RELAXED, __HIP_MEMORY_SCOPE_WORKGROUP));
    lds_set(lds + C_FA + wid * 4);
    ld_final();
    set_vh();
    if (!early) {
      const bool ok = __all(ld_flag(wsr, vfi, FA_OFF) == tag);
      WAITV(0);
      st_flag(wsr, vfo, FB_OFF, tag);
      STAMP(6);
      if (!ok) poll_flags(wsr, vfi, FA_OFF, tag);
    }
    asm volatile("" ::: "memory");
    STAMP(7);
    bf16x8 hf[56];
#pragma unroll
    for (int q = 0; q < DE; q++) hf[q] = ldh(gstep(q >> 2), q & 3);
    if (early) { WAITV(DE + 5); st_flag(wsr, vfo, FB_OFF, tag); }
    SCHED(); prep_final();
#pragma unroll
    for (int c = 0; c < 4; c++)
#pragma unroll
      for (int m = 0; m < 4; m++) acc2[c][m] = (f32x4){0.f, 0.f, 0.f, 0.f};
    LAS char* pw[4];
    SCHED();
    u64 fbv = 0;
#pragma unroll
    for (int k = 0; k < 14; k++) {
      if (k == CHKB - 2) fbv = ld_flag(wsr, vfi, FB_OFF);
      if (k == CHKB) {
        if (!__all(fbv == tag)) poll_flags(wsr, vfi, FB_OFF, tag);
        asm volatile("" ::: "memory");
        STAMP(12);
      }
      if (k == 5) lds_wait(lds + C_F2 + wid * 4);
      if (k == 8) lds_wait(lds + C_F2 + wid * 4, 2u);
      if (k == 9) { lds_set(lds + C_F3 + wid * 4); if (!EARLYF) ld_final(); }
      if (k == 12) {
        if (!EARLYF) prep_final();
        int ln = lane;
        PIN(ln);
#pragma unroll
        for (int c = 0; c < 4; c++) { pw[c] = lds + wid * 12288 + c * 1024 + (ln >> 4) * 256 + (((ln & 15) ^ (4 * c + (ln >> 4))) << 4); PIN(pw[c]); }
      }
#pragma unroll
      for (int m = 0; m < 4; m++) {
        if (k + 1 >= 5 && k + 1 < 14) wf[(k + 1) % 6][m] = ldw(k + 1, m);
#pragma unroll
        for (int c = 0; c < 4; c++) acc2[c][m] = __builtin_amdgcn_mfma_f32_16x16x32_bf16(wf[k % 6][c], hf[k * 4 + m], acc2[c][m], 0, 0, 0);
        if (k * 4 + m + DE < 28) hf[k * 4 + m + DE] = ldh(gstep((k * 4 + m + DE) >> 2), (m + DE) & 3);
        if (k * 4 + m + DO >= 28 && k * 4 + m + DO < 56) hf[k * 4 + m + DO] = ldh(gstep((k * 4 + m + DO) >> 2), (m + DO) & 3);
        if (k == 13 && m > 0) {
#pragma unroll
          for (int c = 0; c < 4; c++) *(LAS f32x4*)(pw[c] + wslot(m - 1)) = acc2[c][m - 1];
        }
        SCHED();
      }
    }
    STAMP(8);
#pragma unroll
    for (int c = 0; c < 4; c++) *(LAS f32x4*)(pw[c] + wslot(3)) = acc2[c][3];
  }
  BARRIER();
  STAMP(9);
  {
    const rsrc_t orc = mkrsrc(p.out, L * D * 4);
#pragma unroll
    for (int u = 0; u < 2; u++) {
      f32x4 b = *(LAS f32x4*)sb[u], a = sk[u] + *(LAS f32x4*)sa[u];
#pragma unroll
      for (int w = 1; w < 4; w++) {
        b += *(LAS f32x4*)(sb[u] + w * 12288);
        a += *(LAS f32x4*)(sa[u] + w * 12288);
      }
      const f32x4 o = gv[u] * a + omg[u] * b;
      __builtin_amdgcn_raw_buffer_store_b128(__builtin_bit_cast(u32x4, o), orc, vo + u * 8192, 0, OAUX);
    }
  }
  asm volatile(".Lgm_code_end:");
  STAMP(10);
}

extern "C" void kernel_launch(void* const* d_in, const int* in_sizes, int n_in, void* d_out, int out_size, void* d_ws,
                              size_t ws_size, hipStream_t stream) {
  Params p;
  p.x = (const u16*)d_in[0]; p.lre = (const u16*)d_in[1]; p.lim = (const u16*)d_in[2];
  p.bre = (const u16*)d_in[3]; p.bim = (const u16*)d_in[4]; p.cre = (const u16*)d_in[5]; p.cim = (const u16*)d_in[6];
  p.ldt = (const u16*)d_in[7]; p.dsk = (const u16*)d_in[8];
  p.wg = (const u16*)d_in[10]; p.wu = (const u16*)d_in[11]; p.wd = (const u16*)d_in[12]; p.gw = (const u16*)d_in[13];
  p.ws = (char*)d_ws + WSB; p.out = (float*)d_out;
  (void)hipFuncSetAttribute((const void*)k_fused, hipFuncAttributeMaxDynamicSharedMemorySize, LDS_BYTES);
  k_fused<<<256, 512, LDS_BYTES, stream>>>(p);
}
```

```cpp
#include <hip/hip_runtime.h>
#include <stdint.h>

typedef unsigned short u16;
typedef unsigned long long u64;
typedef __bf16 bf16x8 __attribute__((ext_vector_type(8)));
typedef __bf16 bf16x4 __attribute__((ext_vector_type(4)));
typedef __bf16 bf16x2 __attribute__((ext_vector_type(2)));
typedef float f32x4 __attribute__((ext_vector_type(4)));
typedef float f32x2 __attribute__((ext_vector_type(2)));
typedef unsigned u32x4 __attribute__((ext_vector_type(4)));
typedef unsigned u32x2 __attribute__((ext_vector_type(2)));
#define LAS __attribute__((address_space(3)))
typedef __amdgpu_buffer_rsrc_t rsrc_t;

constexpr int L = 2048, D = 512, P = 512, F = 1792;
constexpr int KS2 = 88;
constexpr int SLOT = 53248;
constexpr int LDS_CTL = 3 * SLOT;
constexpr int LDS_BYTES = 163840;
constexpr int SC1 = 16, NT = 2;
constexpr size_t EPOCH_OFF = 0;
constexpr size_t G_OFF = 65536;
constexpr size_t E_OFF = 131072, E_BYTES = 16 * 512 * 16;
constexpr int FS = 128;
constexpr unsigned FA_OFF = 262144, FB_OFF = FA_OFF + 1024 * FS, FC_OFF = FB_OFF + 1024 * FS;
constexpr size_t CT_OFF = 1u << 20, CT_BYTES = 1u << 20;
constexpr int HM = KS2 * 1024;
constexpr size_t H_OFF = CT_OFF + CT_BYTES, H_BYTES = (size_t)128 * HM;
constexpr u64 TAG_STEP = 0x9E3779B97F4A7C15ull;
constexpr int NA = 3, NB = 4;
constexpr int S1A = 2, S2A = 6, S3A = 9;
constexpr int PUBA = 2;
constexpr int ASKD = 8;
constexpr int DE = 12, DO = 6, DH = 6;
constexpr int CHKB = (28 - DO) / 4;
constexpr unsigned OOR = 0x40000000u;
constexpr unsigned SPIN_CAP = 1u << 18;

#define BARRIER() asm volatile("s_waitcnt lgkmcnt(0)\n\ts_barrier" ::: "memory")
#define WAITV(n) asm volatile("s_waitcnt vmcnt(%0)" ::"i"(n) : "memory")
#define SCHED() __builtin_amdgcn_sched_barrier(0)
#define PIN(x) asm volatile("" : "+v"(x))

__device__ __forceinline__ float bf2f(u16 v) { return __uint_as_float(((unsigned)v) << 16); }
__device__ __forceinline__ float rbf(float f) { return (float)(__bf16)f; }
__device__ __forceinline__ unsigned pack2(float a, float b) {
  bf16x2 v = {(__bf16)a, (__bf16)b};
  return __builtin_bit_cast(unsigned, v);
}
__device__ __forceinline__ rsrc_t mkrsrc(const void* p, unsigned bytes) {
  return __builtin_amdgcn_make_buffer_rsrc((void*)p, 0, bytes, 0x00020000);
}
__device__ __forceinline__ u32x4 mkdesc(const void* p, unsigned bytes) {
  const u64 a = (u64)p;
  return (u32x4){(unsigned)a, (unsigned)(a >> 32) & 0xffffu, bytes, 0x00020000u};
}
__device__ __forceinline__ void dma16(u32x4 d, unsigned lds_dst, unsigned voff, unsigned soff) {
  asm volatile("s_mov_b32 m0, %2\n\ts_nop 3\n\tbuffer_load_dwordx4 %0, %1, %3 offen lds" ::"v"(voff), "s"(d), "s"(lds_dst), "s"(soff) : "memory");
}
__device__ __forceinline__ u32x4 ld16_async(u32x4 d, unsigned voff, unsigned soff) {
  u32x4 r;
  asm volatile("s_nop 4\n\tbuffer_load_dwordx4 %0, %1, %2, %3 offen sc1" : "=v"(r) : "v"(voff), "s"(d), "s"(soff) : "memory");
  return r;
}
__device__ __forceinline__ u64 ld8_async(u32x4 d, unsigned voff) {
  u64 r;
  asm volatile("s_nop 4\n\tbuffer_load_dwordx2 %0, %1, %2, 0 offen sc1" : "=v"(r) : "v"(voff), "s"(d) : "memory");
  return r;
}
__device__ __forceinline__ unsigned ld2_async(const void* base, unsigned voff) {
  unsigned r;
  asm volatile("s_nop 4\n\tglobal_load_ushort %0, %1, %2" : "=v"(r) : "v"(voff), "s"(base) : "memory");
  return r;
}
__device__ __forceinline__ void st16_async(u32x4 d, unsigned voff, u32x4 v) {
  asm volatile("s_nop 4\n\tbuffer_store_dwordx4 %0, %1, %2, 0 offen sc1\n\ts_nop 1" ::"v"(v), "v"(voff), "s"(d) : "memory");
}
__device__ __forceinline__ void lds_set(LAS char* p, unsigned v = 1u) { __hip_atomic_store((LAS unsigned*)p, v, __ATOMIC_RELAXED, __HIP_MEMORY_SCOPE_WORKGROUP); }
__device__ __forceinline__ void touch(u32x4 d, unsigned lds_dst, unsigned voff, unsigned soff) {
  asm volatile("s_mov_b32 m0, %2\n\ts_nop 3\n\tbuffer_load_dword %0, %1, %3 offen lds" ::"v"(voff), "s"(d), "s"(lds_dst), "s"(soff) : "memory");
}
__device__ __forceinline__ void lds_wait(LAS char* p, unsigned want = 1u) {
#pragma unroll 1
  for (unsigned spins = 0; spins < SPIN_CAP * 8; spins++) {
    if ((unsigned)__builtin_amdgcn_readfirstlane(__hip_atomic_load((LAS unsigned*)p, __ATOMIC_RELAXED, __HIP_MEMORY_SCOPE_WORKGROUP)) >= want) break;
    __builtin_amdgcn_s_sleep(1);
  }
  asm volatile("" ::: "memory");
}
__device__ __forceinline__ bf16x8 ld128(LAS char* p) { return *(LAS bf16x8*)p; }
__device__ __forceinline__ bf16x8 ldtr(LAS char* p0, LAS char* p1) {
  bf16x4 a = __builtin_amdgcn_ds_read_tr16_b64_v4bf16((LAS bf16x4*)p0);
  bf16x4 b = __builtin_amdgcn_ds_read_tr16_b64_v4bf16((LAS bf16x4*)p1);
  return __builtin_shufflevector(a, b, 0, 1, 2, 3, 4, 5, 6, 7);
}
__device__ __forceinline__ float sigmoidf_(float g) {
  return __builtin_amdgcn_rcpf(1.0f + __builtin_amdgcn_exp2f(-1.4426950408889634f * g));
}
__device__ __forceinline__ f32x2 fma2(f32x2 a, f32x2 b, f32x2 c) { return __builtin_elementwise_fma(a, b, c); }
__device__ __forceinline__ f32x2 lo2(f32x4 v) { return __builtin_shufflevector(v, v, 0, 1); }
__device__ __forceinline__ f32x2 hi2(f32x4 v) { return __builtin_shufflevector(v, v, 2, 3); }
__device__ __forceinline__ void csq(float& r, float& i) { const float t = r * r - i * i; i = 2.f * r * i; r = t; }

struct Params {
  const u16 *x, *wg, *wu, *bre, *bim, *ldt, *lre, *lim, *cre, *cim, *dsk, *wd, *gw;
  char* ws;
  float* out;
};

__device__ __forceinline__ void ssm_consts(u16 ldt, u16 lre, u16 lim, float& lr, float& li, float& cr, float& ci) {
  const float dt = rbf(expf(bf2f(ldt)));
  const float a = bf2f(lre), b = bf2f(lim);
  const float m = expf(dt * a);
  float s, c;
  sincosf(dt * b, &s, &c);
  lr = m * c;
  li = m * s;
  const float nr = lr - 1.0f, ni = li, rden = 1.0f / (a * a + b * b);
  cr = (nr * a + ni * b) * rden;
  ci = (ni * a - nr * b) * rden;
}
__device__ __forceinline__ void share2(float v, float (&o)[2]) {
  const auto t = __builtin_amdgcn_permlane16_swap(__float_as_uint(v), __float_as_uint(v), false, false);
  o[0] = __uint_as_float(t[0]); o[1] = __uint_as_float(t[1]);
}

__device__ __forceinline__ u64 ld_flag(rsrc_t r, unsigned voff, unsigned soff) {
  return __builtin_bit_cast(u64, __builtin_amdgcn_raw_buffer_load_b64(r, voff, soff, SC1));
}
__device__ __forceinline__ void st_flag(rsrc_t r, unsigned voff, unsigned soff, u64 tag) {
  __builtin_amdgcn_raw_buffer_store_b64(__builtin_bit_cast(u32x2, tag), r, voff, soff, SC1);
}
__device__ __forceinline__ void poll_flags(rsrc_t r, unsigned voff, unsigned soff, u64 tag) {
#pragma unroll 1
  for (unsigned spins = 0; spins < SPIN_CAP; spins++) {
    asm volatile("" ::: "memory");
    if (__all(ld_flag(r, voff, soff) == tag)) break;
    __builtin_amdgcn_s_sleep(2);
  }
  asm volatile("" ::: "memory");
}

template <bool GLU, int NR>
__device__ __forceinline__ void main_loop(LAS char* lds, int wr, int wc, int lane, f32x4 (&acc)[8][NR]) {
  const int i = lane & 15, g = lane >> 4;
  const int sw = (i >> 1) & 7;
  LAS char *resA[2], *rolA[4];
  if constexpr (GLU) {
    resA[0] = lds + (wr * 80 + i) * 128 + ((g ^ sw) << 4);
    resA[1] = lds + (wr * 80 + i) * 128 + ((g ^ sw ^ 4) << 4);
    const int q = (lane & 15) >> 2, pp = lane & 3;
    const int swz = q | ((g & 1) << 2);
#pragma unroll
    for (int c = 0; c < 4; c++) rolA[c] = lds + 20480 + (8 * g + q) * 256 + (((wc * 4 + c) ^ swz) << 5) + pp * 8;
  } else {
    resA[0] = lds + 32768 + (wc * 32 + i) * 128 + ((g ^ sw) << 4);
    resA[1] = lds + 32768 + (wc * 32 + i) * 128 + ((g ^ sw ^ 4) << 4);
    rolA[0] = lds + (wr * 128 + i) * 128 + ((g ^ sw) << 4);
    rolA[1] = lds + (wr * 128 + i) * 128 + ((g ^ sw ^ 4) << 4);
    rolA[2] = rolA[3] = lds;
  }
  auto ld_res = [&](int ks, int r) -> bf16x8 {
    if constexpr (GLU) return ld128(resA[ks] + r * 2048);
    else return ld128(resA[ks] + (r & 1) * 2048 + (r >> 1) * 8192);
  };
  auto ld_rol = [&](int j) -> bf16x8 {
    const int ks = j >> 3, c = j & 7;
    if constexpr (GLU) {
      LAS char* b = rolA[c & 3] + (c >> 2) * 16384 + ks * 8192;
      return ldtr(b, b + 1024);
    } else {
      return ld128(rolA[ks] + c * 2048);
    }
  };
#pragma unroll
  for (int c = 0; c < 8; c++)
#pragma unroll
    for (int r = 0; r < NR; r++) { acc[c][r] = (f32x4){0.f, 0.f, 0.f, 0.f}; PIN(acc[c][r]); }
  bf16x8 res[2][NR], rol[5];
  WAITV(0);
  BARRIER();
  rol[0] = ld_rol(0); SCHED(); res[0][0] = ld_res(0, 0); SCHED(); res[0][1] = ld_res(0, 1); SCHED();
  rol[1] = ld_rol(1); SCHED(); res[0][2] = ld_res(0, 2); SCHED(); res[0][3] = ld_res(0, 3); SCHED();
  rol[2] = ld_rol(2); SCHED();
  if constexpr (NR > 4) res[0][NR - 1] = ld_res(0, NR - 1);
  SCHED();
  int slot = 0;
#pragma unroll 1
  for (unsigned s = 0; s < 8; s++) {
#pragma unroll
    for (int j = 0; j < 16; j++) {
      int nds = 0;
      constexpr int RD = GLU ? 2 : 1;
      if (j == 12) {
        const int delta = (slot == 2) ? -2 * SLOT : SLOT;
        slot = (slot == 2) ? 0 : slot + 1;
        resA[0] += delta; resA[1] += delta;
        rolA[0] += delta; rolA[1] += delta;
        PIN(resA[0]); PIN(resA[1]); PIN(rolA[0]); PIN(rolA[1]);
        if constexpr (GLU) { rolA[2] += delta; rolA[3] += delta; PIN(rolA[2]); PIN(rolA[3]); }
      }
      if (j == 13) {
        BARRIER();
        SCHED();
      }
      if (j != 12) { rol[(j + 3) & 3] = ld_rol((j + 3) & 15); nds += RD; }
      if (j == 10) { rol[4] = ld_rol(15); nds += RD; }
      if (j >= 2 && j < 2 + NR) { res[1][j - 2] = ld_res(1, j - 2); nds++; }
      if (j == 13) { res[0][0] = ld_res(0, 0); res[0][1] = ld_res(0, 1); nds += 2; }
      if (j == 14) { res[0][2] = ld_res(0, 2); res[0][3] = ld_res(0, 3); nds += 2; }
      if (j == 15 && NR > 4) { res[0][NR - 1] = ld_res(0, NR - 1); nds++; }
#pragma unroll
      for (int r = 0; r < NR; r++)
        acc[j & 7][r] = __builtin_amdgcn_mfma_f32_16x16x32_bf16(rol[j == 15 ? 4 : (j & 3)], res[j >> 3][r], acc[j & 7][r], 0, 0, 0);
#pragma unroll
      for (int r = 0; r < NR; r++) {
        __builtin_amdgcn_sched_group_barrier(0x8, 1, 0);
        if (j == 12) __builtin_amdgcn_sched_group_barrier(0x2, 2, 0);
        if (r < nds) __builtin_amdgcn_sched_group_barrier(0x100, 1, 0);
      }
      SCHED();
    }
  }
}

__device__ __forceinline__ constexpr int wslot(int i) { return (i / 3 == 0 ? 2 : (i / 3 == 1 ? 0 : 1)) * SLOT + (i % 3) * 4096; }
__device__ __forceinline__ constexpr int gstep(int i) { return i < 7 ? 2 * i : 2 * i - 13; }
constexpr int C_TAG = LDS_CTL, C_F0 = LDS_CTL + 16, C_F1 = LDS_CTL + 32, C_F2 = LDS_CTL + 48, C_F3 = LDS_CTL + 64, C_FA = LDS_CTL + 80;
constexpr int C_DUMP = LDS_CTL + 1024;

__global__ __launch_bounds__(512, 2) void k_fused(Params p) {
  extern __shared__ __attribute__((aligned(16))) char smem[];
  LAS char* lds = (LAS char*)smem;
  const int tid = threadIdx.x, lane = tid & 63;
  const int wid = __builtin_amdgcn_readfirstlane(tid >> 6);
  const int xg = blockIdx.x & 7, idx = blockIdx.x >> 3;
  u64 pc;
  unsigned clen;
  asm volatile("s_getpc_b64 %0\n.Lgm_code_begin:\n\ts_mov_b32 %1, .Lgm_code_end-.Lgm_code_begin" : "=s"(pc), "=s"(clen));
  touch(mkdesc((const void*)pc, clen), C_DUMP, lane < 2 ? lane * 128 : OOR, (wid * 32 + idx) * 256);
  const int hh = xg & 1, cj = xg >> 1, odd = cj & 1;
  const int ncol = 4 - odd, col0 = (cj >> 1) * 7 + odd * 4;
  const int e_ = idx - 4 - odd * 8, ec = (e_ * 43) >> 8, em = e_ - ncol * 6, es = em - (hh ? ncol - 2 : 2);
  const bool ssm = e_ < 0;
  const int role = ssm ? 1 : (es < 0 ? 0 : 2 + odd);
  const int rt = ssm ? hh * 4 + (idx & 3) : (em < 0 ? hh * 7 + e_ - ec * 6 : (es < 0 ? 6 : (cj + hh) * 2 + es));
  const int ct = ssm ? (cj >> 1) * 4 + odd + (idx >> 2) : col0 + (em < 0 ? ec : hh * 2 + em);
  const int w4 = wid & 3;
  const int n = lane & 15, g = lane >> 4;
  const int k0 = (2 * cj + 4 * hh) & 7, gam = (rt + ct) & 1;
  const u32x4 xr = mkdesc(p.x, L * D * 2);
  const u32x4 w0r = mkdesc(ssm ? p.bre : p.wg, ssm ? P * D * 2 : D * F * 2), w1r = mkdesc(ssm ? p.bim : p.wu, ssm ? P * D * 2 : D * F * 2);
  unsigned vx, vw;
  {
    const int r8 = 8 * w4 + (lane >> 3), ii = r8 & 15;
    const unsigned csw = ((lane & 7) ^ (ii >> 1)) << 4;
    const int xrow = ssm ? rt * 256 + 32 * (ii >> 2) + 16 * (ii & 1) + ((ii >> 1) & 1) + (w4 & 2) : min(rt * 160, L - 160) + r8;
    vx = xrow * 1024 + csw;
    vw = ssm ? (ct * 64 + r8) * 1024 + csw : (4 * w4 + g) * (F * 2) + ct * 256 + ((n ^ ((g | ((w4 & 2) << 1)) << 1)) << 4);
  }
  const unsigned stx = ssm ? 4096 : 32768, stw = ssm ? 32768 : 16 * F * 2, sgw = ssm ? 128 : 64 * F * 2;
  const unsigned lw0 = ssm ? 32768 : 20480, lw1 = ssm ? 40960 : 36864;
  auto pieces = [&](int s, int a, int b) {
    const unsigned sg = ((s ^ gam) + k0) & 7, lb = (s % 3) * SLOT + w4 * 1024;
    auto px = [&](int i) { dma16(xr, lb + i * 4096, vx, sg * 128 + (i >> 2) * 131072 + (i & 3) * stx); };
    auto pw = [&](int k) { dma16((k & 1) ? w1r : w0r, lb + ((k & 1) ? lw1 : lw0) + (k >> 1) * 4096, vw, sg * sgw + (k >> 1) * stw); };
#pragma unroll
    for (int k = a; k < (b < 9 ? b : 9); k++) { if (k < 5) px(k); else pw(k - 5); }
    if (b > 9) {
      if (ssm) { px(5); px(6); px(7); }
      else { pw(4); pw(5); pw(6); pw(7); }
    }
  };
  unsigned c_ldt, c_lre, c_lim;
  if (wid == 0)
    asm volatile("s_mov_b32 m0, %2\n\ts_nop 3\n\tbuffer_load_dword %0, %1, 0 offen sc1 lds" ::"v"(lane * 4), "s"(mkdesc(p.ws + EPOCH_OFF, 8)), "s"(C_TAG) : "memory");
  const int chg = (ct * 64 + (wid & 1) * 32 + (lane & 31)) & (P - 1);
  if (role < 2) {
    if (wid < 4) {
      if (ssm) { c_ldt = ld2_async(p.ldt, chg * 2); c_lre = ld2_async(p.lre, chg * 2); c_lim = ld2_async(p.lim, chg * 2); }
      pieces(0, 0, 6);
    } else {
      pieces(0, 6, 10);
    }
  }
  const int myid = role == 0 ? rt * 14 + ct : (ssm ? 182 + rt * 8 + ct : (role == 2 ? 246 + rt : 254 + (cj >> 1)));
  const int rb = (xg >> 1) * 8 + (idx >> 2), cb = (xg & 1) * 4 + (idx & 3);
  const rsrc_t hr = mkrsrc(p.ws + H_OFF, (unsigned)H_BYTES), wsr = mkrsrc(p.ws, (unsigned)H_OFF);
  f32x4 acc2[4][4];
  u64 tag;
  unsigned vh[4];
  auto ldh = [&](int k, int m) -> bf16x8 {
    return __builtin_bit_cast(bf16x8, __builtin_amdgcn_raw_buffer_load_b128(hr, vh[m] + (k & 3) * 1024, (k & ~3) * 1024, SC1));
  };
  auto set_vh = [&]() { vh[1] = vh[0] + HM; vh[2] = vh[0] + 2 * HM; vh[3] = vh[0] + 3 * HM; PIN(vh[1]); PIN(vh[2]); PIN(vh[3]); };
  auto glu_flags = [&]() -> unsigned {
    const int l14 = lane < 14 ? lane : 0, j = l14 >= 7 ? l14 - 7 : l14, pr = 7 * w4 + j;
    const int mt = rb * 4 + (l14 >= 7 ? 3 : 0), om = mt < 120 ? mt / 5 : (mt < 123 ? 24 : 25);
    return (((om >> 1) * 14 + (pr >> 1)) * 4 + (om & 1) * 2 + (pr & 1)) * FS;
  };
  float gv[2];
  u32x2 gg[2], xv[2], dv;
  unsigned vg;
  auto ld_final = [&]() {
    int ln = lane;
    PIN(ln);
    const int t = rb * 64 + wid * 8 + (ln >> 4), dcol = cb * 64 + (ln & 15) * 4;
    const rsrc_t xr_ = mkrsrc(p.x, L * D * 2);
    vg = t * 8;
#pragma unroll
    for (int u = 0; u < 2; u++) {
      gg[u] = __builtin_amdgcn_raw_buffer_load_b64(wsr, vg + u * 32, (unsigned)G_OFF, SC1);
      xv[u] = __builtin_amdgcn_raw_buffer_load_b64(xr_, t * 1024 + dcol * 2, u * 4096, 0);
    }
    dv = *(const u32x2*)(p.dsk + dcol);
  };

  LAS char *sb[2], *sa[2];
  f32x4 sk[2];
  float omg[2];
  unsigned vo;
  auto prep_final = [&]() {
#pragma unroll 1
    for (unsigned spins = 0; spins < SPIN_CAP && !__all(gg[0][1] == (unsigned)tag && gg[1][1] == (unsigned)tag); spins++) {
      asm volatile("" ::: "memory");
      __builtin_amdgcn_s_sleep(2);
#pragma unroll
      for (int u = 0; u < 2; u++) gg[u] = __builtin_amdgcn_raw_buffer_load_b64(wsr, vg + u * 32, (unsigned)G_OFF, SC1);
    }
    gv[0] = __uint_as_float(gg[0][0]); gv[1] = __uint_as_float(gg[1][0]);
    int ln = lane;
    PIN(ln);
    const int nn = ln & 15, mt = wid >> 1;
    const int ob = mt < 3 ? wslot(0) + mt * 4096 : wslot(3), oa = mt == 0 ? wslot(5) : wslot(6) + (mt - 1) * 4096;
    vo = ((rb * 64 + wid * 8 + (ln >> 4)) * D + cb * 64 + nn * 4) * 4;
#pragma unroll
    for (int u = 0; u < 2; u++) {
      const int sl = ((wid & 1) * 8 + u * 4 + (ln >> 4)) ^ nn;
      sb[u] = lds + ob + nn * 256 + (sl << 4);
      sa[u] = lds + oa + nn * 256 + ((sl ^ (4 * mt)) << 4);
      omg[u] = rbf(1.0f - gv[u]);
#pragma unroll
      for (int e = 0; e < 4; e++) {
        const unsigned xw = xv[u][e >> 1], dw = dv[e >> 1];
        const float xf = __uint_as_float((e & 1) ? (xw & 0xffff0000u) : (xw << 16));
        const float df = __uint_as_float((e & 1) ? (dw & 0xffff0000u) : (dw << 16));
        sk[u][e] = rbf(xf * df);
      }
      PIN(sb[u]); PIN(sa[u]); PIN(omg[u]); PIN(sk[u]);
    }
    PIN(vo);
  };

  if (wid >= 4) {
    const int lw = w4;
    const u32x4 wsd = mkdesc(p.ws, (unsigned)H_OFF);
    const u32x4 wdr = mkdesc(p.wd, F * D * 2);
    const unsigned vwd = (lw * 14 * 32 + (lane >> 3)) * 1024 + cb * 128 + (((lane & 7) ^ (((lane >> 4) & 3) << 1)) << 4);
    u32x4 ctv;
    u64 cfl;
    auto ct_load = [&]() {
      const int b = blockIdx.x;
      ctv = ld16_async(mkdesc((b & 1) ? p.cim : p.cre, D * P * 2),
                       ((b >> 5) * 64 + ((b >> 1) & 3) * 16 + n) * 1024 + ((b >> 3) & 3) * 256 + lw * 64 + g * 16, 0);
    };
    auto ct_store = [&]() { st16_async(wsd, (unsigned)CT_OFF + (blockIdx.x * 4 + lw) * 1024 + lane * 16, ctv); };
    auto ct_flag = [&]() { st_flag(wsr, lane == 0 ? (blockIdx.x * 4 + lw) * FS : OOR, FC_OFF, tag); };
    const unsigned cfo = FC_OFF + ((cb * 4 + lw) * 32 + (lane & 31)) * FS;
    auto ct_ask = [&]() { cfl = ld8_async(wsd, cfo); };
    auto ct_check = [&]() { PIN(cfl); if (!__all(cfl == tag)) poll_flags(wsr, cfo, 0, tag); };
    u32x4 cf[32];
    const unsigned vcf = (cb * 4 + lw) * 32768 + lane * 16;
    auto ldc = [&](int a, int b) {
#pragma unroll
      for (int f = a; f < b; f++) cf[f] = ld16_async(wsd, vcf, (unsigned)CT_OFF + f * 1024);
    };
    auto wpiece = [&](int i, int k) { dma16(wdr, wslot(i < 9 ? i : i - 9) + lw * 12288 + k * 1024, vwd, (gstep(i) * 32 + k * 8) * 1024); };
    auto wround = [&](int r) {
#pragma unroll
      for (int q = 0; q < 12; q++) wpiece(r * 3 + (q >> 2), q & 3);
    };
    const u32x4 gr = mkdesc(p.wg, D * F * 2), ur = mkdesc(p.wu, D * F * 2);
    auto pre_1 = [&]() {
      const unsigned v = (((k0 + 2) & 7) * 64 + idx * 4 + lw) * (F * 2) + col0 * 256 + (lane < ncol * 16 ? lane : 0) * 16;
      dma16(gr, C_DUMP, v, 0);
      dma16(ur, C_DUMP, v, 0);
    };
    auto pre_w = [&]() { touch(wdr, C_DUMP, (lane < 14 ? lane : 0) * 128, (blockIdx.x * 4 + lw) * 1792); };
    constexpr int NP = 12, ALL = 10;
    if (role < 2) {
      if (ssm) {
        const unsigned v = (ct * 64 + (idx & 3) * 16 + lw * 4 + g) * 1024 + ((k0 + 2) & 7) * 128 + n * 16;
        dma16(w0r, C_DUMP, v, 0);
        dma16(w1r, C_DUMP, v, 0);
        ct_load(); pre_1(); pieces(1, 0, S1A); WAITV(5 + S1A);
      } else {
        ct_load(); pre_1(); pieces(1, 0, S1A); WAITV(3 + S1A);
      }
      BARRIER();
      tag = *(LAS u64*)(lds + C_TAG) + TAG_STEP;
      pieces(1, S1A, ALL); pieces(2, 0, S2A); WAITV(S2A);
      BARRIER();
      ct_store(); pieces(2, S2A, ALL); pieces(3, 0, S3A); WAITV(S3A); ct_flag();
      BARRIER();
      pieces(3, S3A, ALL); pieces(4, 0, ALL); WAITV(NP);
      BARRIER();
      if (!ssm) ct_ask();
      pieces(5, 0, ALL); WAITV(NP);
      if (!ssm) { ct_check(); ldc(0, NA); }
      pre_w();
      BARRIER();
      pieces(6, 0, ALL);
      if (!ssm) { ldc(NA, 2 * NA); WAITV(NP + 2 + 2 * NA); } else WAITV(NP + 1);
      BARRIER();
      pieces(7, 0, ALL);
      if (!ssm) { ldc(2 * NA, 3 * NA); WAITV(NP + 1 + 2 * NA); } else WAITV(NP);
      BARRIER();
      wround(0);
      if (!ssm) { ldc(3 * NA, 3 * NA + NB); WAITV(12 + NB); } else { WAITV(12); ct_ask(); }
      BARRIER();
      wround(1);
      if (!ssm) ldc(3 * NA + NB, 3 * NA + 2 * NB);
      BARRIER();
      if (ssm) { WAITV(12); ct_check(); ldc(0, 3 * NA + 2 * NB); }
      WAITV(0);
    } else {
      ct_load();
      BARRIER();
      tag = *(LAS u64*)(lds + C_TAG) + TAG_STEP;
      WAITV(0); ct_store(); WAITV(0); ct_flag();
      pre_1(); pre_w();
      wround(0); wround(1);
      poll_flags(wsr, cfo, 0, tag);
      ldc(0, 3 * NA + 2 * NB);
      WAITV(0);
      if (myid == 255 && lw == 3) {
#pragma unroll 1
        for (int k = 0; k < 4; k++) poll_flags(wsr, (k * 64 + lane) * 4 * FS, FC_OFF, tag);
        st_flag(wsr, lane == 0 ? (unsigned)EPOCH_OFF : OOR, 0, tag);
      }
    }
    lds_set(lds + C_F0 + lw * 4);
    if (role == 0) lds_wait(lds + C_FA + lw * 4);
    wround(2);
    ldc(3 * NA + 2 * NB, 32);
    const unsigned fs = FB_OFF + ((182 + (rb >> 2) * 8 + lw * 2 + ((lane >> 1) & 1)) * 4 + (rb & 2) + (lane & 1)) * FS;
    vh[0] = rb * 4 * HM + (56 + lw * 8) * 1024 + lane * 16;
    set_vh();
    LAS char* pb[4];
#pragma unroll
    for (int k = 0; k < 4; k++) {
      const int mt = n >> 2;
      pb[k] = lds + (mt == 0 ? wslot(5) : wslot(6) + (mt - 1) * 4096) + lw * 12288 + g * 256 + ((((n & 3) ^ g) | ((k ^ mt) << 2)) << 4);
      PIN(pb[k]);
    }
    ld_final();
    lds_wait(lds + C_F1 + lw * 4);
#pragma unroll
    for (int q = 0; q < 20; q++) {
      if (q == 20 - ASKD) cfl = ld8_async(wsd, fs);
      wpiece(9 + (q >> 2), q & 3);
    }
    WAITV(ASKD);
    lds_set(lds + C_F2 + lw * 4);
    PIN(cfl);
    if (!__all(cfl == tag)) { WAITV(0); lds_set(lds + C_F2 + lw * 4, 2u); poll_flags(wsr, fs, 0, tag); }
    asm volatile("" ::: "memory");
    bf16x8 hf[32];
#pragma unroll
    for (int q = 0; q < DH; q++) hf[q] = ldh(q >> 2, q & 3);
    WAITV(DH);
    lds_set(lds + C_F2 + lw * 4, 2u);
    SCHED();
    prep_final();
#pragma unroll
    for (int f = 0; f < 32; f++) PIN(cf[f]);
#pragma unroll
    for (int c = 0; c < 4; c++)
#pragma unroll
      for (int m = 0; m < 4; m++) acc2[c][m] = (f32x4){0.f, 0.f, 0.f, 0.f};
    SCHED();
#pragma unroll
    for (int k = 0; k < 8; k++) {
      if (k == 7) lds_wait(lds + C_F3 + lw * 4);
#pragma unroll
      for (int m = 0; m < 4; m++) {
#pragma unroll
        for (int c = 0; c < 4; c++)
          acc2[c][m] = __builtin_amdgcn_mfma_f32_16x16x32_bf16(__builtin_bit_cast(bf16x8, cf[c * 8 + (k & 1) * 4 + (k >> 1)]), hf[k * 4 + m], acc2[c][m], 0, 0, 0);
        if (k * 4 + m + DH < 32) hf[k * 4 + m + DH] = ldh((k * 4 + m + DH) >> 2, (m + DH) & 3);
        if (k == 7 && m > 0) {
#pragma unroll
          for (int c = 0; c < 4; c++) *(LAS f32x4*)(pb[(m - 1) ^ c] + c * 1024) = acc2[c][m - 1];
        }
        SCHED();
      }
    }
#pragma unroll
    for (int c = 0; c < 4; c++) *(LAS f32x4*)(pb[3 ^ c] + c * 1024) = acc2[c][3];
  } else {
    const int wr = wid >> 1, wc = wid & 1;
    unsigned vfo = lane == 0 && role < 2 ? (myid * 4 + wid) * FS : OOR;
    unsigned vfi = glu_flags();
    vh[0] = rb * 4 * HM + wid * 14 * 1024 + lane * 16;
    LAS char* ra0 = lds + wid * 12288 + (4 * g + (n >> 2)) * 128 + (((2 * g + (n >> 3)) & 3) << 5) + (n & 3) * 8;
    PIN(vfo); PIN(vfi); PIN(vh[0]); PIN(ra0);
    bf16x8 wf[6][4];
    LAS char* ra[3][4];
    auto ldw = [&](int i, int c) -> bf16x8 {
      const int k = i < 9 ? i : i - 9;
      LAS char* a = ra[k / 3][c] + (k % 3) * 4096;
      return ldtr(a, a + 2048);
    };
    auto grab_w = [&]() {
#pragma unroll
      for (int c = 0; c < 4; c++) {
        ra[1][c] = (LAS char*)((unsigned)(size_t)ra0 ^ (c << 5));
        ra[0][c] = ra[1][c] + 2 * SLOT;
        ra[2][c] = ra[1][c] + SLOT;
      }
      lds_wait(lds + C_F0 + wid * 4);
#pragma unroll
      for (int i = 0; i < 5; i++)
#pragma unroll
        for (int c = 0; c < 4; c++) wf[i][c] = ldw(i, c);
      asm volatile("" ::: "memory");
      lds_set(lds + C_F1 + wid * 4);
    };
    if (role == 0) {
      const int mt0 = (min(rt * 160, L - 160) >> 4) + wr * 5;
      unsigned vs[5];
#pragma unroll
      for (int m = 0; m < 5; m++) { vs[m] = mt0 + m < rt * 10 ? OOR : (mt0 + m) * HM + (ct * 4 + wc * 2) * 1024 + lane * 16; PIN(vs[m]); }
      f32x4 acc[8][5];
      main_loop<true, 5>(lds, wr, wc, lane, acc);
      tag = *(LAS u64*)(lds + C_TAG) + TAG_STEP;
#pragma unroll
      for (int h = 0; h < 2; h++) {
#pragma unroll
        for (int m = 0; m < 5; m++) {
          if (h == 1 && m == PUBA) { WAITV(PUBA); st_flag(wsr, vfo, FA_OFF, tag); }
          u32x4 o;
#pragma unroll
          for (int e = 0; e < 4; e++) {
            const int c = 2 * h + (e >> 1);
            const f32x2 gv_ = (e & 1) ? hi2(acc[c][m]) : lo2(acc[c][m]);
            const f32x2 uv = (e & 1) ? hi2(acc[4 + c][m]) : lo2(acc[4 + c][m]);
            f32x2 t = gv_ * -1.4426950408889634f;
            t = (f32x2){__builtin_amdgcn_exp2f(t.x), __builtin_amdgcn_exp2f(t.y)} + 1.0f;
            t = (f32x2){__builtin_amdgcn_rcpf(t.x), __builtin_amdgcn_rcpf(t.y)} * uv;
            o[e] = pack2(t.x, t.y);
          }
          __builtin_amdgcn_raw_buffer_store_b128(o, hr, vs[m] + h * 1024, 0, SC1);
          SCHED();
        }
      }
    } else {
      if (role == 1) {
        float lr[2], li[2], cr[2], ci[2], sr[2], si[2], mr[2], mi[2], wr_[2], wi_[2], qr, qi, zr, zi;
        {
          float a2, a3;
          WAITV(6);
          PIN(c_ldt); PIN(c_lre); PIN(c_lim);
          ssm_consts(c_ldt, c_lre, c_lim, qr, qi, a2, a3);
          share2(qr, lr); share2(qi, li); share2(2.f * a2, cr); share2(2.f * a3, ci);
          csq(qr, qi); csq(qr, qi); csq(qr, qi); csq(qr, qi);
          share2(qr, sr); share2(qi, si);
          csq(qr, qi);
          share2(qr, mr); share2(qi, mi);
          csq(qr, qi); csq(qr, qi);
          zr = qr; zi = qi;
          csq(zr, zi); csq(zr, zi); csq(zr, zi);
          if (rt * 2 + wr <= 8) { zr = 0.f; zi = 0.f; }
#pragma unroll
          for (int u = 0; u < 2; u++) {
            float ar = mr[u], ai = mi[u];
            csq(ar, ai);
            const float br_ = (g & 1) ? mr[u] : 1.f, bi_ = (g & 1) ? mi[u] : 0.f;
            wr_[u] = (g & 2) ? ar * br_ - ai * bi_ : br_;
            wi_[u] = (g & 2) ? ar * bi_ + ai * br_ : bi_;
            PIN(mr[u]); PIN(mi[u]); PIN(lr[u]); PIN(li[u]); PIN(cr[u]); PIN(ci[u]); PIN(sr[u]); PIN(si[u]); PIN(wr_[u]); PIN(wi_[u]);
          }
          PIN(qr); PIN(qi); PIN(zr); PIN(zi);
        }
        const int ck = rt * 2 + wr;
        unsigned veo = lane < 32 ? (unsigned)E_OFF + (ck * 512 + chg) * 16 : OOR;
        const int c0 = ck - 1 - 8 * (lane >> 5);
        int vei = (c0 * 512 + chg) * 16, nval = min(max(c0 + 1, 0), 8);
        const unsigned tb = (wid >> 1) * 2 * SLOT + 49152 + (wid & 1) * 2048;
        LAS char* twp = lds + tb + n * 64 + ((g ^ (((n >> 1) & 1) | ((((n >> 2) ^ (n >> 3)) & 1) << 1))) << 4);
        LAS char* trp[2];
#pragma unroll
        for (int t = 0; t < 2; t++)
          trp[t] = lds + tb + (8 * g + (n >> 2)) * 64 + (((((n >> 1) ^ (n >> 3)) & 1) | (((g ^ t) & 1) << 1)) << 4) + (n & 1) * 8;
        unsigned vso[8];
#pragma unroll
        for (int k = 0; k < 8; k++) { vso[k] = (rt * 16 + wr * 8 + k) * HM + (56 + (ct * 2 + wc) * 2) * 1024 + lane * 16; PIN(vso[k]); }
        PIN(veo); PIN(vei); PIN(nval); PIN(twp); PIN(trp[0]); PIN(trp[1]);
        f32x4 acc[8][4];
        main_loop<false, 4>(lds, wr, wc, lane, acc);
        tag = *(LAS u64*)(lds + C_TAG) + TAG_STEP;
        f32x2 er[2], ei[2];
#pragma unroll
        for (int u = 0; u < 2; u++) { er[u] = lo2(acc[0][u]); ei[u] = lo2(acc[0][2 + u]); }
#pragma unroll
        for (int st = 1; st < 16; st++)
#pragma unroll
          for (int u = 0; u < 2; u++) {
            const f32x2 l2 = {lr[u], lr[u]}, i2 = {li[u], li[u]};
            const f32x2 vr = (st & 1) ? hi2(acc[st >> 1][u]) : lo2(acc[st >> 1][u]);
            const f32x2 vi = (st & 1) ? hi2(acc[st >> 1][2 + u]) : lo2(acc[st >> 1][2 + u]);
            const f32x2 t = fma2(-i2, ei[u], fma2(l2, er[u], vr));
            ei[u] = fma2(i2, er[u], fma2(l2, ei[u], vi));
            er[u] = t;
          }
        float xs[2][4], ys[2][4];
#pragma unroll
        for (int u = 0; u < 2; u++) {
          const float a = fmaf(-si[u], ei[u].x, fmaf(sr[u], er[u].x, er[u].y)), b = fmaf(si[u], er[u].x, fmaf(sr[u], ei[u].x, ei[u].y));
          float a2[2], b2[2];
          share2(a, a2); share2(b, b2);
#pragma unroll
          for (int k = 0; k < 2; k++) {
            const auto ta = __builtin_amdgcn_permlane32_swap(__float_as_uint(a2[k]), __float_as_uint(a2[k]), false, false);
            const auto tb_ = __builtin_amdgcn_permlane32_swap(__float_as_uint(b2[k]), __float_as_uint(b2[k]), false, false);
            xs[u][k] = __uint_as_float(ta[0]); xs[u][2 + k] = __uint_as_float(ta[1]);
            ys[u][k] = __uint_as_float(tb_[0]); ys[u][2 + k] = __uint_as_float(tb_[1]);
          }
#pragma unroll
          for (int k = 1; k < 4; k++) {
            xs[u][k] = fmaf(-mi[u], ys[u][k - 1], fmaf(mr[u], xs[u][k - 1], xs[u][k]));
            ys[u][k] = fmaf(mi[u], xs[u][k - 1], fmaf(mr[u], ys[u][k - 1], ys[u][k]));
            PIN(xs[u][k]); PIN(ys[u][k]);
          }
        }
        const unsigned tg = (unsigned)tag;
        __builtin_amdgcn_raw_buffer_store_b128((u32x4){__float_as_uint(g ? xs[1][3] : xs[0][3]), tg, __float_as_uint(g ? ys[1][3] : ys[0][3]), tg},
                                               wsr, veo, 0, SC1);
        SCHED();
        float pr[2], pi[2];
#pragma unroll
        for (int u = 0; u < 2; u++) {
          pr[u] = g == 0 ? 0.f : (g == 1 ? xs[u][0] : (g == 2 ? xs[u][1] : xs[u][2]));
          pi[u] = g == 0 ? 0.f : (g == 1 ? ys[u][0] : (g == 2 ? ys[u][1] : ys[u][2]));
          PIN(pr[u]); PIN(pi[u]);
        }
        const rsrc_t erc = mkrsrc(p.ws + E_OFF, (unsigned)E_BYTES);
        u32x4 v[8];
        auto ask = [&]() {
#pragma unroll
          for (int d = 0; d < 8; d++) v[d] = __builtin_amdgcn_raw_buffer_load_b128(erc, vei - d * 8192, 0, SC1);
        };
#pragma unroll
        for (int u = 0; u < 2; u++) {
          if (u == 1) { SCHED(); ask(); SCHED(); }
          PIN(cr[u]);
#pragma unroll
          for (int m = 0; m < 8; m++) {
            const f32x4 a = acc[m][u], b = acc[m][2 + u];
            acc[m][u] = cr[u] * a - ci[u] * b;
            acc[m][2 + u] = cr[u] * b + ci[u] * a;
            PIN(acc[m][u]); PIN(acc[m][2 + u]);
          }
        }
#pragma unroll 1
        for (unsigned spins = 0; spins < SPIN_CAP; spins++) {
          int cnt = 0;
#pragma unroll
          for (int d = 0; d < 8; d++) cnt += ((v[d][1] ^ tg) | (v[d][3] ^ tg)) == 0u;
          if (__all(cnt >= nval)) break;
          asm volatile("" ::: "memory");
          __builtin_amdgcn_s_sleep(4);
          ask();
        }
        float kr[2], ki[2];
        {
          float xr_ = __uint_as_float(v[7][0]), xi_ = __uint_as_float(v[7][2]);
#pragma unroll
          for (int d = 6; d >= 0; d--) {
            const float t = fmaf(-qi, xi_, fmaf(qr, xr_, __uint_as_float(v[d][0])));
            xi_ = fmaf(qi, xr_, fmaf(qr, xi_, __uint_as_float(v[d][2])));
            xr_ = t;
            PIN(xr_); PIN(xi_);
          }
          const auto ta = __builtin_amdgcn_permlane32_swap(__float_as_uint(xr_), __float_as_uint(xr_), false, false);
          const auto tb_ = __builtin_amdgcn_permlane32_swap(__float_as_uint(xi_), __float_as_uint(xi_), false, false);
          const float fr = __uint_as_float(ta[1]), fi = __uint_as_float(tb_[1]);
          share2(fmaf(-zi, fi, fmaf(zr, fr, __uint_as_float(ta[0]))), kr);
          share2(fmaf(zi, fr, fmaf(zr, fi, __uint_as_float(tb_[0]))), ki);
        }
        f32x2 hr_[2], hi_[2];
#pragma unroll
        for (int u = 0; u < 2; u++) {
          f32x2 a, b;
          a.x = fmaf(-wi_[u], ki[u], fmaf(wr_[u], kr[u], pr[u])); b.x = fmaf(wi_[u], kr[u], fmaf(wr_[u], ki[u], pi[u]));
          a.y = fmaf(-si[u], b.x, fmaf(sr[u], a.x, er[u].x)); b.y = fmaf(si[u], a.x, fmaf(sr[u], b.x, ei[u].x));
          PIN(a); PIN(b);
          hr_[u] = cr[u] * a - ci[u] * b; hi_[u] = cr[u] * b + ci[u] * a;
        }
        bf16x8 tv[4];
#pragma unroll
        for (int qt = 0; qt < 5; qt++) {
          u32x4 o[2][2];
          if (qt < 4) {
#pragma unroll
            for (int mm = 0; mm < 2; mm++)
#pragma unroll
              for (int u = 0; u < 2; u++) {
                const int m = qt * 2 + mm;
                const f32x2 l2 = {lr[u], lr[u]}, i2 = {li[u], li[u]};
                const f32x2 r0_ = fma2(-i2, hi_[u], fma2(l2, hr_[u], lo2(acc[m][u])));
                const f32x2 i0_ = fma2(i2, hr_[u], fma2(l2, hi_[u], lo2(acc[m][2 + u])));
                hr_[u] = fma2(-i2, i0_, fma2(l2, r0_, hi2(acc[m][u])));
                hi_[u] = fma2(i2, r0_, fma2(l2, i0_, hi2(acc[m][2 + u])));
                o[0][u][mm] = pack2(r0_.x, hr_[u].x); o[0][u][2 + mm] = pack2(r0_.y, hr_[u].y);
                o[1][u][mm] = pack2(-i0_.x, -hi_[u].x); o[1][u][2 + mm] = pack2(-i0_.y, -hi_[u].y);
              }
          }
          SCHED();
          if (qt > 0) {
#pragma unroll
            for (int k = 0; k < 4; k++) __builtin_amdgcn_raw_buffer_store_b128(__builtin_bit_cast(u32x4, tv[k]), hr, vso[(k & 1) * 4 + qt - 1] + (k >> 1) * 1024, 0, SC1);
          }
          if (qt < 4) {
#pragma unroll
            for (int part = 0; part < 2; part++) {
              asm volatile("" ::: "memory");
              *(LAS u32x4*)twp = o[part][0];
              *(LAS u32x4*)(twp + 1024) = o[part][1];
              asm volatile("" ::: "memory");
#pragma unroll
              for (int rho = 0; rho < 2; rho++) tv[part * 2 + rho] = ldtr(trp[rho], trp[rho ^ 1] + 256);
            }
          }
          SCHED();
        }
      } else {
        WAITV(0);
        BARRIER();
        tag = *(LAS u64*)(lds + C_TAG) + TAG_STEP;
        if (role == 2) {
          const u32x4 gwv = *(const u32x4*)(p.gw + lane * 8);
          f32x2 gwf[4];
#pragma unroll
          for (int e = 0; e < 4; e++) gwf[e] = (f32x2){__uint_as_float(gwv[e] << 16), __uint_as_float(gwv[e] & 0xffff0000u)};
          const bf16x2 ones = {(__bf16)1.0f, (__bf16)1.0f};
#pragma unroll 1
          for (int it = 0; it < 4; it++) {
            const int r0 = rt * 256 + wid * 64 + it * 16;
            u32x4 xw[16];
#pragma unroll
            for (int r = 0; r < 16; r++) xw[r] = *(const u32x4*)(p.x + (size_t)(r0 + r) * D + lane * 8);
            float s[16];
#pragma unroll
            for (int r = 0; r < 16; r++) {
              s[r] = 0.f;
#pragma unroll
              for (int e = 0; e < 4; e++) {
                const unsigned w = xw[r][e];
                const f32x2 pr = (f32x2){__uint_as_float(w << 16), __uint_as_float(w & 0xffff0000u)} * gwf[e];
                const bf16x2 pb = {(__bf16)pr.x, (__bf16)pr.y};
                s[r] = __builtin_amdgcn_fdot2_f32_bf16(pb, ones, s[r], false);
              }
            }
#pragma unroll
            for (int r = 0; r < 8; r++) {
              const auto t = __builtin_amdgcn_permlane32_swap(__float_as_uint(s[r]), __float_as_uint(s[r + 8]), false, false);
              s[r] = __uint_as_float(t[0]) + __uint_as_float(t[1]);
            }
#pragma unroll
            for (int r = 0; r < 4; r++) {
              const auto t = __builtin_amdgcn_permlane16_swap(__float_as_uint(s[r]), __float_as_uint(s[r + 4]), false, false);
              s[r] = __uint_as_float(t[0]) + __uint_as_float(t[1]);
            }
#pragma unroll
            for (int r = 0; r < 4; r++) {
              s[r] += __uint_as_float(__builtin_amdgcn_update_dpp(0, __float_as_uint(s[r]), 0x128, 0xf, 0xf, false));
              s[r] += __uint_as_float(__builtin_amdgcn_update_dpp(0, __float_as_uint(s[r]), 0x124, 0xf, 0xf, false));
              s[r] += __uint_as_float(__builtin_amdgcn_update_dpp(0, __float_as_uint(s[r]), 0x122, 0xf, 0xf, false));
              s[r] += __uint_as_float(__builtin_amdgcn_update_dpp(0, __float_as_uint(s[r]), 0x121, 0xf, 0xf, false));
            }
            const float sv = (n & 2) ? ((n & 1) ? s[3] : s[2]) : ((n & 1) ? s[1] : s[0]);
            __builtin_amdgcn_raw_buffer_store_b64((u32x2){__float_as_uint(rbf(sigmoidf_(rbf(sv)))), (unsigned)tag}, wsr,
                                                  n < 4 ? (r0 + n + 4 * (g & 1) + 8 * (g >> 1)) * 8 : OOR, (unsigned)G_OFF, SC1);
          }
        }
      }
    }
    lds_set(lds + C_FA + wid * 4);
    {
      const u64 fv = ld_flag(wsr, vfi, FA_OFF);
      ld_final();
      set_vh();
      SCHED(); grab_w(); SCHED();
      WAITV(6);
      st_flag(wsr, vfo, FB_OFF, tag);
      if (!__all(fv == tag)) poll_flags(wsr, vfi, FA_OFF, tag);
    }
    asm volatile("" ::: "memory");
    bf16x8 hf[56];
#pragma unroll
    for (int q = 0; q < DE; q++) hf[q] = ldh(gstep(q >> 2), q & 3);
    SCHED(); prep_final();
#pragma unroll
    for (int c = 0; c < 4; c++)
#pragma unroll
      for (int m = 0; m < 4; m++) acc2[c][m] = (f32x4){0.f, 0.f, 0.f, 0.f};
    LAS char* pw[4];
    SCHED();
    u64 fbv = 0;
#pragma unroll
    for (int k = 0; k < 14; k++) {
      if (k == CHKB - 2) fbv = ld_flag(wsr, vfi, FB_OFF);
      if (k == CHKB) {
        if (!__all(fbv == tag)) poll_flags(wsr, vfi, FB_OFF, tag);
        asm volatile("" ::: "memory");
      }
      if (k == 5) lds_wait(lds + C_F2 + wid * 4);
      if (k == 8) lds_wait(lds + C_F2 + wid * 4, 2u);
      if (k == 9) lds_set(lds + C_F3 + wid * 4);
      if (k == 12) {
        int ln = lane;
        PIN(ln);
#pragma unroll
        for (int c = 0; c < 4; c++) { pw[c] = lds + wid * 12288 + c * 1024 + (ln >> 4) * 256 + (((ln & 15) ^ (4 * c + (ln >> 4))) << 4); PIN(pw[c]); }
      }
#pragma unroll
      for (int m = 0; m < 4; m++) {
        if (k + 1 >= 5 && k + 1 < 14) wf[(k + 1) % 6][m] = ldw(k + 1, m);
#pragma unroll
        for (int c = 0; c < 4; c++) acc2[c][m] = __builtin_amdgcn_mfma_f32_16x16x32_bf16(wf[k % 6][c], hf[k * 4 + m], acc2[c][m], 0, 0, 0);
        if (k * 4 + m + DE < 28) hf[k * 4 + m + DE] = ldh(gstep((k * 4 + m + DE) >> 2), (m + DE) & 3);
        if (k * 4 + m + DO >= 28 && k * 4 + m + DO < 56) hf[k * 4 + m + DO] = ldh(gstep((k * 4 + m + DO) >> 2), (m + DO) & 3);
        if (k == 13 && m > 0) {
#pragma unroll
          for (int c = 0; c < 4; c++) *(LAS f32x4*)(pw[c] + wslot(m - 1)) = acc2[c][m - 1];
        }
        SCHED();
      }
    }
#pragma unroll
    for (int c = 0; c < 4; c++) *(LAS f32x4*)(pw[c] + wslot(3)) = acc2[c][3];
  }
  BARRIER();
  {
    const rsrc_t orc = mkrsrc(p.out, L * D * 4);
#pragma unroll
    for (int u = 0; u < 2; u++) {
      f32x4 b = *(LAS f32x4*)sb[u], a = sk[u] + *(LAS f32x4*)sa[u];
#pragma unroll
      for (int w = 1; w < 4; w++) {
        b += *(LAS f32x4*)(sb[u] + w * 12288);
        a += *(LAS f32x4*)(sa[u] + w * 12288);
      }
      const f32x4 o = gv[u] * a + omg[u] * b;
      __builtin_amdgcn_raw_buffer_store_b128(__builtin_bit_cast(u32x4, o), orc, vo + u * 8192, 0, SC1 | NT);
    }
  }
  asm volatile(".Lgm_code_end:");
}

extern "C" void kernel_launch(void* const* d_in, const int* in_sizes, int n_in, void* d_out, int out_size, void* d_ws,
                              size_t ws_size, hipStream_t stream) {
  Params p;
  p.x = (const u16*)d_in[0]; p.lre = (const u16*)d_in[1]; p.lim = (const u16*)d_in[2];
  p.bre = (const u16*)d_in[3]; p.bim = (const u16*)d_in[4]; p.cre = (const u16*)d_in[5]; p.cim = (const u16*)d_in[6];
  p.ldt = (const u16*)d_in[7]; p.dsk = (const u16*)d_in[8];
  p.wg = (const u16*)d_in[10]; p.wu = (const u16*)d_in[11]; p.wd = (const u16*)d_in[12]; p.gw = (const u16*)d_in[13];
  p.ws = (char*)d_ws; p.out = (float*)d_out;
  (void)hipFuncSetAttribute((const void*)k_fused, hipFuncAttributeMaxDynamicSharedMemorySize, LDS_BYTES);
  k_fused<<<256, 512, LDS_BYTES, stream>>>(p);
}
```

```cpp
#include <hip/hip_runtime.h>
#include <stdint.h>

typedef unsigned short u16;
typedef unsigned long long u64;
typedef __bf16 bf16x8 __attribute__((ext_vector_type(8)));
typedef __bf16 bf16x4 __attribute__((ext_vector_type(4)));
typedef __bf16 bf16x2 __attribute__((ext_vector_type(2)));
typedef float f32x4 __attribute__((ext_vector_type(4)));
typedef float f32x2 __attribute__((ext_vector_type(2)));
typedef unsigned u32x4 __attribute__((ext_vector_type(4)));
typedef unsigned u32x2 __attribute__((ext_vector_type(2)));
#define LAS __attribute__((address_space(3)))
#define GAS __attribute__((address_space(1)))
#define RLX_AGENT __ATOMIC_RELAXED, __HIP_MEMORY_SCOPE_AGENT
typedef GAS unsigned gu32;
typedef GAS u64 gu64;
typedef __amdgpu_buffer_rsrc_t rsrc_t;

constexpr int L = 2048, D = 512, P = 512, F = 1792;
constexpr int KS2 = 88;
constexpr int SLOT = 53248;
constexpr int LDS_CTL = 3 * SLOT;
constexpr int LDS_BYTES = 163840;
constexpr int SC1 = 16;
constexpr size_t EPOCH_OFF = 0;
constexpr size_t G_OFF = 65536;
constexpr size_t E_OFF = 131072, E_BYTES = 16 * 512 * 16;
#define FS 128
constexpr unsigned FA_OFF = 262144, FB_OFF = FA_OFF + 1024 * FS, FC_OFF = FB_OFF + 1024 * FS;
constexpr size_t CT_OFF = 1u << 20, CT_BYTES = 1u << 20;
constexpr int HM = KS2 * 1024;
constexpr size_t H_OFF = CT_OFF + CT_BYTES, H_BYTES = (size_t)128 * HM;
constexpr u64 TAG_STEP = 0x9E3779B97F4A7C15ull;
#define OAUX 18
#define PSL 2
#define MAP2 1
#define ESL 4
#define DMAUX ""
#define PWL 1
#define NOG 0
#define PREB 1
#define HOLD 10
#define WSB 0
#define DH 6
#define EARLYF 1
#define WATCH 0
#define ASKE 1
#define PRE2 0
#define GRABL 1
#define NA 3
#define NB 4
#define ASKD 8
#define PUBA 2
#define S1A 2
#define S2A 6
#define S3A 9
#define DE 12
#define DO 6
#define CHKB ((28 - DO) / 4)
#define SNC 1
#define CPOS 0
constexpr unsigned OOR = 0x40000000u;
constexpr unsigned SPIN_CAP = 1u << 18;

#define BARRIER() asm volatile("s_waitcnt lgkmcnt(0)\n\ts_barrier" ::: "memory")
#define WAITV(n) asm volatile("s_waitcnt vmcnt(%0)" ::"i"(n) : "memory")
#define WAITL0() asm volatile("s_waitcnt lgkmcnt(0)" ::: "memory")
#define SCHED() __builtin_amdgcn_sched_barrier(0)
#define PIN(x) asm volatile("" : "+v"(x))

#define STAMP0() do {} while (0)
#define STAMP(id) do {} while (0)
#define LST(id) do {} while (0)

__device__ __forceinline__ float bf2f(u16 v) { return __uint_as_float(((unsigned)v) << 16); }
__device__ __forceinline__ float rbf(float f) { return (float)(__bf16)f; }
__device__ __forceinline__ unsigned pack2(float a, float b) {
  bf16x2 v = {(__bf16)a, (__bf16)b};
  return __builtin_bit_cast(unsigned, v);
}
__device__ __forceinline__ rsrc_t mkrsrc(const void* p, unsigned bytes) {
  return __builtin_amdgcn_make_buffer_rsrc((void*)p, 0, bytes, 0x00020000);
}
__device__ __forceinline__ u32x4 mkdesc(const void* p, unsigned bytes) {
  const u64 a = (u64)p;
  return (u32x4){(unsigned)a, (unsigned)(a >> 32) & 0xffffu, bytes, 0x00020000u};
}
__device__ __forceinline__ void dma16(u32x4 d, unsigned lds_dst, unsigned voff, unsigned soff) {
  asm volatile("s_mov_b32 m0, %2\n\ts_nop 3\n\tbuffer_load_dwordx4 %0, %1, %3 offen" DMAUX " lds" ::"v"(voff), "s"(d), "s"(lds_dst), "s"(soff) : "memory");
}
__device__ __forceinline__ u32x4 ld16_async(u32x4 d, unsigned voff, unsigned soff) {
  u32x4 r;
  asm volatile("s_nop 4\n\tbuffer_load_dwordx4 %0, %1, %2, %3 offen sc1" : "=v"(r) : "v"(voff), "s"(d), "s"(soff) : "memory");
  return r;
}
__device__ __forceinline__ u64 ld8_async(u32x4 d, unsigned voff) {
  u64 r;
  asm volatile("s_nop 4\n\tbuffer_load_dwordx2 %0, %1, %2, 0 offen sc1" : "=v"(r) : "v"(voff), "s"(d) : "memory");
  return r;
}
__device__ __forceinline__ unsigned ld2_async(const void* base, unsigned voff) {
  unsigned r;
  asm volatile("s_nop 4\n\tglobal_load_ushort %0, %1, %2" : "=v"(r) : "v"(voff), "s"(base) : "memory");
  return r;
}
__device__ __forceinline__ void st16_async(u32x4 d, unsigned voff, u32x4 v) {
  asm volatile("s_nop 4\n\tbuffer_store_dwordx4 %0, %1, %2, 0 offen sc1\n\ts_nop 1" ::"v"(v), "v"(voff), "s"(d) : "memory");
}
__device__ __forceinline__ void lds_set(LAS char* p, unsigned v = 1u) { __hip_atomic_store((LAS unsigned*)p, v, __ATOMIC_RELAXED, __HIP_MEMORY_SCOPE_WORKGROUP); }
__device__ __forceinline__ void touch(u32x4 d, unsigned lds_dst, unsigned voff, unsigned soff) {
  asm volatile("s_mov_b32 m0, %2\n\ts_nop 3\n\tbuffer_load_dword %0, %1, %3 offen lds" ::"v"(voff), "s"(d), "s"(lds_dst), "s"(soff) : "memory");
}
__device__ __forceinline__ void lds_wait(LAS char* p, unsigned want = 1u) {
#pragma unroll 1
  for (unsigned spins = 0; spins < SPIN_CAP * 8; spins++) {
    if ((unsigned)__builtin_amdgcn_readfirstlane(__hip_atomic_load((LAS unsigned*)p, __ATOMIC_RELAXED, __HIP_MEMORY_SCOPE_WORKGROUP)) >= want) break;
    __builtin_amdgcn_s_sleep(1);
  }
  asm volatile("" ::: "memory");
}
__device__ __forceinline__ bf16x8 ld128(LAS char* p) { return *(LAS bf16x8*)p; }
__device__ __forceinline__ bf16x8 ldtr(LAS char* p0, LAS char* p1) {
  bf16x4 a = __builtin_amdgcn_ds_read_tr16_b64_v4bf16((LAS bf16x4*)p0);
  bf16x4 b = __builtin_amdgcn_ds_read_tr16_b64_v4bf16((LAS bf16x4*)p1);
  return __builtin_shufflevector(a, b, 0, 1, 2, 3, 4, 5, 6, 7);
}
__device__ __forceinline__ float sigmoidf_(float g) {
  return __builtin_amdgcn_rcpf(1.0f + __builtin_amdgcn_exp2f(-1.4426950408889634f * g));
}
__device__ __forceinline__ f32x2 fma2(f32x2 a, f32x2 b, f32x2 c) { return __builtin_elementwise_fma(a, b, c); }
__device__ __forceinline__ f32x2 lo2(f32x4 v) { return __builtin_shufflevector(v, v, 0, 1); }
__device__ __forceinline__ f32x2 hi2(f32x4 v) { return __builtin_shufflevector(v, v, 2, 3); }
__device__ __forceinline__ void csq(float& r, float& i) { const float t = r * r - i * i; i = 2.f * r * i; r = t; }

struct Params {
  const u16 *x, *wg, *wu, *bre, *bim, *ldt, *lre, *lim, *cre, *cim, *dsk, *wd, *gw;
  char* ws;
  float* out;
};

__device__ __forceinline__ void ssm_consts(u16 ldt, u16 lre, u16 lim, float& lr, float& li, float& cr, float& ci) {
  const float dt = rbf(expf(bf2f(ldt)));
  const float a = bf2f(lre), b = bf2f(lim);
  const float m = expf(dt * a);
  float s, c;
  sincosf(dt * b, &s, &c);
  lr = m * c;
  li = m * s;
  const float nr = lr - 1.0f, ni = li, rden = 1.0f / (a * a + b * b);
  cr = (nr * a + ni * b) * rden;
  ci = (ni * a - nr * b) * rden;
}
__device__ __forceinline__ void share2(float v, float (&o)[2]) {
  const auto t = __builtin_amdgcn_permlane16_swap(__float_as_uint(v), __float_as_uint(v), false, false);
  o[0] = __uint_as_float(t[0]); o[1] = __uint_as_float(t[1]);
}

__device__ __forceinline__ u64 ld_flag(rsrc_t r, unsigned voff, unsigned soff) {
  return __builtin_bit_cast(u64, __builtin_amdgcn_raw_buffer_load_b64(r, voff, soff, SC1));
}
__device__ __forceinline__ void st_flag(rsrc_t r, unsigned voff, unsigned soff, u64 tag) {
  __builtin_amdgcn_raw_buffer_store_b64(__builtin_bit_cast(u32x2, tag), r, voff, soff, SC1);
}
__device__ __forceinline__ void poll_flags(rsrc_t r, unsigned voff, unsigned soff, u64 tag) {
#pragma unroll 1
  for (unsigned spins = 0; spins < SPIN_CAP; spins++) {
    asm volatile("" ::: "memory");
    if (__all(ld_flag(r, voff, soff) == tag)) break;
    __builtin_amdgcn_s_sleep(PSL);
  }
  asm volatile("" ::: "memory");
}

template <bool GLU, int NR>
__device__ __forceinline__ void main_loop(LAS char* lds, int wr, int wc, int lane, f32x4 (&acc)[8][NR]) {
  const int i = lane & 15, g = lane >> 4;
  const int sw = (i >> 1) & 7;
  LAS char *resA[2], *rolA[4];
  if constexpr (GLU) {
    resA[0] = lds + (wr * 80 + i) * 128 + ((g ^ sw) << 4);
    resA[1] = lds + (wr * 80 + i) * 128 + ((g ^ sw ^ 4) << 4);
    const int q = (lane & 15) >> 2, pp = lane & 3;
    const int swz = q | ((g & 1) << 2);
#pragma unroll
    for (int c = 0; c < 4; c++) rolA[c] = lds + 20480 + (8 * g + q) * 256 + (((wc * 4 + c) ^ swz) << 5) + pp * 8;
  } else {
    resA[0] = lds + 32768 + (wc * 32 + i) * 128 + ((g ^ sw) << 4);
    resA[1] = lds + 32768 + (wc * 32 + i) * 128 + ((g ^ sw ^ 4) << 4);
    rolA[0] = lds + (wr * 128 + i) * 128 + ((g ^ sw) << 4);
    rolA[1] = lds + (wr * 128 + i) * 128 + ((g ^ sw ^ 4) << 4);
    rolA[2] = rolA[3] = lds;
  }
  auto ld_res = [&](int ks, int r) -> bf16x8 {
    if constexpr (GLU) return ld128(resA[ks] + r * 2048);
    else return ld128(resA[ks] + (r & 1) * 2048 + (r >> 1) * 8192);
  };
  auto ld_rol = [&](int j) -> bf16x8 {
    const int ks = j >> 3, c = j & 7;
    if constexpr (GLU) {
      LAS char* b = rolA[c & 3] + (c >> 2) * 16384 + ks * 8192;
      return ldtr(b, b + 1024);
    } else {
      return ld128(rolA[ks] + c * 2048);
    }
  };
#pragma unroll
  for (int c = 0; c < 8; c++)
#pragma unroll
    for (int r = 0; r < NR; r++) { acc[c][r] = (f32x4){0.f, 0.f, 0.f, 0.f}; PIN(acc[c][r]); }
  bf16x8 res[2][NR], rol[5];
  WAITV(0);
  BARRIER();
  rol[0] = ld_rol(0); SCHED(); res[0][0] = ld_res(0, 0); SCHED(); res[0][1] = ld_res(0, 1); SCHED();
  rol[1] = ld_rol(1); SCHED(); res[0][2] = ld_res(0, 2); SCHED(); res[0][3] = ld_res(0, 3); SCHED();
  rol[2] = ld_rol(2); SCHED();
  if constexpr (NR > 4) res[0][NR - 1] = ld_res(0, NR - 1);
  SCHED();
  int slot = 0;
#pragma unroll 1
  for (unsigned s = 0; s < 8; s++) {
#pragma unroll
    for (int j = 0; j < 16; j++) {
      int nds = 0;
      constexpr int RD = GLU ? 2 : 1;
      if (j == 12) {
        const int delta = (slot == 2) ? -2 * SLOT : SLOT;
        slot = (slot == 2) ? 0 : slot + 1;
        resA[0] += delta; resA[1] += delta;
        rolA[0] += delta; rolA[1] += delta;
        PIN(resA[0]); PIN(resA[1]); PIN(rolA[0]); PIN(rolA[1]);
        if constexpr (GLU) { rolA[2] += delta; rolA[3] += delta; PIN(rolA[2]); PIN(rolA[3]); }
      }
      if (j == 13) {
        BARRIER();
        SCHED();
      }
      if (j != 12) { rol[(j + 3) & 3] = ld_rol((j + 3) & 15); nds += RD; }
      if (j == 10) { rol[4] = ld_rol(15); nds += RD; }
      if (j >= 2 && j < 2 + NR) { res[1][j - 2] = ld_res(1, j - 2); nds++; }
      if (j == 13) { res[0][0] = ld_res(0, 0); res[0][1] = ld_res(0, 1); nds += 2; }
      if (j == 14) { res[0][2] = ld_res(0, 2); res[0][3] = ld_res(0, 3); nds += 2; }
      if (j == 15 && NR > 4) { res[0][NR - 1] = ld_res(0, NR - 1); nds++; }
#pragma unroll
      for (int r = 0; r < NR; r++)
        acc[j & 7][r] = __builtin_amdgcn_mfma_f32_16x16x32_bf16(rol[j == 15 ? 4 : (j & 3)], res[j >> 3][r], acc[j & 7][r], 0, 0, 0);
#pragma unroll
      for (int r = 0; r < NR; r++) {
        __builtin_amdgcn_sched_group_barrier(0x8, 1, 0);
        if (j == 12) __builtin_amdgcn_sched_group_barrier(0x2, 2, 0);
        if (r < nds) __builtin_amdgcn_sched_group_barrier(0x100, 1, 0);
      }
      SCHED();
    }
  }
}

__device__ __forceinline__ constexpr int wslot(int i) { return (i / 3 == 0 ? 2 : (i / 3 == 1 ? 0 : 1)) * SLOT + (i % 3) * 4096; }
__device__ __forceinline__ constexpr int gstep(int i) { return i < 7 ? 2 * i : 2 * i - 13; }
constexpr int C_TAG = LDS_CTL, C_F0 = LDS_CTL + 16, C_F1 = LDS_CTL + 32, C_F2 = LDS_CTL + 48, C_F3 = LDS_CTL + 64, C_FA = LDS_CTL + 80, C_FE = LDS_CTL + 96;
constexpr int C_DUMP = LDS_CTL + 1024;

__global__ __launch_bounds__(512, 2) void k_fused(Params p) {
  extern __shared__ __attribute__((aligned(16))) char smem[];
  LAS char* lds = (LAS char*)smem;
  const int tid = threadIdx.x, lane = tid & 63;
  STAMP0();
  const int wid = __builtin_amdgcn_readfirstlane(tid >> 6);
  const int xg = blockIdx.x & 7, idx = blockIdx.x >> 3;
  u64 pc;
  unsigned clen;
  asm volatile("s_getpc_b64 %0\n.Lgm_code_begin:\n\ts_mov_b32 %1, .Lgm_code_end-.Lgm_code_begin" : "=s"(pc), "=s"(clen));
  auto code_touch = [&]() { touch(mkdesc((const void*)pc, clen), C_DUMP, lane < 2 ? lane * 128 : OOR, (wid * 32 + idx) * 256); };
  if (CPOS == 0) code_touch();
  const int hh = xg & 1, cj = xg >> 1, odd = cj & 1;
  const int ncol = 4 - odd, col0 = (cj >> 1) * 7 + odd * 4;
  const int e_ = idx - 4 - odd * 8, ec = (e_ * 43) >> 8, em = e_ - ncol * 6, es = em - (hh ? ncol - 2 : 2);
  const bool ssm = e_ < 0;
  const int role = ssm ? 1 : (es < 0 ? 0 : 2 + odd);
  const int rt = ssm ? hh * 4 + (idx & 3) : (em < 0 ? hh * 7 + e_ - ec * 6 : (es < 0 ? 6 : (cj + hh) * 2 + es));
  const int ct = ssm ? (cj >> 1) * 4 + odd + (idx >> 2) : col0 + (em < 0 ? ec : hh * 2 + em);
  const int w4 = wid & 3;
  const int n = lane & 15, g = lane >> 4;
  const int k0 = (2 * cj + 4 * hh) & 7, gam = (rt + ct) & 1;
  const unsigned okb = role < 2 ? ~0u : 0u;
  const u32x4 xr = mkdesc(p.x, L * D * 2 & okb);
  const u32x4 w0r = mkdesc(ssm ? p.bre : p.wg, (ssm ? P * D * 2 : D * F * 2) & okb), w1r = mkdesc(ssm ? p.bim : p.wu, (ssm ? P * D * 2 : D * F * 2) & okb);
  unsigned vx, vw;
  {
    const int r8 = 8 * w4 + (lane >> 3), ii = r8 & 15;
    const unsigned csw = ((lane & 7) ^ (ii >> 1)) << 4;
    const int xrow = ssm ? rt * 256 + 32 * (ii >> 2) + 16 * (ii & 1) + ((ii >> 1) & 1) + (w4 & 2) : min(rt * 160, L - 160) + r8;
    vx = xrow * 1024 + csw;
    vw = ssm ? (ct * 64 + r8) * 1024 + csw : (4 * w4 + g) * (F * 2) + ct * 256 + ((n ^ ((g | ((w4 & 2) << 1)) << 1)) << 4);
  }
  const unsigned stx = ssm ? 4096 : 32768, stw = ssm ? 32768 : 16 * F * 2, sgw = ssm ? 128 : 64 * F * 2;
  const unsigned lw0 = ssm ? 32768 : 20480, lw1 = ssm ? 40960 : 36864;
  auto pieces = [&](int s, int a, int b) {
    const unsigned sg = ((s ^ gam) + k0) & 7, lb = (s % 3) * SLOT + w4 * 1024;
    auto px = [&](int i) { dma16(xr, lb + i * 4096, vx, sg * 128 + (i >> 2) * 131072 + (i & 3) * stx); };
    auto pw = [&](int k) { dma16((k & 1) ? w1r : w0r, lb + ((k & 1) ? lw1 : lw0) + (k >> 1) * 4096, vw, sg * sgw + (k >> 1) * stw); };
#pragma unroll
    for (int k = a; k < (b < 9 ? b : 9); k++) { if (k < 5) px(k); else pw(k - 5); }
    if (b > 9) {
      if (ssm) { px(5); px(6); px(7); }
      else { pw(4); pw(5); pw(6); pw(7); }
    }
  };
  unsigned c_ldt, c_lre, c_lim;
  const int chg = (ct * 64 + (wid & 1) * 32 + (lane & 31)) & (P - 1);
  if (wid < 4) {
    if (ssm) { c_ldt = ld2_async(p.ldt, chg * 2); c_lre = ld2_async(p.lre, chg * 2); c_lim = ld2_async(p.lim, chg * 2); }
    pieces(0, 0, 6);
    if (CPOS == 1) touch(mkdesc((const void*)pc, clen), C_DUMP, lane < 4 ? lane * 128 : OOR, (wid * 32 + idx) * 512);
  } else {
    pieces(0, 6, 10);
  }
  const int myid = role == 0 ? rt * 14 + ct : (ssm ? 182 + rt * 8 + ct : (role == 2 ? 246 + rt : 254 + (cj >> 1)));
  const int rb = MAP2 ? (xg >> 1) * 8 + (idx >> 2) : xg * 4 + (idx >> 3), cb = MAP2 ? (xg & 1) * 4 + (idx & 3) : idx & 7;
  const rsrc_t hr = mkrsrc(p.ws + H_OFF, (unsigned)H_BYTES), wsr = mkrsrc(p.ws, (unsigned)H_OFF);
  f32x4 acc2[4][4];
  u64 tag;
  unsigned vh[4];
  auto ldh = [&](int k, int m) -> bf16x8 {
    return __builtin_bit_cast(bf16x8, __builtin_amdgcn_raw_buffer_load_b128(hr, vh[m] + (k & 3) * 1024, (k & ~3) * 1024, SC1));
  };
  auto set_vh = [&]() { vh[1] = vh[0] + HM; vh[2] = vh[0] + 2 * HM; vh[3] = vh[0] + 3 * HM; PIN(vh[1]); PIN(vh[2]); PIN(vh[3]); };
  auto glu_flags = [&]() -> unsigned {
    const int l14 = lane < 14 ? lane : 0, j = l14 >= 7 ? l14 - 7 : l14, pr = 7 * w4 + j;
    const int mt = rb * 4 + (l14 >= 7 ? 3 : 0), om = mt < 120 ? mt / 5 : (mt < 123 ? 24 : 25);
    return (((om >> 1) * 14 + (pr >> 1)) * 4 + (om & 1) * 2 + (pr & 1)) * FS;
  };
  float gv[2];
  u32x2 gg[2], xv[2], dv;
  unsigned vg;
  auto ld_final = [&]() {
    int ln = lane;
    PIN(ln);
    const int t = rb * 64 + wid * 8 + (ln >> 4), dcol = cb * 64 + (ln & 15) * 4;
    const rsrc_t xr_ = mkrsrc(p.x, L * D * 2);
    vg = t * 8;
#pragma unroll
    for (int u = 0; u < 2; u++) {
      gg[u] = __builtin_amdgcn_raw_buffer_load_b64(wsr, vg + u * 32, (unsigned)G_OFF, SC1);
      xv[u] = __builtin_amdgcn_raw_buffer_load_b64(xr_, t * 1024 + dcol * 2, u * 4096, 0);
    }
    dv = *(const u32x2*)(p.dsk + dcol);
  };

  LAS char *sb[2], *sa[2];
  f32x4 sk[2];
  float omg[2];
  unsigned vo;
  auto prep_final = [&]() {
#pragma unroll 1
    for (unsigned spins = 0; spins < SPIN_CAP && !__all(gg[0][1] == (unsigned)tag && gg[1][1] == (unsigned)tag); spins++) {
      asm volatile("" ::: "memory");
      __builtin_amdgcn_s_sleep(PSL);
#pragma unroll
      for (int u = 0; u < 2; u++) gg[u] = __builtin_amdgcn_raw_buffer_load_b64(wsr, vg + u * 32, (unsigned)G_OFF, SC1);
    }
    gv[0] = __uint_as_float(gg[0][0]); gv[1] = __uint_as_float(gg[1][0]);
    int ln = lane;
    PIN(ln);
    const int nn = ln & 15, mt = wid >> 1;
    const int ob = mt < 3 ? wslot(0) + mt * 4096 : wslot(3), oa = mt == 0 ? wslot(5) : wslot(6) + (mt - 1) * 4096;
    vo = ((rb * 64 + wid * 8 + (ln >> 4)) * D + cb * 64 + nn * 4) * 4;
#pragma unroll
    for (int u = 0; u < 2; u++) {
      const int sl = ((wid & 1) * 8 + u * 4 + (ln >> 4)) ^ nn;
      sb[u] = lds + ob + nn * 256 + (sl << 4);
      sa[u] = lds + oa + nn * 256 + ((sl ^ (4 * mt)) << 4);
      omg[u] = rbf(1.0f - gv[u]);
#pragma unroll
      for (int e = 0; e < 4; e++) {
        const unsigned xw = xv[u][e >> 1], dw = dv[e >> 1];
        const float xf = __uint_as_float((e & 1) ? (xw & 0xffff0000u) : (xw << 16));
        const float df = __uint_as_float((e & 1) ? (dw & 0xffff0000u) : (dw << 16));
        sk[u][e] = rbf(xf * df);
      }
      PIN(sb[u]); PIN(sa[u]); PIN(omg[u]); PIN(sk[u]);
    }
    PIN(vo);
  };

  if (wid >= 4) {
    const int lw = w4;
    const u32x4 wsd = mkdesc(p.ws, (unsigned)H_OFF);
    const u32x4 wdr = mkdesc(p.wd, F * D * 2);
    const unsigned vwd = (lw * 14 * 32 + (lane >> 3)) * 1024 + cb * 128 + (((lane & 7) ^ (((lane >> 4) & 3) << 1)) << 4);
    u32x4 ctv;
    u64 cfl;
    auto ct_load = [&]() {
      const int b = blockIdx.x;
      ctv = ld16_async(mkdesc((b & 1) ? p.cim : p.cre, D * P * 2),
                       ((b >> 5) * 64 + ((b >> 1) & 3) * 16 + n) * 1024 + ((b >> 3) & 3) * 256 + lw * 64 + g * 16, 0);
    };
    auto ct_store = [&]() { st16_async(wsd, (unsigned)CT_OFF + (blockIdx.x * 4 + lw) * 1024 + lane * 16, ctv); };
    auto ct_flag = [&]() { st_flag(wsr, lane == 0 ? (blockIdx.x * 4 + lw) * FS : OOR, FC_OFF, tag); };
    const unsigned cfo = FC_OFF + ((cb * 4 + lw) * 32 + (lane & 31)) * FS;
    auto ct_ask = [&]() { cfl = ld8_async(wsd, cfo); };
    auto ct_check = [&]() { PIN(cfl); if (!__all(cfl == tag)) poll_flags(wsr, cfo, 0, tag); };
    u32x4 cf[32];
    const unsigned vcf = (cb * 4 + lw) * 32768 + lane * 16;
    auto ldc = [&](int a, int b) {
#pragma unroll
      for (int f = a; f < b; f++) cf[f] = ld16_async(wsd, vcf, (unsigned)CT_OFF + f * 1024);
    };
    auto wpiece = [&](int i, int k) { dma16(wdr, wslot(i < 9 ? i : i - 9) + lw * 12288 + k * 1024, vwd, (gstep(i) * 32 + k * 8) * 1024); };
    auto wround = [&](int r) {
#pragma unroll
      for (int q = 0; q < 12; q++) wpiece(r * 3 + (q >> 2), q & 3);
    };
    const u32x4 gr = mkdesc(p.wg, D * F * 2), ur = mkdesc(p.wu, D * F * 2);
    auto pre_1 = [&]() {
      const unsigned v = (((k0 + 2) & 7) * 64 + idx * 4 + lw) * (F * 2) + col0 * 256 + (lane < ncol * 16 ? lane : 0) * 16;
      dma16(gr, C_DUMP, v, 0);
      dma16(ur, C_DUMP, v, 0);
    };
    auto pre_w = [&]() { touch(wdr, C_DUMP, (lane < 14 ? lane : 0) * 128, (blockIdx.x * 4 + lw) * 1792); };
    constexpr int NP = 12, ALL = 10;
    if (role < 2) {
      if (PREB && ssm) {
        const unsigned v = (ct * 64 + (idx & 3) * 16 + lw * 4 + g) * 1024 + ((k0 + 2) & 7) * 128 + n * 16;
        dma16(w0r, C_DUMP, v, 0);
        dma16(w1r, C_DUMP, v, 0);
        ct_load(); pre_1(); pieces(1, 0, S1A); LST(0); WAITV(5 + S1A);
      } else {
        ct_load(); pre_1(); pieces(1, 0, S1A); LST(0); WAITV(3 + S1A);
      }
      LST(1); BARRIER(); LST(2);
      tag = *(LAS u64*)(lds + C_TAG);
      pieces(1, S1A, ALL); pieces(2, 0, S2A); LST(3); WAITV(S2A); LST(4); BARRIER(); LST(5);
      ct_store(); pieces(2, S2A, ALL); pieces(3, 0, S3A); LST(6); WAITV(S3A); ct_flag(); LST(7); BARRIER(); LST(8);
      pieces(3, S3A, ALL);
      pieces(4, 0, ALL); LST(9); WAITV(NP); LST(10); BARRIER(); LST(11);
      if (!ssm) ct_ask();
      pieces(5, 0, ALL); LST(12); WAITV(NP);
      if (!ssm) { ct_check(); ldc(0, NA); }
      pre_w();
      LST(13); BARRIER(); LST(14);
      pieces(6, 0, ALL); LST(15);
      if (!ssm) { ldc(NA, 2 * NA); WAITV(NP + 2 + 2 * NA); } else WAITV(NP + 1);
      LST(16); BARRIER(); LST(17);
      if (ssm && !SNC) ct_ask();
      pieces(7, 0, ALL); LST(18);
      if (!ssm) { ldc(2 * NA, 3 * NA); WAITV(NP + 1 + 2 * NA); } else WAITV(NP);
      LST(19); BARRIER(); LST(20);
      if (ssm && !SNC) ct_check();
      wround(0);
      if (!(SNC && ssm)) { ldc(3 * NA, 3 * NA + NB); WAITV(12 + NB); } else { WAITV(12); ct_ask(); }
      LST(22); BARRIER(); LST(23);
      wround(1);
      if (!(SNC && ssm)) ldc(3 * NA + NB, 3 * NA + 2 * NB);
      LST(25); BARRIER(); LST(26);
#pragma unroll
      for (int q = 0; q < PRE2; q++) wpiece(6 + (q >> 2), q & 3);
      if (ssm) { WAITV(12 + PRE2); ct_check(); ldc(0, 3 * NA + 2 * NB); }
      WAITV(0);
    } else {
      ct_load();
      BARRIER();
      tag = *(LAS u64*)(lds + C_TAG);
      WAITV(0); ct_store(); WAITV(0); ct_flag();
      pre_1(); pre_w();
      wround(0); wround(1);
#pragma unroll
      for (int q = 0; q < PRE2; q++) wpiece(6 + (q >> 2), q & 3);
      poll_flags(wsr, cfo, 0, tag);
      ldc(0, 3 * NA + 2 * NB);
      WAITV(0);
      if (myid == 255 && lw == 3) {
#pragma unroll 1
        for (int k = 0; k < 4; k++) poll_flags(wsr, (k * 64 + lane) * 4 * FS, FC_OFF, tag);
        st_flag(wsr, lane == 0 ? (unsigned)EPOCH_OFF : OOR, 0, tag);
      }
    }
    lds_set(lds + C_F0 + lw * 4);
    STAMP(1);
    if (role == 0) lds_wait(lds + C_FA + lw * 4);
#pragma unroll
    for (int q = PRE2; q < 12; q++) wpiece(6 + (q >> 2), q & 3);
    ldc(3 * NA + 2 * NB, 32);
    const unsigned fs = FB_OFF + ((182 + (rb >> 2) * 8 + lw * 2 + ((lane >> 1) & 1)) * 4 + (rb & 2) + (lane & 1)) * FS;
    vh[0] = rb * 4 * HM + (56 + lw * 8) * 1024 + lane * 16;
    set_vh();
    LAS char* pb[4];
#pragma unroll
    for (int k = 0; k < 4; k++) {
      const int mt = n >> 2;
      pb[k] = lds + (mt == 0 ? wslot(5) : wslot(6) + (mt - 1) * 4096) + lw * 12288 + g * 256 + ((((n & 3) ^ g) | ((k ^ mt) << 2)) << 4);
      PIN(pb[k]);
    }
    if (EARLYF) ld_final();
    STAMP(2);
    lds_wait(lds + C_F1 + lw * 4);
    STAMP(3);
#pragma unroll
    for (int q = 0; q < 20; q++) {
      if (q == 20 - ASKD) cfl = ld8_async(wsd, fs);
      wpiece(9 + (q >> 2), q & 3);
    }
    WAITV(ASKD);
    lds_set(lds + C_F2 + lw * 4);
    STAMP(6);
    PIN(cfl);
    if (!__all(cfl == tag)) { WAITV(0); lds_set(lds + C_F2 + lw * 4, 2u); poll_flags(wsr, fs, 0, tag); }
    asm volatile("" ::: "memory");
    STAMP(7);
    bf16x8 hf[32];
#pragma unroll
    for (int q = 0; q < DH; q++) hf[q] = ldh(q >> 2, q & 3);
    WAITV(DH);
    lds_set(lds + C_F2 + lw * 4, 2u);
    if (EARLYF) { SCHED(); prep_final(); }
#pragma unroll
    for (int f = 0; f < 32; f++) PIN(cf[f]);
#pragma unroll
    for (int c = 0; c < 4; c++)
#pragma unroll
      for (int m = 0; m < 4; m++) acc2[c][m] = (f32x4){0.f, 0.f, 0.f, 0.f};
    SCHED();
#pragma unroll
    for (int k = 0; k < 8; k++) {
      if (!EARLYF && k == 3) ld_final();
      if (!EARLYF && k == 6) prep_final();
      if (k == 7) lds_wait(lds + C_F3 + lw * 4);
#pragma unroll
      for (int m = 0; m < 4; m++) {
#pragma unroll
        for (int c = 0; c < 4; c++)
          acc2[c][m] = __builtin_amdgcn_mfma_f32_16x16x32_bf16(__builtin_bit_cast(bf16x8, cf[c * 8 + (k & 1) * 4 + (k >> 1)]), hf[k * 4 + m], acc2[c][m], 0, 0, 0);
        if (k * 4 + m + DH < 32) hf[k * 4 + m + DH] = ldh((k * 4 + m + DH) >> 2, (m + DH) & 3);
        if (k == 7 && m > 0) {
#pragma unroll
          for (int c = 0; c < 4; c++) *(LAS f32x4*)(pb[(m - 1) ^ c] + c * 1024) = acc2[c][m - 1];
        }
        SCHED();
      }
    }
    STAMP(8);
#pragma unroll
    for (int c = 0; c < 4; c++) *(LAS f32x4*)(pb[3 ^ c] + c * 1024) = acc2[c][3];
  } else {
    const int wr = wid >> 1, wc = wid & 1;
    if (wid == 0) {
      u64 w0;
      asm volatile("s_load_dwordx2 %0, %1, 0x0\n\ts_waitcnt lgkmcnt(0)" : "=s"(w0) : "s"(p.ws + EPOCH_OFF) : "memory");
      *(LAS u64*)(lds + C_TAG + lane * 8) = lane == 0 ? w0 + TAG_STEP : 0ull;
    }
    unsigned vfo = lane == 0 && role < 2 ? (myid * 4 + wid) * FS : OOR;
    unsigned vfi = glu_flags();
    vh[0] = rb * 4 * HM + wid * 14 * 1024 + lane * 16;
    LAS char* ra0 = lds + wid * 12288 + (4 * g + (n >> 2)) * 128 + (((2 * g + (n >> 3)) & 3) << 5) + (n & 3) * 8;
    PIN(vfo); PIN(vfi); PIN(vh[0]); PIN(ra0);
    bf16x8 wf[6][4];
    LAS char* ra[3][4];
    auto ldw = [&](int i, int c) -> bf16x8 {
      const int k = i < 9 ? i : i - 9;
      LAS char* a = ra[k / 3][c] + (k % 3) * 4096;
      return ldtr(a, a + 2048);
    };
    auto grab_w = [&]() {
#pragma unroll
      for (int c = 0; c < 4; c++) {
        ra[1][c] = (LAS char*)((unsigned)(size_t)ra0 ^ (c << 5));
        ra[0][c] = ra[1][c] + 2 * SLOT;
        ra[2][c] = ra[1][c] + SLOT;
      }
      lds_wait(lds + C_F0 + wid * 4);
#pragma unroll
      for (int i = 0; i < 5; i++)
#pragma unroll
        for (int c = 0; c < 4; c++) wf[i][c] = ldw(i, c);
      asm volatile("" ::: "memory");
      lds_set(lds + C_F1 + wid * 4);
    };
    if (role == 0) {
      const int mt0 = (min(rt * 160, L - 160) >> 4) + wr * 5;
      unsigned vs[5];
#pragma unroll
      for (int m = 0; m < 5; m++) { vs[m] = mt0 + m < rt * 10 ? OOR : (mt0 + m) * HM + (ct * 4 + wc * 2) * 1024 + lane * 16; PIN(vs[m]); }
      f32x4 acc[8][5];
      STAMP(1);
      main_loop<true, 5>(lds, wr, wc, lane, acc);
      STAMP(2);
      tag = *(LAS u64*)(lds + C_TAG);
#pragma unroll
      for (int h = 0; h < 2; h++) {
        if (h == 1 && !GRABL) { SCHED(); STAMP(13); grab_w(); STAMP(14); SCHED(); }
#pragma unroll
        for (int m = 0; m < 5; m++) {
          if (h == 1 && m == PUBA) { WAITV(PUBA); st_flag(wsr, vfo, FA_OFF, tag); STAMP(3); }
          u32x4 o;
#pragma unroll
          for (int e = 0; e < 4; e++) {
            const int c = 2 * h + (e >> 1);
            const f32x2 gv_ = (e & 1) ? hi2(acc[c][m]) : lo2(acc[c][m]);
            const f32x2 uv = (e & 1) ? hi2(acc[4 + c][m]) : lo2(acc[4 + c][m]);
            f32x2 t = gv_ * -1.4426950408889634f;
            t = (f32x2){__builtin_amdgcn_exp2f(t.x), __builtin_amdgcn_exp2f(t.y)} + 1.0f;
            t = (f32x2){__builtin_amdgcn_rcpf(t.x), __builtin_amdgcn_rcpf(t.y)} * uv;
            o[e] = pack2(t.x, t.y);
          }
          __builtin_amdgcn_raw_buffer_store_b128(o, hr, vs[m] + h * 1024, 0, SC1);
          SCHED();
        }
      }
    } else {
      if (role == 1) {
        float lr[2], li[2], cr[2], ci[2], sr[2], si[2], mr[2], mi[2], wr_[2], wi_[2], qr, qi, zr, zi;
        {
          float a2, a3;
          WAITV(6);
          PIN(c_ldt); PIN(c_lre); PIN(c_lim);
          ssm_consts(c_ldt, c_lre, c_lim, qr, qi, a2, a3);
          share2(qr, lr); share2(qi, li); share2(2.f * a2, cr); share2(2.f * a3, ci);
          csq(qr, qi); csq(qr, qi); csq(qr, qi); csq(qr, qi);
          share2(qr, sr); share2(qi, si);
          csq(qr, qi);
          share2(qr, mr); share2(qi, mi);
          csq(qr, qi); csq(qr, qi);
          zr = qr; zi = qi;
          csq(zr, zi); csq(zr, zi); csq(zr, zi);
          if (rt * 2 + wr <= 8) { zr = 0.f; zi = 0.f; }
#pragma unroll
          for (int u = 0; u < 2; u++) {
            float ar = mr[u], ai = mi[u];
            csq(ar, ai);
            const float br_ = (g & 1) ? mr[u] : 1.f, bi_ = (g & 1) ? mi[u] : 0.f;
            wr_[u] = (g & 2) ? ar * br_ - ai * bi_ : br_;
            wi_[u] = (g & 2) ? ar * bi_ + ai * br_ : bi_;
            PIN(mr[u]); PIN(mi[u]); PIN(lr[u]); PIN(li[u]); PIN(cr[u]); PIN(ci[u]); PIN(sr[u]); PIN(si[u]); PIN(wr_[u]); PIN(wi_[u]);
          }
          PIN(qr); PIN(qi); PIN(zr); PIN(zi);
        }
        const int ck = rt * 2 + wr;
        unsigned veo = lane < 32 ? (unsigned)E_OFF + (ck * 512 + chg) * 16 : OOR;
        const int c0 = ck - 1 - 8 * (lane >> 5);
        int vei = (c0 * 512 + chg) * 16, nval = min(max(c0 + 1, 0), 8);
        const unsigned tb = (wid >> 1) * 2 * SLOT + 49152 + (wid & 1) * 2048;
        LAS char* twp = lds + tb + n * 64 + ((g ^ (((n >> 1) & 1) | ((((n >> 2) ^ (n >> 3)) & 1) << 1))) << 4);
        LAS char* trp[2];
#pragma unroll
        for (int t = 0; t < 2; t++)
          trp[t] = lds + tb + (8 * g + (n >> 2)) * 64 + (((((n >> 1) ^ (n >> 3)) & 1) | (((g ^ t) & 1) << 1)) << 4) + (n & 1) * 8;
        unsigned vso[8];
#pragma unroll
        for (int k = 0; k < 8; k++) { vso[k] = (rt * 16 + wr * 8 + k) * HM + (56 + (ct * 2 + wc) * 2) * 1024 + lane * 16; PIN(vso[k]); }
        PIN(veo); PIN(vei); PIN(nval); PIN(twp); PIN(trp[0]); PIN(trp[1]);
        f32x4 acc[8][4];
        STAMP(1);
        main_loop<false, 4>(lds, wr, wc, lane, acc);
        STAMP(2);
        tag = *(LAS u64*)(lds + C_TAG);
        f32x2 er[2], ei[2];
#pragma unroll
        for (int u = 0; u < 2; u++) { er[u] = lo2(acc[0][u]); ei[u] = lo2(acc[0][2 + u]); }
#pragma unroll
        for (int st = 1; st < 16; st++)
#pragma unroll
          for (int u = 0; u < 2; u++) {
            const f32x2 l2 = {lr[u], lr[u]}, i2 = {li[u], li[u]};
            const f32x2 vr = (st & 1) ? hi2(acc[st >> 1][u]) : lo2(acc[st >> 1][u]);
            const f32x2 vi = (st & 1) ? hi2(acc[st >> 1][2 + u]) : lo2(acc[st >> 1][2 + u]);
            const f32x2 t = fma2(-i2, ei[u], fma2(l2, er[u], vr));
            ei[u] = fma2(i2, er[u], fma2(l2, ei[u], vi));
            er[u] = t;
          }
        float xs[2][4], ys[2][4];
#pragma unroll
        for (int u = 0; u < 2; u++) {
          const float a = fmaf(-si[u], ei[u].x, fmaf(sr[u], er[u].x, er[u].y)), b = fmaf(si[u], er[u].x, fmaf(sr[u], ei[u].x, ei[u].y));
          float a2[2], b2[2];
          share2(a, a2); share2(b, b2);
#pragma unroll
          for (int k = 0; k < 2; k++) {
            const auto ta = __builtin_amdgcn_permlane32_swap(__float_as_uint(a2[k]), __float_as_uint(a2[k]), false, false);
            const auto tb_ = __builtin_amdgcn_permlane32_swap(__float_as_uint(b2[k]), __float_as_uint(b2[k]), false, false);
            xs[u][k] = __uint_as_float(ta[0]); xs[u][2 + k] = __uint_as_float(ta[1]);
            ys[u][k] = __uint_as_float(tb_[0]); ys[u][2 + k] = __uint_as_float(tb_[1]);
          }
#pragma unroll
          for (int k = 1; k < 4; k++) {
            xs[u][k] = fmaf(-mi[u], ys[u][k - 1], fmaf(mr[u], xs[u][k - 1], xs[u][k]));
            ys[u][k] = fmaf(mi[u], xs[u][k - 1], fmaf(mr[u], ys[u][k - 1], ys[u][k]));
            PIN(xs[u][k]); PIN(ys[u][k]);
          }
        }
        const unsigned tg = (unsigned)tag;
        __builtin_amdgcn_raw_buffer_store_b128((u32x4){__float_as_uint(g ? xs[1][3] : xs[0][3]), tg, __float_as_uint(g ? ys[1][3] : ys[0][3]), tg},
                                               wsr, veo, 0, SC1);
        STAMP(3);
        SCHED();
        float pr[2], pi[2];
#pragma unroll
        for (int u = 0; u < 2; u++) {
          pr[u] = g == 0 ? 0.f : (g == 1 ? xs[u][0] : (g == 2 ? xs[u][1] : xs[u][2]));
          pi[u] = g == 0 ? 0.f : (g == 1 ? ys[u][0] : (g == 2 ? ys[u][1] : ys[u][2]));
          PIN(pr[u]); PIN(pi[u]);
        }
        const rsrc_t erc = mkrsrc(p.ws + E_OFF, (unsigned)E_BYTES);
        u32x4 v[8];
        auto ask = [&]() {
#pragma unroll
          for (int d = 0; d < 8; d++) v[d] = __builtin_amdgcn_raw_buffer_load_b128(erc, vei - d * 8192, 0, SC1);
        };
#pragma unroll
        for (int u = 0; u < 2; u++) {
          if (u == ASKE) { SCHED(); ask(); SCHED(); }
          PIN(cr[u]);
#pragma unroll
          for (int m = 0; m < 8; m++) {
            const f32x4 a = acc[m][u], b = acc[m][2 + u];
            acc[m][u] = cr[u] * a - ci[u] * b;
            acc[m][2 + u] = cr[u] * b + ci[u] * a;
            PIN(acc[m][u]); PIN(acc[m][2 + u]);
          }
        }
        if (ASKE >= 2) { __builtin_amdgcn_s_sleep(ASKE - 2); ask(); }
#pragma unroll 1
        for (unsigned spins = 0; spins < SPIN_CAP; spins++) {
          int cnt = 0;
#pragma unroll
          for (int d = 0; d < 8; d++) cnt += ((v[d][1] ^ tg) | (v[d][3] ^ tg)) == 0u;
          if (__all(cnt >= nval)) break;
          asm volatile("" ::: "memory");
          __builtin_amdgcn_s_sleep(ESL);
          ask();
        }
        float kr[2], ki[2];
        {
          float xr_ = __uint_as_float(v[7][0]), xi_ = __uint_as_float(v[7][2]);
#pragma unroll
          for (int d = 6; d >= 0; d--) {
            const float t = fmaf(-qi, xi_, fmaf(qr, xr_, __uint_as_float(v[d][0])));
            xi_ = fmaf(qi, xr_, fmaf(qr, xi_, __uint_as_float(v[d][2])));
            xr_ = t;
            PIN(xr_); PIN(xi_);
          }
          const auto ta = __builtin_amdgcn_permlane32_swap(__float_as_uint(xr_), __float_as_uint(xr_), false, false);
          const auto tb_ = __builtin_amdgcn_permlane32_swap(__float_as_uint(xi_), __float_as_uint(xi_), false, false);
          const float fr = __uint_as_float(ta[1]), fi = __uint_as_float(tb_[1]);
          share2(fmaf(-zi, fi, fmaf(zr, fr, __uint_as_float(ta[0]))), kr);
          share2(fmaf(zi, fr, fmaf(zr, fi, __uint_as_float(tb_[0]))), ki);
        }
        STAMP(4);
        f32x2 hr_[2], hi_[2];
#pragma unroll
        for (int u = 0; u < 2; u++) {
          f32x2 a, b;
          a.x = fmaf(-wi_[u], ki[u], fmaf(wr_[u], kr[u], pr[u])); b.x = fmaf(wi_[u], kr[u], fmaf(wr_[u], ki[u], pi[u]));
          a.y = fmaf(-si[u], b.x, fmaf(sr[u], a.x, er[u].x)); b.y = fmaf(si[u], a.x, fmaf(sr[u], b.x, ei[u].x));
          PIN(a); PIN(b);
          hr_[u] = cr[u] * a - ci[u] * b; hi_[u] = cr[u] * b + ci[u] * a;
        }
        bf16x8 tv[4];
#pragma unroll
        for (int qt = 0; qt < 5; qt++) {
          u32x4 o[2][2];
          if (qt < 4) {
#pragma unroll
            for (int mm = 0; mm < 2; mm++)
#pragma unroll
              for (int u = 0; u < 2; u++) {
                const int m = qt * 2 + mm;
                const f32x2 l2 = {lr[u], lr[u]}, i2 = {li[u], li[u]};
                const f32x2 r0_ = fma2(-i2, hi_[u], fma2(l2, hr_[u], lo2(acc[m][u])));
                const f32x2 i0_ = fma2(i2, hr_[u], fma2(l2, hi_[u], lo2(acc[m][2 + u])));
                hr_[u] = fma2(-i2, i0_, fma2(l2, r0_, hi2(acc[m][u])));
                hi_[u] = fma2(i2, r0_, fma2(l2, i0_, hi2(acc[m][2 + u])));
                o[0][u][mm] = pack2(r0_.x, hr_[u].x); o[0][u][2 + mm] = pack2(r0_.y, hr_[u].y);
                o[1][u][mm] = pack2(-i0_.x, -hi_[u].x); o[1][u][2 + mm] = pack2(-i0_.y, -hi_[u].y);
              }
          }
          SCHED();
          if (qt > 0) {
#pragma unroll
            for (int k = 0; k < 4; k++) __builtin_amdgcn_raw_buffer_store_b128(__builtin_bit_cast(u32x4, tv[k]), hr, vso[(k & 1) * 4 + qt - 1] + (k >> 1) * 1024, 0, SC1);
          }
          if (qt < 4) {
#pragma unroll
            for (int part = 0; part < 2; part++) {
              asm volatile("" ::: "memory");
              *(LAS u32x4*)twp = o[part][0];
              *(LAS u32x4*)(twp + 1024) = o[part][1];
              asm volatile("" ::: "memory");
#pragma unroll
              for (int rho = 0; rho < 2; rho++) tv[part * 2 + rho] = ldtr(trp[rho], trp[rho ^ 1] + 256);
            }
          }
          SCHED();
        }
        STAMP(13);
      } else {
        WAITV(0);
        BARRIER();
        tag = *(LAS u64*)(lds + C_TAG);
        if (role == 2) {
          const u32x4 gwv = *(const u32x4*)(p.gw + lane * 8);
          f32x2 gwf[4];
#pragma unroll
          for (int e = 0; e < 4; e++) gwf[e] = (f32x2){__uint_as_float(gwv[e] << 16), __uint_as_float(gwv[e] & 0xffff0000u)};
          const bf16x2 ones = {(__bf16)1.0f, (__bf16)1.0f};
#pragma unroll 1
          for (int it = 0; it < 4; it++) {
            const int r0 = rt * 256 + wid * 64 + it * 16;
            u32x4 xw[16];
#pragma unroll
            for (int r = 0; r < 16; r++) xw[r] = *(const u32x4*)(p.x + (size_t)(r0 + r) * D + lane * 8);
            float s[16];
#pragma unroll
            for (int r = 0; r < 16; r++) {
              s[r] = 0.f;
#pragma unroll
              for (int e = 0; e < 4; e++) {
                const unsigned w = xw[r][e];
                const f32x2 pr = (f32x2){__uint_as_float(w << 16), __uint_as_float(w & 0xffff0000u)} * gwf[e];
                const bf16x2 pb = {(__bf16)pr.x, (__bf16)pr.y};
                s[r] = __builtin_amdgcn_fdot2_f32_bf16(pb, ones, s[r], false);
              }
            }
#pragma unroll
            for (int r = 0; r < 8; r++) {
              const auto t = __builtin_amdgcn_permlane32_swap(__float_as_uint(s[r]), __float_as_uint(s[r + 8]), false, false);
              s[r] = __uint_as_float(t[0]) + __uint_as_float(t[1]);
            }
#pragma unroll
            for (int r = 0; r < 4; r++) {
              const auto t = __builtin_amdgcn_permlane16_swap(__float_as_uint(s[r]), __float_as_uint(s[r + 4]), false, false);
              s[r] = __uint_as_float(t[0]) + __uint_as_float(t[1]);
            }
#pragma unroll
            for (int r = 0; r < 4; r++) {
              s[r] += __uint_as_float(__builtin_amdgcn_update_dpp(0, __float_as_uint(s[r]), 0x128, 0xf, 0xf, false));
              s[r] += __uint_as_float(__builtin_amdgcn_update_dpp(0, __float_as_uint(s[r]), 0x124, 0xf, 0xf, false));
              s[r] += __uint_as_float(__builtin_amdgcn_update_dpp(0, __float_as_uint(s[r]), 0x122, 0xf, 0xf, false));
              s[r] += __uint_as_float(__builtin_amdgcn_update_dpp(0, __float_as_uint(s[r]), 0x121, 0xf, 0xf, false));
            }
            const float sv = (n & 2) ? ((n & 1) ? s[3] : s[2]) : ((n & 1) ? s[1] : s[0]);
            __builtin_amdgcn_raw_buffer_store_b64((u32x2){__float_as_uint(rbf(sigmoidf_(rbf(sv)))), (unsigned)tag}, wsr,
                                                  n < 4 ? (r0 + n + 4 * (g & 1) + 8 * (g >> 1)) * 8 : OOR, (unsigned)G_OFF, SC1);
          }
        }
      }
      if (!GRABL) grab_w();
    }
    STAMP(5);
    lds_set(lds + C_FA + wid * 4);
    {
      const u64 fv = ld_flag(wsr, vfi, FA_OFF);
      ld_final();
      set_vh();
      if (GRABL) { SCHED(); grab_w(); SCHED(); }
      WAITV(6);
      st_flag(wsr, vfo, FB_OFF, tag);
      STAMP(6);
      if (!__all(fv == tag)) poll_flags(wsr, vfi, FA_OFF, tag);
    }
    asm volatile("" ::: "memory");
    STAMP(7);
    bf16x8 hf[56];
#pragma unroll
    for (int q = 0; q < DE; q++) hf[q] = ldh(gstep(q >> 2), q & 3);
    SCHED(); prep_final();
#pragma unroll
    for (int c = 0; c < 4; c++)
#pragma unroll
      for (int m = 0; m < 4; m++) acc2[c][m] = (f32x4){0.f, 0.f, 0.f, 0.f};
    LAS char* pw[4];
    SCHED();
    u64 fbv = 0;
#pragma unroll
    for (int k = 0; k < 14; k++) {
      if (k == CHKB - 2) fbv = ld_flag(wsr, vfi, FB_OFF);
      if (k == CHKB) {
        if (!__all(fbv == tag)) poll_flags(wsr, vfi, FB_OFF, tag);
        asm volatile("" ::: "memory");
        STAMP(12);
      }
      if (k == 5) lds_wait(lds + C_F2 + wid * 4);
      if (k == 8) lds_wait(lds + C_F2 + wid * 4, 2u);
      if (k == 9) { lds_set(lds + C_F3 + wid * 4); if (!EARLYF) ld_final(); }
      if (k == 12) {
        if (!EARLYF) prep_final();
        int ln = lane;
        PIN(ln);
#pragma unroll
        for (int c = 0; c < 4; c++) { pw[c] = lds + wid * 12288 + c * 1024 + (ln >> 4) * 256 + (((ln & 15) ^ (4 * c + (ln >> 4))) << 4); PIN(pw[c]); }
      }
#pragma unroll
      for (int m = 0; m < 4; m++) {
        if (k + 1 >= 5 && k + 1 < 14) wf[(k + 1) % 6][m] = ldw(k + 1, m);
#pragma unroll
        for (int c = 0; c < 4; c++) acc2[c][m] = __builtin_amdgcn_mfma_f32_16x16x32_bf16(wf[k % 6][c], hf[k * 4 + m], acc2[c][m], 0, 0, 0);
        if (k * 4 + m + DE < 28) hf[k * 4 + m + DE] = ldh(gstep((k * 4 + m + DE) >> 2), (m + DE) & 3);
        if (k * 4 + m + DO >= 28 && k * 4 + m + DO < 56) hf[k * 4 + m + DO] = ldh(gstep((k * 4 + m + DO) >> 2), (m + DO) & 3);
        if (k == 13 && m > 0) {
#pragma unroll
          for (int c = 0; c < 4; c++) *(LAS f32x4*)(pw[c] + wslot(m - 1)) = acc2[c][m - 1];
        }
        SCHED();
      }
    }
    STAMP(8);
#pragma unroll
    for (int c = 0; c < 4; c++) *(LAS f32x4*)(pw[c] + wslot(3)) = acc2[c][3];
  }
  BARRIER();
  STAMP(9);
  {
    const rsrc_t orc = mkrsrc(p.out, L * D * 4);
#pragma unroll
    for (int u = 0; u < 2; u++) {
      f32x4 b = *(LAS f32x4*)sb[u], a = sk[u] + *(LAS f32x4*)sa[u];
#pragma unroll
      for (int w = 1; w < 4; w++) {
        b += *(LAS f32x4*)(sb[u] + w * 12288);
        a += *(LAS f32x4*)(sa[u] + w * 12288);
      }
      const f32x4 o = gv[u] * a + omg[u] * b;
      __builtin_amdgcn_raw_buffer_store_b128(__builtin_bit_cast(u32x4, o), orc, vo + u * 8192, 0, OAUX);
    }
  }
  asm volatile(".Lgm_code_end:");
  STAMP(10);
}

extern "C" void kernel_launch(void* const* d_in, const int* in_sizes, int n_in, void* d_out, int out_size, void* d_ws,
                              size_t ws_size, hipStream_t stream) {
  Params p;
  p.x = (const u16*)d_in[0]; p.lre = (const u16*)d_in[1]; p.lim = (const u16*)d_in[2];
  p.bre = (const u16*)d_in[3]; p.bim = (const u16*)d_in[4]; p.cre = (const u16*)d_in[5]; p.cim = (const u16*)d_in[6];
  p.ldt = (const u16*)d_in[7]; p.dsk = (const u16*)d_in[8];
  p.wg = (const u16*)d_in[10]; p.wu = (const u16*)d_in[11]; p.wd = (const u16*)d_in[12]; p.gw = (const u16*)d_in[13];
  p.ws = (char*)d_ws + WSB; p.out = (float*)d_out;
  (void)hipFuncSetAttribute((const void*)k_fused, hipFuncAttributeMaxDynamicSharedMemorySize, LDS_BYTES);
  k_fused<<<256, 512, LDS_BYTES, stream>>>(p);
}
```

```cpp
#include <hip/hip_runtime.h>
#include <stdint.h>

typedef unsigned short u16;
typedef unsigned long long u64;
typedef __bf16 bf16x8 __attribute__((ext_vector_type(8)));
typedef __bf16 bf16x4 __attribute__((ext_vector_type(4)));
typedef __bf16 bf16x2 __attribute__((ext_vector_type(2)));
typedef float f32x4 __attribute__((ext_vector_type(4)));
typedef float f32x2 __attribute__((ext_vector_type(2)));
typedef unsigned u32x4 __attribute__((ext_vector_type(4)));
typedef unsigned u32x2 __attribute__((ext_vector_type(2)));
#define LAS __attribute__((address_space(3)))
typedef __amdgpu_buffer_rsrc_t rsrc_t;

constexpr int L = 2048, D = 512, P = 512, F = 1792;
constexpr int KS2 = 88;
constexpr int SLOT = 53248;
constexpr int LDS_CTL = 3 * SLOT;
constexpr int LDS_BYTES = 163840;
constexpr int SC1 = 16, NT = 2;
constexpr size_t EPOCH_OFF = 0;
constexpr size_t G_OFF = 65536;
constexpr size_t E_OFF = 131072, E_BYTES = 16 * 512 * 16;
constexpr int FS = 128;
constexpr unsigned FA_OFF = 262144, FB_OFF = FA_OFF + 1024 * FS, FC_OFF = FB_OFF + 1024 * FS;
constexpr size_t CT_OFF = 1u << 20, CT_BYTES = 1u << 20;
constexpr int HM = KS2 * 1024;
constexpr size_t H_OFF = CT_OFF + CT_BYTES, H_BYTES = (size_t)128 * HM;
constexpr u64 TAG_STEP = 0x9E3779B97F4A7C15ull;
constexpr int NA = 3, NB = 4;
constexpr int S1A = 2, S2A = 6, S3A = 9;
constexpr int PUBA = 2;
constexpr int ASKD = 8;
constexpr int DE = 12, DO = 6, DH = 6;
constexpr int CHKB = (28 - DO) / 4;
constexpr unsigned OOR = 0x40000000u;
constexpr unsigned SPIN_CAP = 1u << 18;

#define BARRIER() asm volatile("s_waitcnt lgkmcnt(0)\n\ts_barrier" ::: "memory")
#define WAITV(n) asm volatile("s_waitcnt vmcnt(%0)" ::"i"(n) : "memory")
#define SCHED() __builtin_amdgcn_sched_barrier(0)
#define PIN(x) asm volatile("" : "+v"(x))

__device__ __forceinline__ float bf2f(u16 v) { return __uint_as_float(((unsigned)v) << 16); }
__device__ __forceinline__ float rbf(float f) { return (float)(__bf16)f; }
__device__ __forceinline__ unsigned pack2(float a, float b) {
  bf16x2 v = {(__bf16)a, (__bf16)b};
  return __builtin_bit_cast(unsigned, v);
}
__device__ __forceinline__ rsrc_t mkrsrc(const void* p, unsigned bytes) {
  return __builtin_amdgcn_make_buffer_rsrc((void*)p, 0, bytes, 0x00020000);
}
__device__ __forceinline__ u32x4 mkdesc(const void* p, unsigned bytes) {
  const u64 a = (u64)p;
  return (u32x4){(unsigned)a, (unsigned)(a >> 32) & 0xffffu, bytes, 0x00020000u};
}
__device__ __forceinline__ void dma16(u32x4 d, unsigned lds_dst, unsigned voff, unsigned soff) {
  asm volatile("s_mov_b32 m0, %2\n\ts_nop 3\n\tbuffer_load_dwordx4 %0, %1, %3 offen lds" ::"v"(voff), "s"(d), "s"(lds_dst), "s"(soff) : "memory");
}
__device__ __forceinline__ u32x4 ld16_async(u32x4 d, unsigned voff, unsigned soff) {
  u32x4 r;
  asm volatile("s_nop 4\n\tbuffer_load_dwordx4 %0, %1, %2, %3 offen sc1" : "=v"(r) : "v"(voff), "s"(d), "s"(soff) : "memory");
  return r;
}
__device__ __forceinline__ u64 ld8_async(u32x4 d, unsigned voff) {
  u64 r;
  asm volatile("s_nop 4\n\tbuffer_load_dwordx2 %0, %1, %2, 0 offen sc1" : "=v"(r) : "v"(voff), "s"(d) : "memory");
  return r;
}
__device__ __forceinline__ unsigned ld2_async(const void* base, unsigned voff) {
  unsigned r;
  asm volatile("s_nop 4\n\tglobal_load_ushort %0, %1, %2" : "=v"(r) : "v"(voff), "s"(base) : "memory");
  return r;
}
__device__ __forceinline__ void st16_async(u32x4 d, unsigned voff, u32x4 v) {
  asm volatile("s_nop 4\n\tbuffer_store_dwordx4 %0, %1, %2, 0 offen sc1\n\ts_nop 1" ::"v"(v), "v"(voff), "s"(d) : "memory");
}
__device__ __forceinline__ void lds_set(LAS char* p, unsigned v = 1u) { __hip_atomic_store((LAS unsigned*)p, v, __ATOMIC_RELAXED, __HIP_MEMORY_SCOPE_WORKGROUP); }
__device__ __forceinline__ void touch(u32x4 d, unsigned lds_dst, unsigned voff, unsigned soff) {
  asm volatile("s_mov_b32 m0, %2\n\ts_nop 3\n\tbuffer_load_dword %0, %1, %3 offen lds" ::"v"(voff), "s"(d), "s"(lds_dst), "s"(soff) : "memory");
}
__device__ __forceinline__ void lds_wait(LAS char* p, unsigned want = 1u) {
#pragma unroll 1
  for (unsigned spins = 0; spins < SPIN_CAP * 8; spins++) {
    if ((unsigned)__builtin_amdgcn_readfirstlane(__hip_atomic_load((LAS unsigned*)p, __ATOMIC_RELAXED, __HIP_MEMORY_SCOPE_WORKGROUP)) >= want) break;
    __builtin_amdgcn_s_sleep(1);
  }
  asm volatile("" ::: "memory");
}
__device__ __forceinline__ bf16x8 ld128(LAS char* p) { return *(LAS bf16x8*)p; }
__device__ __forceinline__ bf16x8 ldtr(LAS char* p0, LAS char* p1) {
  bf16x4 a = __builtin_amdgcn_ds_read_tr16_b64_v4bf16((LAS bf16x4*)p0);
  bf16x4 b = __builtin_amdgcn_ds_read_tr16_b64_v4bf16((LAS bf16x4*)p1);
  return __builtin_shufflevector(a, b, 0, 1, 2, 3, 4, 5, 6, 7);
}
__device__ __forceinline__ float sigmoidf_(float g) {
  return __builtin_amdgcn_rcpf(1.0f + __builtin_amdgcn_exp2f(-1.4426950408889634f * g));
}
__device__ __forceinline__ f32x2 fma2(f32x2 a, f32x2 b, f32x2 c) { return __builtin_elementwise_fma(a, b, c); }
__device__ __forceinline__ f32x2 lo2(f32x4 v) { return __builtin_shufflevector(v, v, 0, 1); }
__device__ __forceinline__ f32x2 hi2(f32x4 v) { return __builtin_shufflevector(v, v, 2, 3); }
__device__ __forceinline__ void csq(float& r, float& i) { const float t = r * r - i * i; i = 2.f * r * i; r = t; }

struct Params {
  const u16 *x, *wg, *wu, *bre, *bim, *ldt, *lre, *lim, *cre, *cim, *dsk, *wd, *gw;
  char* ws;
  float* out;
};

__device__ __forceinline__ void ssm_consts(u16 ldt, u16 lre, u16 lim, float& lr, float& li, float& cr, float& ci) {
  const float dt = rbf(expf(bf2f(ldt)));
  const float a = bf2f(lre), b = bf2f(lim);
  const float m = expf(dt * a);
  float s, c;
  sincosf(dt * b, &s, &c);
  lr = m * c;
  li = m * s;
  const float nr = lr - 1.0f, ni = li, rden = 1.0f / (a * a + b * b);
  cr = (nr * a + ni * b) * rden;
  ci = (ni * a - nr * b) * rden;
}
__device__ __forceinline__ void share2(float v, float (&o)[2]) {
  const auto t = __builtin_amdgcn_permlane16_swap(__float_as_uint(v), __float_as_uint(v), false, false);
  o[0] = __uint_as_float(t[0]); o[1] = __uint_as_float(t[1]);
}

__device__ __forceinline__ u64 ld_flag(rsrc_t r, unsigned voff, unsigned soff) {
  return __builtin_bit_cast(u64, __builtin_amdgcn_raw_buffer_load_b64(r, voff, soff, SC1));
}
__device__ __forceinline__ void st_flag(rsrc_t r, unsigned voff, unsigned soff, u64 tag) {
  __builtin_amdgcn_raw_buffer_store_b64(__builtin_bit_cast(u32x2, tag), r, voff, soff, SC1);
}
__device__ __forceinline__ void poll_flags(rsrc_t r, unsigned voff, unsigned soff, u64 tag) {
#pragma unroll 1
  for (unsigned spins = 0; spins < SPIN_CAP; spins++) {
    asm volatile("" ::: "memory");
    if (__all(ld_flag(r, voff, soff) == tag)) break;
    __builtin_amdgcn_s_sleep(2);
  }
  asm volatile("" ::: "memory");
}

template <bool GLU, int NR>
__device__ __forceinline__ void main_loop(LAS char* lds, int wr, int wc, int lane, f32x4 (&acc)[8][NR]) {
  const int i = lane & 15, g = lane >> 4;
  const int sw = (i >> 1) & 7;
  LAS char *resA[2], *rolA[4];
  if constexpr (GLU) {
    resA[0] = lds + (wr * 80 + i) * 128 + ((g ^ sw) << 4);
    resA[1] = lds + (wr * 80 + i) * 128 + ((g ^ sw ^ 4) << 4);
    const int q = (lane & 15) >> 2, pp = lane & 3;
    const int swz = q | ((g & 1) << 2);
#pragma unroll
    for (int c = 0; c < 4; c++) rolA[c] = lds + 20480 + (8 * g + q) * 256 + (((wc * 4 + c) ^ swz) << 5) + pp * 8;
  } else {
    resA[0] = lds + 32768 + (wc * 32 + i) * 128 + ((g ^ sw) << 4);
    resA[1] = lds + 32768 + (wc * 32 + i) * 128 + ((g ^ sw ^ 4) << 4);
    rolA[0] = lds + (wr * 128 + i) * 128 + ((g ^ sw) << 4);
    rolA[1] = lds + (wr * 128 + i) * 128 + ((g ^ sw ^ 4) << 4);
    rolA[2] = rolA[3] = lds;
  }
  auto ld_res = [&](int ks, int r) -> bf16x8 {
    if constexpr (GLU) return ld128(resA[ks] + r * 2048);
    else return ld128(resA[ks] + (r & 1) * 2048 + (r >> 1) * 8192);
  };
  auto ld_rol = [&](int j) -> bf16x8 {
    const int ks = j >> 3, c = j & 7;
    if constexpr (GLU) {
      LAS char* b = rolA[c & 3] + (c >> 2) * 16384 + ks * 8192;
      return ldtr(b, b + 1024);
    } else {
      return ld128(rolA[ks] + c * 2048);
    }
  };
#pragma unroll
  for (int c = 0; c < 8; c++)
#pragma unroll
    for (int r = 0; r < NR; r++) { acc[c][r] = (f32x4){0.f, 0.f, 0.f, 0.f}; PIN(acc[c][r]); }
  bf16x8 res[2][NR], rol[5];
  WAITV(0);
  BARRIER();
  rol[0] = ld_rol(0); SCHED(); res[0][0] = ld_res(0, 0); SCHED(); res[0][1] = ld_res(0, 1); SCHED();
  rol[1] = ld_rol(1); SCHED(); res[0][2] = ld_res(0, 2); SCHED(); res[0][3] = ld_res(0, 3); SCHED();
  rol[2] = ld_rol(2); SCHED();
  if constexpr (NR > 4) res[0][NR - 1] = ld_res(0, NR - 1);
  SCHED();
  int slot = 0;
#pragma unroll 1
  for (unsigned s = 0; s < 8; s++) {
#pragma unroll
    for (int j = 0; j < 16; j++) {
      int nds = 0;
      constexpr int RD = GLU ? 2 : 1;
      if (j == 12) {
        const int delta = (slot == 2) ? -2 * SLOT : SLOT;
        slot = (slot == 2) ? 0 : slot + 1;
        resA[0] += delta; resA[1] += delta;
        rolA[0] += delta; rolA[1] += delta;
        PIN(resA[0]); PIN(resA[1]); PIN(rolA[0]); PIN(rolA[1]);
        if constexpr (GLU) { rolA[2] += delta; rolA[3] += delta; PIN(rolA[2]); PIN(rolA[3]); }
      }
      if (j == 13) {
        BARRIER();
        SCHED();
      }
      if (j != 12) { rol[(j + 3) & 3] = ld_rol((j + 3) & 15); nds += RD; }
      if (j == 10) { rol[4] = ld_rol(15); nds += RD; }
      if (j >= 2 && j < 2 + NR) { res[1][j - 2] = ld_res(1, j - 2); nds++; }
      if (j == 13) { res[0][0] = ld_res(0, 0); res[0][1] = ld_res(0, 1); nds += 2; }
      if (j == 14) { res[0][2] = ld_res(0, 2); res[0][3] = ld_res(0, 3); nds += 2; }
      if (j == 15 && NR > 4) { res[0][NR - 1] = ld_res(0, NR - 1); nds++; }
#pragma unroll
      for (int r = 0; r < NR; r++)
        acc[j & 7][r] = __builtin_amdgcn_mfma_f32_16x16x32_bf16(rol[j == 15 ? 4 : (j & 3)], res[j >> 3][r], acc[j & 7][r], 0, 0, 0);
#pragma unroll
      for (int r = 0; r < NR; r++) {
        __builtin_amdgcn_sched_group_barrier(0x8, 1, 0);
        if (j == 12) __builtin_amdgcn_sched_group_barrier(0x2, 2, 0);
        if (r < nds) __builtin_amdgcn_sched_group_barrier(0x100, 1, 0);
      }
      SCHED();
    }
  }
}

__device__ __forceinline__ constexpr int wslot(int i) { return (i / 3 == 0 ? 2 : (i / 3 == 1 ? 0 : 1)) * SLOT + (i % 3) * 4096; }
__device__ __forceinline__ constexpr int gstep(int i) { return i < 7 ? 2 * i : 2 * i - 13; }
constexpr int C_TAG = LDS_CTL, C_F0 = LDS_CTL + 16, C_F1 = LDS_CTL + 32, C_F2 = LDS_CTL + 48, C_F3 = LDS_CTL + 64, C_FA = LDS_CTL + 80;
constexpr int C_DUMP = LDS_CTL + 1024;

__global__ __launch_bounds__(512, 2) void k_fused(Params p) {
  extern __shared__ __attribute__((aligned(16))) char smem[];
  LAS char* lds = (LAS char*)smem;
  const int tid = threadIdx.x, lane = tid & 63;
  u64 ka0, ka1;
  asm volatile("s_load_dwordx2 %0, %2, 0x0\n\ts_load_dwordx2 %1, %2, 0x40" : "=&s"(ka0), "=&s"(ka1) : "s"(__builtin_amdgcn_kernarg_segment_ptr()));
  const int wid = __builtin_amdgcn_readfirstlane(tid >> 6);
  const int xg = blockIdx.x & 7, idx = blockIdx.x >> 3;
  u64 pc;
  unsigned clen;
  asm volatile("s_getpc_b64 %0\n.Lgm_code_begin:\n\ts_mov_b32 %1, .Lgm_code_end-.Lgm_code_begin" : "=s"(pc), "=s"(clen));
  touch(mkdesc((const void*)pc, clen), C_DUMP, lane < 2 ? lane * 128 : OOR, (wid * 32 + idx) * 256);
  const int hh = xg & 1, cj = xg >> 1, odd = cj & 1;
  const int ncol = 4 - odd, col0 = (cj >> 1) * 7 + odd * 4;
  const int e_ = idx - 4 - odd * 8, ec = (e_ * 43) >> 8, em = e_ - ncol * 6, es = em - (hh ? ncol - 2 : 2);
  const bool ssm = e_ < 0;
  const int role = ssm ? 1 : (es < 0 ? 0 : 2 + odd);
  const int rt = ssm ? hh * 4 + (idx & 3) : (em < 0 ? hh * 7 + e_ - ec * 6 : (es < 0 ? 6 : (cj + hh) * 2 + es));
  const int ct = ssm ? (cj >> 1) * 4 + odd + (idx >> 2) : col0 + (em < 0 ? ec : hh * 2 + em);
  const int w4 = wid & 3;
  const int n = lane & 15, g = lane >> 4;
  const int k0 = (2 * cj + 4 * hh) & 7, gam = (rt + ct) & 1;
  const u32x4 xr = mkdesc(p.x, L * D * 2);
  const u32x4 w0r = mkdesc(ssm ? p.bre : p.wg, ssm ? P * D * 2 : D * F * 2), w1r = mkdesc(ssm ? p.bim : p.wu, ssm ? P * D * 2 : D * F * 2);
  unsigned vx, vw;
  {
    const int r8 = 8 * w4 + (lane >> 3), ii = r8 & 15;
    const unsigned csw = ((lane & 7) ^ (ii >> 1)) << 4;
    const int xrow = ssm ? rt * 256 + 32 * (ii >> 2) + 16 * (ii & 1) + ((ii >> 1) & 1) + (w4 & 2) : min(rt * 160, L - 160) + r8;
    vx = xrow * 1024 + csw;
    vw = ssm ? (ct * 64 + r8) * 1024 + csw : (4 * w4 + g) * (F * 2) + ct * 256 + ((n ^ ((g | ((w4 & 2) << 1)) << 1)) << 4);
  }
  const unsigned stx = ssm ? 4096 : 32768, stw = ssm ? 32768 : 16 * F * 2, sgw = ssm ? 128 : 64 * F * 2;
  const unsigned lw0 = ssm ? 32768 : 20480, lw1 = ssm ? 40960 : 36864;
  auto pieces = [&](int s, int a, int b) {
    const unsigned sg = ((s ^ gam) + k0) & 7, lb = (s % 3) * SLOT + w4 * 1024;
    auto px = [&](int i) { dma16(xr, lb + i * 4096, vx, sg * 128 + (i >> 2) * 131072 + (i & 3) * stx); };
    auto pw = [&](int k) { dma16((k & 1) ? w1r : w0r, lb + ((k & 1) ? lw1 : lw0) + (k >> 1) * 4096, vw, sg * sgw + (k >> 1) * stw); };
#pragma unroll
    for (int k = a; k < (b < 9 ? b : 9); k++) { if (k < 5) px(k); else pw(k - 5); }
    if (b > 9) {
      if (ssm) { px(5); px(6); px(7); }
      else { pw(4); pw(5); pw(6); pw(7); }
    }
  };
  unsigned c_ldt, c_lre, c_lim;
  if (wid == 0)
    asm volatile("s_mov_b32 m0, %2\n\ts_nop 3\n\tbuffer_load_dword %0, %1, 0 offen sc1 lds" ::"v"(lane * 4), "s"(mkdesc(p.ws + EPOCH_OFF, 8)), "s"(C_TAG) : "memory");
  const int chg = (ct * 64 + (wid & 1) * 32 + (lane & 31)) & (P - 1);
  if (role < 2) {
    if (wid < 4) {
      if (ssm) { c_ldt = ld2_async(p.ldt, chg * 2); c_lre = ld2_async(p.lre, chg * 2); c_lim = ld2_async(p.lim, chg * 2); }
      pieces(0, 0, 6);
    } else {
      pieces(0, 6, 10);
    }
  }
  asm volatile("s_waitcnt lgkmcnt(0)" ::"s"(ka0), "s"(ka1));
  const int myid = role == 0 ? rt * 14 + ct : (ssm ? 182 + rt * 8 + ct : (role == 2 ? 246 + rt : 254 + (cj >> 1)));
  const int rb = (xg >> 1) * 8 + (idx >> 2), cb = (xg & 1) * 4 + (idx & 3);
  const rsrc_t hr = mkrsrc(p.ws + H_OFF, (unsigned)H_BYTES), wsr = mkrsrc(p.ws, (unsigned)H_OFF);
  f32x4 acc2[4][4];
  u64 tag;
  unsigned vh[4];
  auto ldh = [&](int k, int m) -> bf16x8 {
    return __builtin_bit_cast(bf16x8, __builtin_amdgcn_raw_buffer_load_b128(hr, vh[m] + (k & 3) * 1024, (k & ~3) * 1024, SC1));
  };
  auto set_vh = [&]() { vh[1] = vh[0] + HM; vh[2] = vh[0] + 2 * HM; vh[3] = vh[0] + 3 * HM; PIN(vh[1]); PIN(vh[2]); PIN(vh[3]); };
  auto glu_flags = [&]() -> unsigned {
    const int l14 = lane < 14 ? lane : 0, j = l14 >= 7 ? l14 - 7 : l14, pr = 7 * w4 + j;
    const int mt = rb * 4 + (l14 >= 7 ? 3 : 0), om = mt < 120 ? mt / 5 : (mt < 123 ? 24 : 25);
    return (((om >> 1) * 14 + (pr >> 1)) * 4 + (om & 1) * 2 + (pr & 1)) * FS;
  };
  float gv[2];
  u32x2 gg[2], xv[2], dv;
  unsigned vg;
  auto ld_final = [&]() {
    int ln = lane;
    PIN(ln);
    const int t = rb * 64 + wid * 8 + (ln >> 4), dcol = cb * 64 + (ln & 15) * 4;
    const rsrc_t xr_ = mkrsrc(p.x, L * D * 2);
    vg = t * 8;
#pragma unroll
    for (int u = 0; u < 2; u++) {
      gg[u] = __builtin_amdgcn_raw_buffer_load_b64(wsr, vg + u * 32, (unsigned)G_OFF, SC1);
      xv[u] = __builtin_amdgcn_raw_buffer_load_b64(xr_, t * 1024 + dcol * 2, u * 4096, 0);
    }
    dv = *(const u32x2*)(p.dsk + dcol);
  };

  LAS char *sb[2], *sa[2];
  f32x4 sk[2];
  float omg[2];
  unsigned vo;
  auto prep_final = [&]() {
#pragma unroll 1
    for (unsigned spins = 0; spins < SPIN_CAP && !__all(gg[0][1] == (unsigned)tag && gg[1][1] == (unsigned)tag); spins++) {
      asm volatile("" ::: "memory");
      __builtin_amdgcn_s_sleep(2);
#pragma unroll
      for (int u = 0; u < 2; u++) gg[u] = __builtin_amdgcn_raw_buffer_load_b64(wsr, vg + u * 32, (unsigned)G_OFF, SC1);
    }
    gv[0] = __uint_as_float(gg[0][0]); gv[1] = __uint_as_float(gg[1][0]);
    int ln = lane;
    PIN(ln);
    const int nn = ln & 15, mt = wid >> 1;
    const int ob = mt < 3 ? wslot(0) + mt * 4096 : wslot(3), oa = mt == 0 ? wslot(5) : wslot(6) + (mt - 1) * 4096;
    vo = ((rb * 64 + wid * 8 + (ln >> 4)) * D + cb * 64 + nn * 4) * 4;
#pragma unroll
    for (int u = 0; u < 2; u++) {
      const int sl = ((wid & 1) * 8 + u * 4 + (ln >> 4)) ^ nn;
      sb[u] = lds + ob + nn * 256 + (sl << 4);
      sa[u] = lds + oa + nn * 256 + ((sl ^ (4 * mt)) << 4);
      omg[u] = rbf(1.0f - gv[u]);
#pragma unroll
      for (int e = 0; e < 4; e++) {
        const unsigned xw = xv[u][e >> 1], dw = dv[e >> 1];
        const float xf = __uint_as_float((e & 1) ? (xw & 0xffff0000u) : (xw << 16));
        const float df = __uint_as_float((e & 1) ? (dw & 0xffff0000u) : (dw << 16));
        sk[u][e] = rbf(xf * df);
      }
      PIN(sb[u]); PIN(sa[u]); PIN(omg[u]); PIN(sk[u]);
    }
    PIN(vo);
  };

  if (wid >= 4) {
    const int lw = w4;
    const u32x4 wsd = mkdesc(p.ws, (unsigned)H_OFF);
    const u32x4 wdr = mkdesc(p.wd, F * D * 2);
    const unsigned vwd = (lw * 14 * 32 + (lane >> 3)) * 1024 + cb * 128 + (((lane & 7) ^ (((lane >> 4) & 3) << 1)) << 4);
    u32x4 ctv;
    u64 cfl;
    auto ct_load = [&]() {
      const int b = blockIdx.x;
      ctv = ld16_async(mkdesc((b & 1) ? p.cim : p.cre, D * P * 2),
                       ((b >> 5) * 64 + ((b >> 1) & 3) * 16 + n) * 1024 + ((b >> 3) & 3) * 256 + lw * 64 + g * 16, 0);
    };
    auto ct_store = [&]() { st16_async(wsd, (unsigned)CT_OFF + (blockIdx.x * 4 + lw) * 1024 + lane * 16, ctv); };
    auto ct_flag = [&]() { st_flag(wsr, lane == 0 ? (blockIdx.x * 4 + lw) * FS : OOR, FC_OFF, tag); };
    const unsigned cfo = FC_OFF + ((cb * 4 + lw) * 32 + (lane & 31)) * FS;
    auto ct_ask = [&]() { cfl = ld8_async(wsd, cfo); };
    auto ct_check = [&]() { PIN(cfl); if (!__all(cfl == tag)) poll_flags(wsr, cfo, 0, tag); };
    u32x4 cf[32];
    const unsigned vcf = (cb * 4 + lw) * 32768 + lane * 16;
    auto ldc = [&](int a, int b) {
#pragma unroll
      for (int f = a; f < b; f++) cf[f] = ld16_async(wsd, vcf, (unsigned)CT_OFF + f * 1024);
    };
    auto wpiece = [&](int i, int k) { dma16(wdr, wslot(i < 9 ? i : i - 9) + lw * 12288 + k * 1024, vwd, (gstep(i) * 32 + k * 8) * 1024); };
    auto wround = [&](int r) {
#pragma unroll
      for (int q = 0; q < 12; q++) wpiece(r * 3 + (q >> 2), q & 3);
    };
    const u32x4 gr = mkdesc(p.wg, D * F * 2), ur = mkdesc(p.wu, D * F * 2);
    auto pre_1 = [&]() {
      const unsigned v = (((k0 + 2) & 7) * 64 + idx * 4 + lw) * (F * 2) + col0 * 256 + (lane < ncol * 16 ? lane : 0) * 16;
      dma16(gr, C_DUMP, v, 0);
      dma16(ur, C_DUMP, v, 0);
    };
    auto pre_w = [&]() { touch(wdr, C_DUMP, (lane < 14 ? lane : 0) * 128, (blockIdx.x * 4 + lw) * 1792); };
    constexpr int NP = 12, ALL = 10;
    if (role < 2) {
      if (ssm) {
        const unsigned v = (ct * 64 + (idx & 3) * 16 + lw * 4 + g) * 1024 + ((k0 + 2) & 7) * 128 + n * 16;
        dma16(w0r, C_DUMP, v, 0);
        dma16(w1r, C_DUMP, v, 0);
        pre_1(); pieces(1, 0, S1A); WAITV(4 + S1A);
      } else {
        pre_1(); pieces(1, 0, S1A); WAITV(2 + S1A);
      }
      BARRIER();
      tag = *(LAS u64*)(lds + C_TAG) + TAG_STEP;
      pieces(1, S1A, ALL); ct_load(); pieces(2, 0, S2A); WAITV(1 + S2A);
      BARRIER();
      WAITV(S2A);
      ct_store(); pieces(2, S2A, ALL); pieces(3, 0, S3A); WAITV(S3A); ct_flag();
      BARRIER();
      pieces(3, S3A, ALL); pieces(4, 0, ALL); WAITV(NP);
      BARRIER();
      if (!ssm) ct_ask();
      pieces(5, 0, ALL); WAITV(NP);
      if (!ssm) { ct_check(); ldc(0, NA); }
      pre_w();
      BARRIER();
      pieces(6, 0, ALL);
      if (!ssm) { ldc(NA, 2 * NA); WAITV(NP + 2 + 2 * NA); } else WAITV(NP + 1);
      BARRIER();
      pieces(7, 0, ALL);
      if (!ssm) { ldc(2 * NA, 3 * NA); WAITV(NP + 1 + 2 * NA); } else WAITV(NP);
      BARRIER();
      wround(0);
      if (!ssm) { ldc(3 * NA, 3 * NA + NB); WAITV(12 + NB); } else { WAITV(12); ct_ask(); }
      BARRIER();
      wround(1);
      if (!ssm) ldc(3 * NA + NB, 3 * NA + 2 * NB);
      BARRIER();
      if (ssm) { WAITV(12); ct_check(); ldc(0, 3 * NA + 2 * NB); }
      WAITV(0);
    } else {
      ct_load();
      BARRIER();
      tag = *(LAS u64*)(lds + C_TAG) + TAG_STEP;
      WAITV(0); ct_store(); WAITV(0); ct_flag();
      pre_1(); pre_w();
      wround(0); wround(1);
      poll_flags(wsr, cfo, 0, tag);
      ldc(0, 3 * NA + 2 * NB);
      WAITV(0);
      if (myid == 255 && lw == 3) {
#pragma unroll 1
        for (int k = 0; k < 4; k++) poll_flags(wsr, (k * 64 + lane) * 4 * FS, FC_OFF, tag);
        st_flag(wsr, lane == 0 ? (unsigned)EPOCH_OFF : OOR, 0, tag);
      }
    }
    lds_set(lds + C_F0 + lw * 4);
    if (role == 0) lds_wait(lds + C_FA + lw * 4);
    wround(2);
    ldc(3 * NA + 2 * NB, 32);
    const unsigned fs = FB_OFF + ((182 + (rb >> 2) * 8 + lw * 2 + ((lane >> 1) & 1)) * 4 + (rb & 2) + (lane & 1)) * FS;
    vh[0] = rb * 4 * HM + (56 + lw * 8) * 1024 + lane * 16;
    set_vh();
    LAS char* pb[4];
#pragma unroll
    for (int k = 0; k < 4; k++) {
      const int mt = n >> 2;
      pb[k] = lds + (mt == 0 ? wslot(5) : wslot(6) + (mt - 1) * 4096) + lw * 12288 + g * 256 + ((((n & 3) ^ g) | ((k ^ mt) << 2)) << 4);
      PIN(pb[k]);
    }
    ld_final();
    lds_wait(lds + C_F1 + lw * 4);
#pragma unroll
    for (int q = 0; q < 20; q++) {
      if (q == 20 - ASKD) cfl = ld8_async(wsd, fs);
      wpiece(9 + (q >> 2), q & 3);
    }
    WAITV(ASKD);
    lds_set(lds + C_F2 + lw * 4);
    PIN(cfl);
    if (!__all(cfl == tag)) { WAITV(0); lds_set(lds + C_F2 + lw * 4, 2u); poll_flags(wsr, fs, 0, tag); }
    asm volatile("" ::: "memory");
    bf16x8 hf[32];
#pragma unroll
    for (int q = 0; q < DH; q++) hf[q] = ldh(q >> 2, q & 3);
    WAITV(DH);
    lds_set(lds + C_F2 + lw * 4, 2u);
    SCHED();
    prep_final();
#pragma unroll
    for (int f = 0; f < 32; f++) PIN(cf[f]);
#pragma unroll
    for (int c = 0; c < 4; c++)
#pragma unroll
      for (int m = 0; m < 4; m++) acc2[c][m] = (f32x4){0.f, 0.f, 0.f, 0.f};
    SCHED();
#pragma unroll
    for (int k = 0; k < 8; k++) {
      if (k == 7) lds_wait(lds + C_F3 + lw * 4);
#pragma unroll
      for (int m = 0; m < 4; m++) {
#pragma unroll
        for (int c = 0; c < 4; c++)
          acc2[c][m] = __builtin_amdgcn_mfma_f32_16x16x32_bf16(__builtin_bit_cast(bf16x8, cf[c * 8 + (k & 1) * 4 + (k >> 1)]), hf[k * 4 + m], acc2[c][m], 0, 0, 0);
        if (k * 4 + m + DH < 32) hf[k * 4 + m + DH] = ldh((k * 4 + m + DH) >> 2, (m + DH) & 3);
        if (k == 7 && m > 0) {
#pragma unroll
          for (int c = 0; c < 4; c++) *(LAS f32x4*)(pb[(m - 1) ^ c] + c * 1024) = acc2[c][m - 1];
        }
        SCHED();
      }
    }
#pragma unroll
    for (int c = 0; c < 4; c++) *(LAS f32x4*)(pb[3 ^ c] + c * 1024) = acc2[c][3];
  } else {
    const int wr = wid >> 1, wc = wid & 1;
    unsigned vfo = lane == 0 && role < 2 ? (myid * 4 + wid) * FS : OOR;
    unsigned vfi = glu_flags();
    vh[0] = rb * 4 * HM + wid * 14 * 1024 + lane * 16;
    LAS char* ra0 = lds + wid * 12288 + (4 * g + (n >> 2)) * 128 + (((2 * g + (n >> 3)) & 3) << 5) + (n & 3) * 8;
    PIN(vfo); PIN(vfi); PIN(vh[0]); PIN(ra0);
    bf16x8 wf[6][4];
    LAS char* ra[3][4];
    auto ldw = [&](int i, int c) -> bf16x8 {
      const int k = i < 9 ? i : i - 9;
      LAS char* a = ra[k / 3][c] + (k % 3) * 4096;
      return ldtr(a, a + 2048);
    };
    auto grab_w = [&]() {
#pragma unroll
      for (int c = 0; c < 4; c++) {
        ra[1][c] = (LAS char*)((unsigned)(size_t)ra0 ^ (c << 5));
        ra[0][c] = ra[1][c] + 2 * SLOT;
        ra[2][c] = ra[1][c] + SLOT;
      }
      lds_wait(lds + C_F0 + wid * 4);
#pragma unroll
      for (int i = 0; i < 5; i++)
#pragma unroll
        for (int c = 0; c < 4; c++) wf[i][c] = ldw(i, c);
      asm volatile("" ::: "memory");
      lds_set(lds + C_F1 + wid * 4);
    };
    if (role == 0) {
      const int mt0 = (min(rt * 160, L - 160) >> 4) + wr * 5;
      unsigned vs[5];
#pragma unroll
      for (int m = 0; m < 5; m++) { vs[m] = mt0 + m < rt * 10 ? OOR : (mt0 + m) * HM + (ct * 4 + wc * 2) * 1024 + lane * 16; PIN(vs[m]); }
      f32x4 acc[8][5];
      main_loop<true, 5>(lds, wr, wc, lane, acc);
      tag = *(LAS u64*)(lds + C_TAG) + TAG_STEP;
#pragma unroll
      for (int h = 0; h < 2; h++) {
#pragma unroll
        for (int m = 0; m < 5; m++) {
          if (h == 1 && m == PUBA) { WAITV(PUBA); st_flag(wsr, vfo, FA_OFF, tag); }
          u32x4 o;
#pragma unroll
          for (int e = 0; e < 4; e++) {
            const int c = 2 * h + (e >> 1);
            const f32x2 gv_ = (e & 1) ? hi2(acc[c][m]) : lo2(acc[c][m]);
            const f32x2 uv = (e & 1) ? hi2(acc[4 + c][m]) : lo2(acc[4 + c][m]);
            f32x2 t = gv_ * -1.4426950408889634f;
            t = (f32x2){__builtin_amdgcn_exp2f(t.x), __builtin_amdgcn_exp2f(t.y)} + 1.0f;
            t = (f32x2){__builtin_amdgcn_rcpf(t.x), __builtin_amdgcn_rcpf(t.y)} * uv;
            o[e] = pack2(t.x, t.y);
          }
          __builtin_amdgcn_raw_buffer_store_b128(o, hr, vs[m] + h * 1024, 0, SC1);
          SCHED();
        }
      }
    } else {
      if (role == 1) {
        float lr[2], li[2], cr[2], ci[2], sr[2], si[2], mr[2], mi[2], wr_[2], wi_[2], qr, qi, zr, zi;
        {
          float a2, a3;
          WAITV(6);
          PIN(c_ldt); PIN(c_lre); PIN(c_lim);
          ssm_consts(c_ldt, c_lre, c_lim, qr, qi, a2, a3);
          share2(qr, lr); share2(qi, li); share2(2.f * a2, cr); share2(2.f * a3, ci);
          csq(qr, qi); csq(qr, qi); csq(qr, qi); csq(qr, qi);
          share2(qr, sr); share2(qi, si);
          csq(qr, qi);
          share2(qr, mr); share2(qi, mi);
          csq(qr, qi); csq(qr, qi);
          zr = qr; zi = qi;
          csq(zr, zi); csq(zr, zi); csq(zr, zi);
          if (rt * 2 + wr <= 8) { zr = 0.f; zi = 0.f; }
#pragma unroll
          for (int u = 0; u < 2; u++) {
            float ar = mr[u], ai = mi[u];
            csq(ar, ai);
            const float br_ = (g & 1) ? mr[u] : 1.f, bi_ = (g & 1) ? mi[u] : 0.f;
            wr_[u] = (g & 2) ? ar * br_ - ai * bi_ : br_;
            wi_[u] = (g & 2) ? ar * bi_ + ai * br_ : bi_;
            PIN(mr[u]); PIN(mi[u]); PIN(lr[u]); PIN(li[u]); PIN(cr[u]); PIN(ci[u]); PIN(sr[u]); PIN(si[u]); PIN(wr_[u]); PIN(wi_[u]);
          }
          PIN(qr); PIN(qi); PIN(zr); PIN(zi);
        }
        const int ck = rt * 2 + wr;
        unsigned veo = lane < 32 ? (unsigned)E_OFF + (ck * 512 + chg) * 16 : OOR;
        const int c0 = ck - 1 - 8 * (lane >> 5);
        int vei = (c0 * 512 + chg) * 16, nval = min(max(c0 + 1, 0), 8);
        const unsigned tb = (wid >> 1) * 2 * SLOT + 49152 + (wid & 1) * 2048;
        LAS char* twp = lds + tb + n * 64 + ((g ^ (((n >> 1) & 1) | ((((n >> 2) ^ (n >> 3)) & 1) << 1))) << 4);
        LAS char* trp[2];
#pragma unroll
        for (int t = 0; t < 2; t++)
          trp[t] = lds + tb + (8 * g + (n >> 2)) * 64 + (((((n >> 1) ^ (n >> 3)) & 1) | (((g ^ t) & 1) << 1)) << 4) + (n & 1) * 8;
        unsigned vso[8];
#pragma unroll
        for (int k = 0; k < 8; k++) { vso[k] = (rt * 16 + wr * 8 + k) * HM + (56 + (ct * 2 + wc) * 2) * 1024 + lane * 16; PIN(vso[k]); }
        PIN(veo); PIN(vei); PIN(nval); PIN(twp); PIN(trp[0]); PIN(trp[1]);
        f32x4 acc[8][4];
        main_loop<false, 4>(lds, wr, wc, lane, acc);
        tag = *(LAS u64*)(lds + C_TAG) + TAG_STEP;
        f32x2 er[2], ei[2];
#pragma unroll
        for (int u = 0; u < 2; u++) { er[u] = lo2(acc[0][u]); ei[u] = lo2(acc[0][2 + u]); }
#pragma unroll
        for (int st = 1; st < 16; st++)
#pragma unroll
          for (int u = 0; u < 2; u++) {
            const f32x2 l2 = {lr[u], lr[u]}, i2 = {li[u], li[u]};
            const f32x2 vr = (st & 1) ? hi2(acc[st >> 1][u]) : lo2(acc[st >> 1][u]);
            const f32x2 vi = (st & 1) ? hi2(acc[st >> 1][2 + u]) : lo2(acc[st >> 1][2 + u]);
            const f32x2 t = fma2(-i2, ei[u], fma2(l2, er[u], vr));
            ei[u] = fma2(i2, er[u], fma2(l2, ei[u], vi));
            er[u] = t;
          }
        float xs[2][4], ys[2][4];
#pragma unroll
        for (int u = 0; u < 2; u++) {
          const float a = fmaf(-si[u], ei[u].x, fmaf(sr[u], er[u].x, er[u].y)), b = fmaf(si[u], er[u].x, fmaf(sr[u], ei[u].x, ei[u].y));
          float a2[2], b2[2];
          share2(a, a2); share2(b, b2);
#pragma unroll
          for (int k = 0; k < 2; k++) {
            const auto ta = __builtin_amdgcn_permlane32_swap(__float_as_uint(a2[k]), __float_as_uint(a2[k]), false, false);
            const auto tb_ = __builtin_amdgcn_permlane32_swap(__float_as_uint(b2[k]), __float_as_uint(b2[k]), false, false);
            xs[u][k] = __uint_as_float(ta[0]); xs[u][2 + k] = __uint_as_float(ta[1]);
            ys[u][k] = __uint_as_float(tb_[0]); ys[u][2 + k] = __uint_as_float(tb_[1]);
          }
#pragma unroll
          for (int k = 1; k < 4; k++) {
            xs[u][k] = fmaf(-mi[u], ys[u][k - 1], fmaf(mr[u], xs[u][k - 1], xs[u][k]));
            ys[u][k] = fmaf(mi[u], xs[u][k - 1], fmaf(mr[u], ys[u][k - 1], ys[u][k]));
            PIN(xs[u][k]); PIN(ys[u][k]);
          }
        }
        const unsigned tg = (unsigned)tag;
        __builtin_amdgcn_raw_buffer_store_b128((u32x4){__float_as_uint(g ? xs[1][3] : xs[0][3]), tg, __float_as_uint(g ? ys[1][3] : ys[0][3]), tg},
                                               wsr, veo, 0, SC1);
        SCHED();
        float pr[2], pi[2];
#pragma unroll
        for (int u = 0; u < 2; u++) {
          pr[u] = g == 0 ? 0.f : (g == 1 ? xs[u][0] : (g == 2 ? xs[u][1] : xs[u][2]));
          pi[u] = g == 0 ? 0.f : (g == 1 ? ys[u][0] : (g == 2 ? ys[u][1] : ys[u][2]));
          PIN(pr[u]); PIN(pi[u]);
        }
        const rsrc_t erc = mkrsrc(p.ws + E_OFF, (unsigned)E_BYTES);
        u32x4 v[8];
        auto ask = [&]() {
#pragma unroll
          for (int d = 0; d < 8; d++) v[d] = __builtin_amdgcn_raw_buffer_load_b128(erc, vei - d * 8192, 0, SC1);
        };
#pragma unroll
        for (int u = 0; u < 2; u++) {
          if (u == 1) { SCHED(); ask(); SCHED(); }
          PIN(cr[u]);
#pragma unroll
          for (int m = 0; m < 8; m++) {
            const f32x4 a = acc[m][u], b = acc[m][2 + u];
            acc[m][u] = cr[u] * a - ci[u] * b;
            acc[m][2 + u] = cr[u] * b + ci[u] * a;
            PIN(acc[m][u]); PIN(acc[m][2 + u]);
          }
        }
#pragma unroll 1
        for (unsigned spins = 0; spins < SPIN_CAP; spins++) {
          int cnt = 0;
#pragma unroll
          for (int d = 0; d < 8; d++) cnt += ((v[d][1] ^ tg) | (v[d][3] ^ tg)) == 0u;
          if (__all(cnt >= nval)) break;
          asm volatile("" ::: "memory");
          __builtin_amdgcn_s_sleep(4);
          ask();
        }
        float kr[2], ki[2];
        {
          float xr_ = __uint_as_float(v[7][0]), xi_ = __uint_as_float(v[7][2]);
#pragma unroll
          for (int d = 6; d >= 0; d--) {
            const float t = fmaf(-qi, xi_, fmaf(qr, xr_, __uint_as_float(v[d][0])));
            xi_ = fmaf(qi, xr_, fmaf(qr, xi_, __uint_as_float(v[d][2])));
            xr_ = t;
            PIN(xr_); PIN(xi_);
          }
          const auto ta = __builtin_amdgcn_permlane32_swap(__float_as_uint(xr_), __float_as_uint(xr_), false, false);
          const auto tb_ = __builtin_amdgcn_permlane32_swap(__float_as_uint(xi_), __float_as_uint(xi_), false, false);
          const float fr = __uint_as_float(ta[1]), fi = __uint_as_float(tb_[1]);
          share2(fmaf(-zi, fi, fmaf(zr, fr, __uint_as_float(ta[0]))), kr);
          share2(fmaf(zi, fr, fmaf(zr, fi, __uint_as_float(tb_[0]))), ki);
        }
        f32x2 hr_[2], hi_[2];
#pragma unroll
        for (int u = 0; u < 2; u++) {
          f32x2 a, b;
          a.x = fmaf(-wi_[u], ki[u], fmaf(wr_[u], kr[u], pr[u])); b.x = fmaf(wi_[u], kr[u], fmaf(wr_[u], ki[u], pi[u]));
          a.y = fmaf(-si[u], b.x, fmaf(sr[u], a.x, er[u].x)); b.y = fmaf(si[u], a.x, fmaf(sr[u], b.x, ei[u].x));
          PIN(a); PIN(b);
          hr_[u] = cr[u] * a - ci[u] * b; hi_[u] = cr[u] * b + ci[u] * a;
        }
        bf16x8 tv[4];
#pragma unroll
        for (int qt = 0; qt < 5; qt++) {
          u32x4 o[2][2];
          if (qt < 4) {
#pragma unroll
            for (int mm = 0; mm < 2; mm++)
#pragma unroll
              for (int u = 0; u < 2; u++) {
                const int m = qt * 2 + mm;
                const f32x2 l2 = {lr[u], lr[u]}, i2 = {li[u], li[u]};
                const f32x2 r0_ = fma2(-i2, hi_[u], fma2(l2, hr_[u], lo2(acc[m][u])));
                const f32x2 i0_ = fma2(i2, hr_[u], fma2(l2, hi_[u], lo2(acc[m][2 + u])));
                hr_[u] = fma2(-i2, i0_, fma2(l2, r0_, hi2(acc[m][u])));
                hi_[u] = fma2(i2, r0_, fma2(l2, i0_, hi2(acc[m][2 + u])));
                o[0][u][mm] = pack2(r0_.x, hr_[u].x); o[0][u][2 + mm] = pack2(r0_.y, hr_[u].y);
                o[1][u][mm] = pack2(-i0_.x, -hi_[u].x); o[1][u][2 + mm] = pack2(-i0_.y, -hi_[u].y);
              }
          }
          SCHED();
          if (qt > 0) {
#pragma unroll
            for (int k = 0; k < 4; k++) __builtin_amdgcn_raw_buffer_store_b128(__builtin_bit_cast(u32x4, tv[k]), hr, vso[(k & 1) * 4 + qt - 1] + (k >> 1) * 1024, 0, SC1);
          }
          if (qt < 4) {
#pragma unroll
            for (int part = 0; part < 2; part++) {
              asm volatile("" ::: "memory");
              *(LAS u32x4*)twp = o[part][0];
              *(LAS u32x4*)(twp + 1024) = o[part][1];
              asm volatile("" ::: "memory");
#pragma unroll
              for (int rho = 0; rho < 2; rho++) tv[part * 2 + rho] = ldtr(trp[rho], trp[rho ^ 1] + 256);
            }
          }
          SCHED();
        }
      } else {
        WAITV(0);
        BARRIER();
        tag = *(LAS u64*)(lds + C_TAG) + TAG_STEP;
        if (role == 2) {
          const u32x4 gwv = *(const u32x4*)(p.gw + lane * 8);
          f32x2 gwf[4];
#pragma unroll
          for (int e = 0; e < 4; e++) gwf[e] = (f32x2){__uint_as_float(gwv[e] << 16), __uint_as_float(gwv[e] & 0xffff0000u)};
          const bf16x2 ones = {(__bf16)1.0f, (__bf16)1.0f};
#pragma unroll 1
          for (int it = 0; it < 4; it++) {
            const int r0 = rt * 256 + wid * 64 + it * 16;
            u32x4 xw[16];
#pragma unroll
            for (int r = 0; r < 16; r++) xw[r] = *(const u32x4*)(p.x + (size_t)(r0 + r) * D + lane * 8);
            float s[16];
#pragma unroll
            for (int r = 0; r < 16; r++) {
              s[r] = 0.f;
#pragma unroll
              for (int e = 0; e < 4; e++) {
                const unsigned w = xw[r][e];
                const f32x2 pr = (f32x2){__uint_as_float(w << 16), __uint_as_float(w & 0xffff0000u)} * gwf[e];
                const bf16x2 pb = {(__bf16)pr.x, (__bf16)pr.y};
                s[r] = __builtin_amdgcn_fdot2_f32_bf16(pb, ones, s[r], false);
              }
            }
#pragma unroll
            for (int r = 0; r < 8; r++) {
              const auto t = __builtin_amdgcn_permlane32_swap(__float_as_uint(s[r]), __float_as_uint(s[r + 8]), false, false);
              s[r] = __uint_as_float(t[0]) + __uint_as_float(t[1]);
            }
#pragma unroll
            for (int r = 0; r < 4; r++) {
              const auto t = __builtin_amdgcn_permlane16_swap(__float_as_uint(s[r]), __float_as_uint(s[r + 4]), false, false);
              s[r] = __uint_as_float(t[0]) + __uint_as_float(t[1]);
            }
#pragma unroll
            for (int r = 0; r < 4; r++) {
              s[r] += __uint_as_float(__builtin_amdgcn_update_dpp(0, __float_as_uint(s[r]), 0x128, 0xf, 0xf, false));
              s[r] += __uint_as_float(__builtin_amdgcn_update_dpp(0, __float_as_uint(s[r]), 0x124, 0xf, 0xf, false));
              s[r] += __uint_as_float(__builtin_amdgcn_update_dpp(0, __float_as_uint(s[r]), 0x122, 0xf, 0xf, false));
              s[r] += __uint_as_float(__builtin_amdgcn_update_dpp(0, __float_as_uint(s[r]), 0x121, 0xf, 0xf, false));
            }
            const float sv = (n & 2) ? ((n & 1) ? s[3] : s[2]) : ((n & 1) ? s[1] : s[0]);
            __builtin_amdgcn_raw_buffer_store_b64((u32x2){__float_as_uint(rbf(sigmoidf_(rbf(sv)))), (unsigned)tag}, wsr,
                                                  n < 4 ? (r0 + n + 4 * (g & 1) + 8 * (g >> 1)) * 8 : OOR, (unsigned)G_OFF, SC1);
          }
        }
      }
    }
    lds_set(lds + C_FA + wid * 4);
    {
      const u64 fv = ld_flag(wsr, vfi, FA_OFF);
      ld_final();
      set_vh();
      SCHED(); grab_w(); SCHED();
      WAITV(6);
      st_flag(wsr, vfo, FB_OFF, tag);
      if (!__all(fv == tag)) poll_flags(wsr, vfi, FA_OFF, tag);
    }
    asm volatile("" ::: "memory");
    bf16x8 hf[56];
#pragma unroll
    for (int q = 0; q < DE; q++) hf[q] = ldh(gstep(q >> 2), q & 3);
    SCHED(); prep_final();
#pragma unroll
    for (int c = 0; c < 4; c++)
#pragma unroll
      for (int m = 0; m < 4; m++) acc2[c][m] = (f32x4){0.f, 0.f, 0.f, 0.f};
    LAS char* pw[4];
    SCHED();
    u64 fbv = 0;
#pragma unroll
    for (int k = 0; k < 14; k++) {
      if (k == CHKB - 2) fbv = ld_flag(wsr, vfi, FB_OFF);
      if (k == CHKB) {
        if (!__all(fbv == tag)) poll_flags(wsr, vfi, FB_OFF, tag);
        asm volatile("" ::: "memory");
      }
      if (k == 5) lds_wait(lds + C_F2 + wid * 4);
      if (k == 8) lds_wait(lds + C_F2 + wid * 4, 2u);
      if (k == 9) lds_set(lds + C_F3 + wid * 4);
      if (k == 12) {
        int ln = lane;
        PIN(ln);
#pragma unroll
        for (int c = 0; c < 4; c++) { pw[c] = lds + wid * 12288 + c * 1024 + (ln >> 4) * 256 + (((ln & 15) ^ (4 * c + (ln >> 4))) << 4); PIN(pw[c]); }
      }
#pragma unroll
      for (int m = 0; m < 4; m++) {
        if (k + 1 >= 5 && k + 1 < 14) wf[(k + 1) % 6][m] = ldw(k + 1, m);
#pragma unroll
        for (int c = 0; c < 4; c++) acc2[c][m] = __builtin_amdgcn_mfma_f32_16x16x32_bf16(wf[k % 6][c], hf[k * 4 + m], acc2[c][m], 0, 0, 0);
        if (k * 4 + m + DE < 28) hf[k * 4 + m + DE] = ldh(gstep((k * 4 + m + DE) >> 2), (m + DE) & 3);
        if (k * 4 + m + DO >= 28 && k * 4 + m + DO < 56) hf[k * 4 + m + DO] = ldh(gstep((k * 4 + m + DO) >> 2), (m + DO) & 3);
        if (k == 13 && m > 0) {
#pragma unroll
          for (int c = 0; c < 4; c++) *(LAS f32x4*)(pw[c] + wslot(m - 1)) = acc2[c][m - 1];
        }
        SCHED();
      }
    }
#pragma unroll
    for (int c = 0; c < 4; c++) *(LAS f32x4*)(pw[c] + wslot(3)) = acc2[c][3];
  }
  BARRIER();
  {
    const rsrc_t orc = mkrsrc(p.out, L * D * 4);
#pragma unroll
    for (int u = 0; u < 2; u++) {
      f32x4 b = *(LAS f32x4*)sb[u], a = sk[u] + *(LAS f32x4*)sa[u];
#pragma unroll
      for (int w = 1; w < 4; w++) {
        b += *(LAS f32x4*)(sb[u] + w * 12288);
        a += *(LAS f32x4*)(sa[u] + w * 12288);
      }
      const f32x4 o = gv[u] * a + omg[u] * b;
      __builtin_amdgcn_raw_buffer_store_b128(__builtin_bit_cast(u32x4, o), orc, vo + u * 8192, 0, SC1 | NT);
    }
  }
  asm volatile(".Lgm_code_end:");
}

extern "C" void kernel_launch(void* const* d_in, const int* in_sizes, int n_in, void* d_out, int out_size, void* d_ws,
                              size_t ws_size, hipStream_t stream) {
  Params p;
  p.x = (const u16*)d_in[0]; p.lre = (const u16*)d_in[1]; p.lim = (const u16*)d_in[2];
  p.bre = (const u16*)d_in[3]; p.bim = (const u16*)d_in[4]; p.cre = (const u16*)d_in[5]; p.cim = (const u16*)d_in[6];
  p.ldt = (const u16*)d_in[7]; p.dsk = (const u16*)d_in[8];
  p.wg = (const u16*)d_in[10]; p.wu = (const u16*)d_in[11]; p.wd = (const u16*)d_in[12]; p.gw = (const u16*)d_in[13];
  p.ws = (char*)d_ws; p.out = (float*)d_out;
  (void)hipFuncSetAttribute((const void*)k_fused, hipFuncAttributeMaxDynamicSharedMemorySize, LDS_BYTES);
  k_fused<<<256, 512, LDS_BYTES, stream>>>(p);
}
```

```cpp
#include <hip/hip_runtime.h>
#include <stdint.h>

typedef unsigned short u16;
typedef unsigned long long u64;
typedef __bf16 bf16x8 __attribute__((ext_vector_type(8)));
typedef __bf16 bf16x4 __attribute__((ext_vector_type(4)));
typedef __bf16 bf16x2 __attribute__((ext_vector_type(2)));
typedef float f32x4 __attribute__((ext_vector_type(4)));
typedef float f32x2 __attribute__((ext_vector_type(2)));
typedef unsigned u32x4 __attribute__((ext_vector_type(4)));
typedef unsigned u32x2 __attribute__((ext_vector_type(2)));
#define LAS __attribute__((address_space(3)))
typedef __amdgpu_buffer_rsrc_t rsrc_t;

constexpr int L = 2048, D = 512, P = 512, F = 1792;
constexpr int KS2 = 88;
constexpr int SLOT = 53248;
constexpr int LDS_CTL = 3 * SLOT;
constexpr int LDS_BYTES = 163840;
constexpr int SC1 = 16, NT = 2;
constexpr size_t EPOCH_OFF = 0;
constexpr size_t G_OFF = 65536;
constexpr size_t E_OFF = 131072, E_BYTES = 16 * 512 * 16;
constexpr int FS = 128;
constexpr unsigned FA_OFF = 262144, FB_OFF = FA_OFF + 1024 * FS, FC_OFF = FB_OFF + 1024 * FS;
constexpr size_t CT_OFF = 1u << 20, CT_BYTES = 1u << 20;
constexpr int HM = KS2 * 1024;
constexpr size_t H_OFF = CT_OFF + CT_BYTES, H_BYTES = (size_t)128 * HM;
constexpr u64 TAG_STEP = 0x9E3779B97F4A7C15ull;
constexpr int NA = 3, NB = 4;
constexpr int S1A = 2, S2A = 6, S3A = 9;
constexpr int PUBA = 2;
constexpr int ASKD = 8;
constexpr int DE = 12, DO = 6, DH = 6;
constexpr int CHKB = (28 - DO) / 4;
constexpr unsigned OOR = 0x40000000u;
constexpr unsigned SPIN_CAP = 1u << 18;

#define BARRIER() asm volatile("s_waitcnt lgkmcnt(0)\n\ts_barrier" ::: "memory")
#define WAITV(n) asm volatile("s_waitcnt vmcnt(%0)" ::"i"(n) : "memory")
#define SCHED() __builtin_amdgcn_sched_barrier(0)
#define PIN(x) asm volatile("" : "+v"(x))

__device__ __forceinline__ float bf2f(u16 v) { return __uint_as_float(((unsigned)v) << 16); }
__device__ __forceinline__ float rbf(float f) { return (float)(__bf16)f; }
__device__ __forceinline__ unsigned pack2(float a, float b) {
  bf16x2 v = {(__bf16)a, (__bf16)b};
  return __builtin_bit_cast(unsigned, v);
}
__device__ __forceinline__ rsrc_t mkrsrc(const void* p, unsigned bytes) {
  return __builtin_amdgcn_make_buffer_rsrc((void*)p, 0, bytes, 0x00020000);
}
__device__ __forceinline__ u32x4 mkdesc(const void* p, unsigned bytes) {
  const u64 a = (u64)p;
  return (u32x4){(unsigned)a, (unsigned)(a >> 32) & 0xffffu, bytes, 0x00020000u};
}
__device__ __forceinline__ void dma16(u32x4 d, unsigned lds_dst, unsigned voff, unsigned soff) {
  asm volatile("s_mov_b32 m0, %2\n\ts_nop 3\n\tbuffer_load_dwordx4 %0, %1, %3 offen lds" ::"v"(voff), "s"(d), "s"(lds_dst), "s"(soff) : "memory");
}
__device__ __forceinline__ u32x4 ld16_async(u32x4 d, unsigned voff, unsigned soff) {
  u32x4 r;
  asm volatile("s_nop 4\n\tbuffer_load_dwordx4 %0, %1, %2, %3 offen sc1" : "=v"(r) : "v"(voff), "s"(d), "s"(soff) : "memory");
  return r;
}
__device__ __forceinline__ u64 ld8_async(u32x4 d, unsigned voff) {
  u64 r;
  asm volatile("s_nop 4\n\tbuffer_load_dwordx2 %0, %1, %2, 0 offen sc1" : "=v"(r) : "v"(voff), "s"(d) : "memory");
  return r;
}
__device__ __forceinline__ unsigned ld2_async(const void* base, unsigned voff) {
  unsigned r;
  asm volatile("s_nop 4\n\tglobal_load_ushort %0, %1, %2" : "=v"(r) : "v"(voff), "s"(base) : "memory");
  return r;
}
__device__ __forceinline__ void st16_async(u32x4 d, unsigned voff, u32x4 v) {
  asm volatile("s_nop 4\n\tbuffer_store_dwordx4 %0, %1, %2, 0 offen sc1\n\ts_nop 1" ::"v"(v), "v"(voff), "s"(d) : "memory");
}
__device__ __forceinline__ void lds_set(LAS char* p, unsigned v = 1u) { __hip_atomic_store((LAS unsigned*)p, v, __ATOMIC_RELAXED, __HIP_MEMORY_SCOPE_WORKGROUP); }
__device__ __forceinline__ void touch(u32x4 d, unsigned lds_dst, unsigned voff, unsigned soff) {
  asm volatile("s_mov_b32 m0, %2\n\ts_nop 3\n\tbuffer_load_dword %0, %1, %3 offen lds" ::"v"(voff), "s"(d), "s"(lds_dst), "s"(soff) : "memory");
}
__device__ __forceinline__ void lds_wait(LAS char* p, unsigned want = 1u) {
#pragma unroll 1
  for (unsigned spins = 0; spins < SPIN_CAP * 8; spins++) {
    if ((unsigned)__builtin_amdgcn_readfirstlane(__hip_atomic_load((LAS unsigned*)p, __ATOMIC_RELAXED, __HIP_MEMORY_SCOPE_WORKGROUP)) >= want) break;
    __builtin_amdgcn_s_sleep(1);
  }
  asm volatile("" ::: "memory");
}
__device__ __forceinline__ bf16x8 ld128(LAS char* p) { return *(LAS bf16x8*)p; }
__device__ __forceinline__ bf16x8 ldtr(LAS char* p0, LAS char* p1) {
  bf16x4 a = __builtin_amdgcn_ds_read_tr16_b64_v4bf16((LAS bf16x4*)p0);
  bf16x4 b = __builtin_amdgcn_ds_read_tr16_b64_v4bf16((LAS bf16x4*)p1);
  return __builtin_shufflevector(a, b, 0, 1, 2, 3, 4, 5, 6, 7);
}
__device__ __forceinline__ float sigmoidf_(float g) {
  return __builtin_amdgcn_rcpf(1.0f + __builtin_amdgcn_exp2f(-1.4426950408889634f * g));
}
__device__ __forceinline__ f32x2 fma2(f32x2 a, f32x2 b, f32x2 c) { return __builtin_elementwise_fma(a, b, c); }
__device__ __forceinline__ f32x2 lo2(f32x4 v) { return __builtin_shufflevector(v, v, 0, 1); }
__device__ __forceinline__ f32x2 hi2(f32x4 v) { return __builtin_shufflevector(v, v, 2, 3); }
__device__ __forceinline__ void csq(float& r, float& i) { const float t = r * r - i * i; i = 2.f * r * i; r = t; }

struct Params {
  const u16 *x, *wg, *wu, *bre, *bim, *ldt, *lre, *lim, *cre, *cim, *dsk, *wd, *gw;
  char* ws;
  float* out;
};

__device__ __forceinline__ void ssm_consts(u16 ldt, u16 lre, u16 lim, float& lr, float& li, float& cr, float& ci) {
  const float dt = rbf(expf(bf2f(ldt)));
  const float a = bf2f(lre), b = bf2f(lim);
  const float m = expf(dt * a);
  float s, c;
  sincosf(dt * b, &s, &c);
  lr = m * c;
  li = m * s;
  const float nr = lr - 1.0f, ni = li, rden = 1.0f / (a * a + b * b);
  cr = (nr * a + ni * b) * rden;
  ci = (ni * a - nr * b) * rden;
}
__device__ __forceinline__ void share2(float v, float (&o)[2]) {
  const auto t = __builtin_amdgcn_permlane16_swap(__float_as_uint(v), __float_as_uint(v), false, false);
  o[0] = __uint_as_float(t[0]); o[1] = __uint_as_float(t[1]);
}

__device__ __forceinline__ u64 ld_flag(rsrc_t r, unsigned voff, unsigned soff) {
  return __builtin_bit_cast(u64, __builtin_amdgcn_raw_buffer_load_b64(r, voff, soff, SC1));
}
__device__ __forceinline__ void st_flag(rsrc_t r, unsigned voff, unsigned soff, u64 tag) {
  __builtin_amdgcn_raw_buffer_store_b64(__builtin_bit_cast(u32x2, tag), r, voff, soff, SC1);
}
__device__ __forceinline__ void poll_flags(rsrc_t r, unsigned voff, unsigned soff, u64 tag) {
#pragma unroll 1
  for (unsigned spins = 0; spins < SPIN_CAP; spins++) {
    asm volatile("" ::: "memory");
    if (__all(ld_flag(r, voff, soff) == tag)) break;
    __builtin_amdgcn_s_sleep(2);
  }
  asm volatile("" ::: "memory");
}

template <bool GLU, int NR>
__device__ __forceinline__ void main_loop(LAS char* lds, int wr, int wc, int lane, f32x4 (&acc)[8][NR]) {
  const int i = lane & 15, g = lane >> 4;
  const int sw = (i >> 1) & 7;
  LAS char *resA[2], *rolA[4];
  if constexpr (GLU) {
    resA[0] = lds + (wr * 80 + i) * 128 + ((g ^ sw) << 4);
    resA[1] = lds + (wr * 80 + i) * 128 + ((g ^ sw ^ 4) << 4);
    const int q = (lane & 15) >> 2, pp = lane & 3;
    const int swz = q | ((g & 1) << 2);
#pragma unroll
    for (int c = 0; c < 4; c++) rolA[c] = lds + 20480 + (8 * g + q) * 256 + (((wc * 4 + c) ^ swz) << 5) + pp * 8;
  } else {
    resA[0] = lds + 32768 + (wc * 32 + i) * 128 + ((g ^ sw) << 4);
    resA[1] = lds + 32768 + (wc * 32 + i) * 128 + ((g ^ sw ^ 4) << 4);
    rolA[0] = lds + (wr * 128 + i) * 128 + ((g ^ sw) << 4);
    rolA[1] = lds + (wr * 128 + i) * 128 + ((g ^ sw ^ 4) << 4);
    rolA[2] = rolA[3] = lds;
  }
  auto ld_res = [&](int ks, int r) -> bf16x8 {
    if constexpr (GLU) return ld128(resA[ks] + r * 2048);
    else return ld128(resA[ks] + (r & 1) * 2048 + (r >> 1) * 8192);
  };
  auto ld_rol = [&](int j) -> bf16x8 {
    const int ks = j >> 3, c = j & 7;
    if constexpr (GLU) {
      LAS char* b = rolA[c & 3] + (c >> 2) * 16384 + ks * 8192;
      return ldtr(b, b + 1024);
    } else {
      return ld128(rolA[ks] + c * 2048);
    }
  };
#pragma unroll
  for (int c = 0; c < 8; c++)
#pragma unroll
    for (int r = 0; r < NR; r++) { acc[c][r] = (f32x4){0.f, 0.f, 0.f, 0.f}; PIN(acc[c][r]); }
  bf16x8 res[2][NR], rol[5];
  WAITV(0);
  BARRIER();
  rol[0] = ld_rol(0); SCHED(); res[0][0] = ld_res(0, 0); SCHED(); res[0][1] = ld_res(0, 1); SCHED();
  rol[1] = ld_rol(1); SCHED(); res[0][2] = ld_res(0, 2); SCHED(); res[0][3] = ld_res(0, 3); SCHED();
  rol[2] = ld_rol(2); SCHED();
  if constexpr (NR > 4) res[0][NR - 1] = ld_res(0, NR - 1);
  SCHED();
  int slot = 0;
#pragma unroll 1
  for (unsigned s = 0; s < 8; s++) {
#pragma unroll
    for (int j = 0; j < 16; j++) {
      int nds = 0;
      constexpr int RD = GLU ? 2 : 1;
      if (j == 12) {
        const int delta = (slot == 2) ? -2 * SLOT : SLOT;
        slot = (slot == 2) ? 0 : slot + 1;
        resA[0] += delta; resA[1] += delta;
        rolA[0] += delta; rolA[1] += delta;
        PIN(resA[0]); PIN(resA[1]); PIN(rolA[0]); PIN(rolA[1]);
        if constexpr (GLU) { rolA[2] += delta; rolA[3] += delta; PIN(rolA[2]); PIN(rolA[3]); }
      }
      if (j == 13) {
        BARRIER();
        SCHED();
      }
      if (j != 12) { rol[(j + 3) & 3] = ld_rol((j + 3) & 15); nds += RD; }
      if (j == 10) { rol[4] = ld_rol(15); nds += RD; }
      if (j >= 2 && j < 2 + NR) { res[1][j - 2] = ld_res(1, j - 2); nds++; }
      if (j == 13) { res[0][0] = ld_res(0, 0); res[0][1] = ld_res(0, 1); nds += 2; }
      if (j == 14) { res[0][2] = ld_res(0, 2); res[0][3] = ld_res(0, 3); nds += 2; }
      if (j == 15 && NR > 4) { res[0][NR - 1] = ld_res(0, NR - 1); nds++; }
#pragma unroll
      for (int r = 0; r < NR; r++)
        acc[j & 7][r] = __builtin_amdgcn_mfma_f32_16x16x32_bf16(rol[j == 15 ? 4 : (j & 3)], res[j >> 3][r], acc[j & 7][r], 0, 0, 0);
#pragma unroll
      for (int r = 0; r < NR; r++) {
        __builtin_amdgcn_sched_group_barrier(0x8, 1, 0);
        if (j == 12) __builtin_amdgcn_sched_group_barrier(0x2, 2, 0);
        if (r < nds) __builtin_amdgcn_sched_group_barrier(0x100, 1, 0);
      }
      SCHED();
    }
  }
}

__device__ __forceinline__ constexpr int wslot(int i) { return (i / 3 == 0 ? 2 : (i / 3 == 1 ? 0 : 1)) * SLOT + (i % 3) * 4096; }
__device__ __forceinline__ constexpr int gstep(int i) { return i < 7 ? 2 * i : 2 * i - 13; }
constexpr int C_TAG = LDS_CTL, C_F0 = LDS_CTL + 16, C_F1 = LDS_CTL + 32, C_F2 = LDS_CTL + 48, C_F3 = LDS_CTL + 64, C_FA = LDS_CTL + 80;
constexpr int C_DUMP = LDS_CTL + 1024;

__global__ __launch_bounds__(512, 2) void k_fused(Params p) {
  extern __shared__ __attribute__((aligned(16))) char smem[];
  LAS char* lds = (LAS char*)smem;
  const int tid = threadIdx.x, lane = tid & 63;
  u64 ka0, ka1;
  asm volatile("s_load_dwordx2 %0, %2, 0x0\n\ts_load_dwordx2 %1, %2, 0x40" : "=&s"(ka0), "=&s"(ka1) : "s"(__builtin_amdgcn_kernarg_segment_ptr()));
  const int wid = __builtin_amdgcn_readfirstlane(tid >> 6);
  const int xg = blockIdx.x & 7, idx = blockIdx.x >> 3;
  u64 pc;
  unsigned clen;
  asm volatile("s_getpc_b64 %0\n.Lgm_code_begin:\n\ts_mov_b32 %1, .Lgm_code_end-.Lgm_code_begin" : "=s"(pc), "=s"(clen));
  touch(mkdesc((const void*)pc, clen), C_DUMP, lane < 2 ? (wid * 32 + idx) * 256 + lane * 128 : OOR, 0);
  const int hh = xg & 1, cj = xg >> 1, odd = cj & 1;
  const int ncol = 4 - odd, col0 = (cj >> 1) * 7 + odd * 4;
  const int e_ = idx - 4 - odd * 8, ec = (e_ * 43) >> 8, em = e_ - ncol * 6, es = em - (hh ? ncol - 2 : 2);
  const bool ssm = e_ < 0;
  const int role = ssm ? 1 : (es < 0 ? 0 : 2 + odd);
  const int rt = ssm ? hh * 4 + (idx & 3) : (em < 0 ? hh * 7 + e_ - ec * 6 : (es < 0 ? 6 : (cj + hh) * 2 + es));
  const int ct = ssm ? (cj >> 1) * 4 + odd + (idx >> 2) : col0 + (em < 0 ? ec : hh * 2 + em);
  const int w4 = wid & 3;
  const int n = lane & 15, g = lane >> 4;
  const int k0 = (2 * cj + 4 * hh) & 7, gam = (rt + ct) & 1;
  const u32x4 xr = mkdesc(p.x, L * D * 2);
  const u32x4 w0r = mkdesc(ssm ? p.bre : p.wg, ssm ? P * D * 2 : D * F * 2), w1r = mkdesc(ssm ? p.bim : p.wu, ssm ? P * D * 2 : D * F * 2);
  unsigned vx, vw;
  {
    const int r8 = 8 * w4 + (lane >> 3), ii = r8 & 15;
    const unsigned csw = ((lane & 7) ^ (ii >> 1)) << 4;
    const int xrow = ssm ? rt * 256 + 32 * (ii >> 2) + 16 * (ii & 1) + ((ii >> 1) & 1) + (w4 & 2) : min(rt * 160, L - 160) + r8;
    vx = xrow * 1024 + csw;
    vw = ssm ? (ct * 64 + r8) * 1024 + csw : (4 * w4 + g) * (F * 2) + ct * 256 + ((n ^ ((g | ((w4 & 2) << 1)) << 1)) << 4);
  }
  const unsigned stx = ssm ? 4096 : 32768, stw = ssm ? 32768 : 16 * F * 2, sgw = ssm ? 128 : 64 * F * 2;
  const unsigned lw0 = ssm ? 32768 : 20480, lw1 = ssm ? 40960 : 36864;
  auto pieces = [&](int s, int a, int b) {
    const unsigned sg = ((s ^ gam) + k0) & 7, lb = (s % 3) * SLOT + w4 * 1024;
    auto px = [&](int i) { dma16(xr, lb + i * 4096, vx, sg * 128 + (i >> 2) * 131072 + (i & 3) * stx); };
    auto pw = [&](int k) { dma16((k & 1) ? w1r : w0r, lb + ((k & 1) ? lw1 : lw0) + (k >> 1) * 4096, vw, sg * sgw + (k >> 1) * stw); };
#pragma unroll
    for (int k = a; k < (b < 9 ? b : 9); k++) { if (k < 5) px(k); else pw(k - 5); }
    if (b > 9) {
      if (ssm) { px(5); px(6); px(7); }
      else { pw(4); pw(5); pw(6); pw(7); }
    }
  };
  unsigned c_ldt, c_lre, c_lim;
  if (wid == 0)
    asm volatile("s_mov_b32 m0, %2\n\ts_nop 3\n\tbuffer_load_dword %0, %1, 0 offen sc1 lds" ::"v"(lane * 4), "s"(mkdesc(p.ws + EPOCH_OFF, 8)), "s"(C_TAG) : "memory");
  const int chg = (ct * 64 + (wid & 1) * 32 + (lane & 31)) & (P - 1);
  if (role < 2) {
    if (wid < 4) {
      if (ssm) { c_ldt = ld2_async(p.ldt, chg * 2); c_lre = ld2_async(p.lre, chg * 2); c_lim = ld2_async(p.lim, chg * 2); }
      pieces(0, 0, 6);
    } else {
      pieces(0, 6, 10);
    }
  }
  asm volatile("s_waitcnt lgkmcnt(0)" ::"s"(ka0), "s"(ka1));
  const int myid = role == 0 ? rt * 14 + ct : (ssm ? 182 + rt * 8 + ct : (role == 2 ? 246 + rt : 254 + (cj >> 1)));
  const int rb = (xg >> 1) * 8 + (idx >> 2), cb = (xg & 1) * 4 + (idx & 3);
  const rsrc_t hr = mkrsrc(p.ws + H_OFF, (unsigned)H_BYTES), wsr = mkrsrc(p.ws, (unsigned)H_OFF);
  f32x4 acc2[4][4];
  u64 tag;
  unsigned vh[4];
  auto ldh = [&](int k, int m) -> bf16x8 {
    return __builtin_bit_cast(bf16x8, __builtin_amdgcn_raw_buffer_load_b128(hr, vh[m] + (k & 3) * 1024, (k & ~3) * 1024, SC1));
  };
  auto set_vh = [&]() { vh[1] = vh[0] + HM; vh[2] = vh[0] + 2 * HM; vh[3] = vh[0] + 3 * HM; PIN(vh[1]); PIN(vh[2]); PIN(vh[3]); };
  auto glu_flags = [&]() -> unsigned {
    const int l14 = lane < 14 ? lane : 0, j = l14 >= 7 ? l14 - 7 : l14, pr = 7 * w4 + j;
    const int mt = rb * 4 + (l14 >= 7 ? 3 : 0), om = mt < 120 ? mt / 5 : (mt < 123 ? 24 : 25);
    return (((om >> 1) * 14 + (pr >> 1)) * 4 + (om & 1) * 2 + (pr & 1)) * FS;
  };
  float gv[2];
  u32x2 gg[2], xv[2], dv;
  unsigned vg;
  auto ld_final = [&]() {
    int ln = lane;
    PIN(ln);
    const int t = rb * 64 + wid * 8 + (ln >> 4), dcol = cb * 64 + (ln & 15) * 4;
    const rsrc_t xr_ = mkrsrc(p.x, L * D * 2);
    vg = t * 8;
#pragma unroll
    for (int u = 0; u < 2; u++) {
      gg[u] = __builtin_amdgcn_raw_buffer_load_b64(wsr, vg + u * 32, (unsigned)G_OFF, SC1);
      xv[u] = __builtin_amdgcn_raw_buffer_load_b64(xr_, t * 1024 + dcol * 2, u * 4096, 0);
    }
    dv = *(const u32x2*)(p.dsk + dcol);
  };

  LAS char *sb[2], *sa[2];
  f32x4 sk[2];
  float omg[2];
  unsigned vo;
  auto prep_final = [&]() {
#pragma unroll 1
    for (unsigned spins = 0; spins < SPIN_CAP && !__all(gg[0][1] == (unsigned)tag && gg[1][1] == (unsigned)tag); spins++) {
      asm volatile("" ::: "memory");
      __builtin_amdgcn_s_sleep(2);
#pragma unroll
      for (int u = 0; u < 2; u++) gg[u] = __builtin_amdgcn_raw_buffer_load_b64(wsr, vg + u * 32, (unsigned)G_OFF, SC1);
    }
    gv[0] = __uint_as_float(gg[0][0]); gv[1] = __uint_as_float(gg[1][0]);
    int ln = lane;
    PIN(ln);
    const int nn = ln & 15, mt = wid >> 1;
    const int ob = mt < 3 ? wslot(0) + mt * 4096 : wslot(3), oa = mt == 0 ? wslot(5) : wslot(6) + (mt - 1) * 4096;
    vo = ((rb * 64 + wid * 8 + (ln >> 4)) * D + cb * 64 + nn * 4) * 4;
#pragma unroll
    for (int u = 0; u < 2; u++) {
      const int sl = ((wid & 1) * 8 + u * 4 + (ln >> 4)) ^ nn;
      sb[u] = lds + ob + nn * 256 + (sl << 4);
      sa[u] = lds + oa + nn * 256 + ((sl ^ (4 * mt)) << 4);
      omg[u] = rbf(1.0f - gv[u]);
#pragma unroll
      for (int e = 0; e < 4; e++) {
        const unsigned xw = xv[u][e >> 1], dw = dv[e >> 1];
        const float xf = __uint_as_float((e & 1) ? (xw & 0xffff0000u) : (xw << 16));
        const float df = __uint_as_float((e & 1) ? (dw & 0xffff0000u) : (dw << 16));
        sk[u][e] = rbf(xf * df);
      }
      PIN(sb[u]); PIN(sa[u]); PIN(omg[u]); PIN(sk[u]);
    }
    PIN(vo);
  };

  if (wid >= 4) {
    const int lw = w4;
    const u32x4 wsd = mkdesc(p.ws, (unsigned)H_OFF);
    const u32x4 wdr = mkdesc(p.wd, F * D * 2);
    const unsigned vwd = (lw * 14 * 32 + (lane >> 3)) * 1024 + cb * 128 + (((lane & 7) ^ (((lane >> 4) & 3) << 1)) << 4);
    u32x4 ctv;
    u64 cfl;
    auto ct_load = [&]() {
      const int b = blockIdx.x;
      ctv = ld16_async(mkdesc((b & 1) ? p.cim : p.cre, D * P * 2),
                       ((b >> 5) * 64 + ((b >> 1) & 3) * 16 + n) * 1024 + ((b >> 3) & 3) * 256 + lw * 64 + g * 16, 0);
    };
    auto ct_store = [&]() { st16_async(wsd, (unsigned)CT_OFF + (blockIdx.x * 4 + lw) * 1024 + lane * 16, ctv); };
    auto ct_flag = [&]() { st_flag(wsr, lane == 0 ? (blockIdx.x * 4 + lw) * FS : OOR, FC_OFF, tag); };
    const unsigned cfo = FC_OFF + ((cb * 4 + lw) * 32 + (lane & 31)) * FS;
    auto ct_ask = [&]() { cfl = ld8_async(wsd, cfo); };
    auto ct_check = [&]() { PIN(cfl); if (!__all(cfl == tag)) poll_flags(wsr, cfo, 0, tag); };
    u32x4 cf[32];
    const unsigned vcf = (cb * 4 + lw) * 32768 + lane * 16;
    auto ldc = [&](int a, int b) {
#pragma unroll
      for (int f = a; f < b; f++) cf[f] = ld16_async(wsd, vcf, (unsigned)CT_OFF + f * 1024);
    };
    auto wpiece = [&](int i, int k) { dma16(wdr, wslot(i < 9 ? i : i - 9) + lw * 12288 + k * 1024, vwd, (gstep(i) * 32 + k * 8) * 1024); };
    auto wround = [&](int r) {
#pragma unroll
      for (int q = 0; q < 12; q++) wpiece(r * 3 + (q >> 2), q & 3);
    };
    const u32x4 gr = mkdesc(p.wg, D * F * 2), ur = mkdesc(p.wu, D * F * 2);
    auto pre_1 = [&]() {
      const unsigned v = (((k0 + 2) & 7) * 64 + idx * 4 + lw) * (F * 2) + col0 * 256 + (lane < ncol * 16 ? lane : 0) * 16;
      dma16(gr, C_DUMP, v, 0);
      dma16(ur, C_DUMP, v, 0);
    };
    auto pre_w = [&]() { touch(wdr, C_DUMP, (lane < 14 ? lane : 0) * 128, (blockIdx.x * 4 + lw) * 1792); };
    constexpr int NP = 12, ALL = 10;
    if (role < 2) {
      if (ssm) {
        const unsigned v = (ct * 64 + (idx & 3) * 16 + lw * 4 + g) * 1024 + ((k0 + 2) & 7) * 128 + n * 16;
        dma16(w0r, C_DUMP, v, 0);
        dma16(w1r, C_DUMP, v, 0);
        pre_1(); pieces(1, 0, S1A); WAITV(4 + S1A);
      } else {
        pre_1(); pieces(1, 0, S1A); WAITV(2 + S1A);
      }
      BARRIER();
      tag = *(LAS u64*)(lds + C_TAG) + TAG_STEP;
      pieces(1, S1A, ALL); ct_load(); pieces(2, 0, S2A); WAITV(1 + S2A);
      BARRIER();
      WAITV(S2A);
      ct_store(); pieces(2, S2A, ALL); pieces(3, 0, S3A); WAITV(S3A); ct_flag();
      BARRIER();
      pieces(3, S3A, ALL); pieces(4, 0, ALL); WAITV(NP);
      BARRIER();
      if (!ssm) ct_ask();
      pieces(5, 0, ALL); WAITV(NP);
      if (!ssm) { ct_check(); ldc(0, NA); }
      pre_w();
      BARRIER();
      pieces(6, 0, ALL);
      if (!ssm) { ldc(NA, 2 * NA); WAITV(NP + 2 + 2 * NA); } else WAITV(NP + 1);
      BARRIER();
      pieces(7, 0, ALL);
      if (!ssm) { ldc(2 * NA, 3 * NA); WAITV(NP + 1 + 2 * NA); } else WAITV(NP);
      BARRIER();
      wround(0);
      if (!ssm) { ldc(3 * NA, 3 * NA + NB); WAITV(12 + NB); } else { WAITV(12); ct_ask(); }
      BARRIER();
      wround(1);
      if (!ssm) ldc(3 * NA + NB, 3 * NA + 2 * NB);
      BARRIER();
      if (ssm) { WAITV(12); ct_check(); ldc(0, 3 * NA + 2 * NB); }
      WAITV(0);
    } else {
      ct_load();
      BARRIER();
      tag = *(LAS u64*)(lds + C_TAG) + TAG_STEP;
      WAITV(0); ct_store(); WAITV(0); ct_flag();
      pre_1(); pre_w();
      wround(0); wround(1);
      poll_flags(wsr, cfo, 0, tag);
      ldc(0, 3 * NA + 2 * NB);
      WAITV(0);
      if (myid == 255 && lw == 3) {
#pragma unroll 1
        for (int k = 0; k < 4; k++) poll_flags(wsr, (k * 64 + lane) * 4 * FS, FC_OFF, tag);
        st_flag(wsr, lane == 0 ? (unsigned)EPOCH_OFF : OOR, 0, tag);
      }
    }
    lds_set(lds + C_F0 + lw * 4);
    if (role == 0) lds_wait(lds + C_FA + lw * 4);
    wround(2);
    ldc(3 * NA + 2 * NB, 32);
    const unsigned fs = FB_OFF + ((182 + (rb >> 2) * 8 + lw * 2 + ((lane >> 1) & 1)) * 4 + (rb & 2) + (lane & 1)) * FS;
    vh[0] = rb * 4 * HM + (56 + lw * 8) * 1024 + lane * 16;
    set_vh();
    LAS char* pb[4];
#pragma unroll
    for (int k = 0; k < 4; k++) {
      const int mt = n >> 2;
      pb[k] = lds + (mt == 0 ? wslot(5) : wslot(6) + (mt - 1) * 4096) + lw * 12288 + g * 256 + ((((n & 3) ^ g) | ((k ^ mt) << 2)) << 4);
      PIN(pb[k]);
    }
    ld_final();
    lds_wait(lds + C_F1 + lw * 4);
#pragma unroll
    for (int q = 0; q < 20; q++) {
      if (q == 20 - ASKD) cfl = ld8_async(wsd, fs);
      wpiece(9 + (q >> 2), q & 3);
    }
    WAITV(ASKD);
    lds_set(lds + C_F2 + lw * 4);
    PIN(cfl);
    if (!__all(cfl == tag)) { WAITV(0); lds_set(lds + C_F2 + lw * 4, 2u); poll_flags(wsr, fs, 0, tag); }
    asm volatile("" ::: "memory");
    bf16x8 hf[32];
#pragma unroll
    for (int q = 0; q < DH; q++) hf[q] = ldh(q >> 2, q & 3);
    WAITV(DH);
    lds_set(lds + C_F2 + lw * 4, 2u);
    SCHED();
    prep_final();
#pragma unroll
    for (int f = 0; f < 32; f++) PIN(cf[f]);
#pragma unroll
    for (int c = 0; c < 4; c++)
#pragma unroll
      for (int m = 0; m < 4; m++) acc2[c][m] = (f32x4){0.f, 0.f, 0.f, 0.f};
    SCHED();
#pragma unroll
    for (int k = 0; k < 8; k++) {
      if (k == 7) lds_wait(lds + C_F3 + lw * 4);
#pragma unroll
      for (int m = 0; m < 4; m++) {
#pragma unroll
        for (int c = 0; c < 4; c++)
          acc2[c][m] = __builtin_amdgcn_mfma_f32_16x16x32_bf16(__builtin_bit_cast(bf16x8, cf[c * 8 + (k & 1) * 4 + (k >> 1)]), hf[k * 4 + m], acc2[c][m], 0, 0, 0);
        if (k * 4 + m + DH < 32) hf[k * 4 + m + DH] = ldh((k * 4 + m + DH) >> 2, (m + DH) & 3);
        if (k == 7 && m > 0) {
#pragma unroll
          for (int c = 0; c < 4; c++) *(LAS f32x4*)(pb[(m - 1) ^ c] + c * 1024) = acc2[c][m - 1];
        }
        SCHED();
      }
    }
#pragma unroll
    for (int c = 0; c < 4; c++) *(LAS f32x4*)(pb[3 ^ c] + c * 1024) = acc2[c][3];
  } else {
    const int wr = wid >> 1, wc = wid & 1;
    unsigned vfo = lane == 0 && role < 2 ? (myid * 4 + wid) * FS : OOR;
    unsigned vfi = glu_flags();
    vh[0] = rb * 4 * HM + wid * 14 * 1024 + lane * 16;
    LAS char* ra0 = lds + wid * 12288 + (4 * g + (n >> 2)) * 128 + (((2 * g + (n >> 3)) & 3) << 5) + (n & 3) * 8;
    PIN(vfo); PIN(vfi); PIN(vh[0]); PIN(ra0);
    bf16x8 wf[6][4];
    LAS char* ra[3][4];
    auto ldw = [&](int i, int c) -> bf16x8 {
      const int k = i < 9 ? i : i - 9;
      LAS char* a = ra[k / 3][c] + (k % 3) * 4096;
      return ldtr(a, a + 2048);
    };
    auto grab_w = [&]() {
#pragma unroll
      for (int c = 0; c < 4; c++) {
        ra[1][c] = (LAS char*)(size_t)((unsigned)(size_t)ra0 ^ (c << 5));
        ra[0][c] = ra[1][c] + 2 * SLOT;
        ra[2][c] = ra[1][c] + SLOT;
      }
      lds_wait(lds + C_F0 + wid * 4);
#pragma unroll
      for (int i = 0; i < 5; i++)
#pragma unroll
        for (int c = 0; c < 4; c++) wf[i][c] = ldw(i, c);
      asm volatile("" ::: "memory");
      lds_set(lds + C_F1 + wid * 4);
    };
    if (role == 0) {
      const int mt0 = (min(rt * 160, L - 160) >> 4) + wr * 5;
      unsigned vs[5];
#pragma unroll
      for (int m = 0; m < 5; m++) { vs[m] = mt0 + m < rt * 10 ? OOR : (mt0 + m) * HM + (ct * 4 + wc * 2) * 1024 + lane * 16; PIN(vs[m]); }
      f32x4 acc[8][5];
      main_loop<true, 5>(lds, wr, wc, lane, acc);
      tag = *(LAS u64*)(lds + C_TAG) + TAG_STEP;
#pragma unroll
      for (int h = 0; h < 2; h++) {
#pragma unroll
        for (int mi = 0; mi < 5; mi++) {
          const int m = (mi + 2) % 5;
          if (h == 1 && mi == PUBA) { WAITV(PUBA); st_flag(wsr, vfo, FA_OFF, tag); }
          u32x4 o;
#pragma unroll
          for (int e = 0; e < 4; e++) {
            const int c = 2 * h + (e >> 1);
            const f32x2 gv_ = (e & 1) ? hi2(acc[c][m]) : lo2(acc[c][m]);
            const f32x2 uv = (e & 1) ? hi2(acc[4 + c][m]) : lo2(acc[4 + c][m]);
            f32x2 t = gv_ * -1.4426950408889634f;
            t = (f32x2){__builtin_amdgcn_exp2f(t.x), __builtin_amdgcn_exp2f(t.y)} + 1.0f;
            t = (f32x2){__builtin_amdgcn_rcpf(t.x), __builtin_amdgcn_rcpf(t.y)} * uv;
            o[e] = pack2(t.x, t.y);
          }
          __builtin_amdgcn_raw_buffer_store_b128(o, hr, vs[m] + h * 1024, 0, SC1);
          SCHED();
        }
      }
    } else {
      if (role == 1) {
        float lr[2], li[2], cr[2], ci[2], sr[2], si[2], mr[2], mi[2], wr_[2], wi_[2], qr, qi, zr, zi;
        {
          float a2, a3;
          WAITV(6);
          PIN(c_ldt); PIN(c_lre); PIN(c_lim);
          ssm_consts(c_ldt, c_lre, c_lim, qr, qi, a2, a3);
          share2(qr, lr); share2(qi, li); share2(2.f * a2, cr); share2(2.f * a3, ci);
          csq(qr, qi); csq(qr, qi); csq(qr, qi); csq(qr, qi);
          share2(qr, sr); share2(qi, si);
          csq(qr, qi);
          share2(qr, mr); share2(qi, mi);
          csq(qr, qi); csq(qr, qi);
          zr = qr; zi = qi;
          csq(zr, zi); csq(zr, zi); csq(zr, zi);
          if (rt * 2 + wr <= 8) { zr = 0.f; zi = 0.f; }
#pragma unroll
          for (int u = 0; u < 2; u++) {
            float ar = mr[u], ai = mi[u];
            csq(ar, ai);
            const float br_ = (g & 1) ? mr[u] : 1.f, bi_ = (g & 1) ? mi[u] : 0.f;
            wr_[u] = (g & 2) ? ar * br_ - ai * bi_ : br_;
            wi_[u] = (g & 2) ? ar * bi_ + ai * br_ : bi_;
            PIN(mr[u]); PIN(mi[u]); PIN(lr[u]); PIN(li[u]); PIN(cr[u]); PIN(ci[u]); PIN(sr[u]); PIN(si[u]); PIN(wr_[u]); PIN(wi_[u]);
          }
          PIN(qr); PIN(qi); PIN(zr); PIN(zi);
        }
        const int ck = rt * 2 + wr;
        unsigned veo = lane < 32 ? (unsigned)E_OFF + (ck * 512 + chg) * 16 : OOR;
        const int c0 = ck - 1 - 8 * (lane >> 5);
        int vei = (c0 * 512 + chg) * 16, nval = min(max(c0 + 1, 0), 8);
        const unsigned tb = (wid >> 1) * 2 * SLOT + 49152 + (wid & 1) * 2048;
        LAS char* twp = lds + tb + n * 64 + ((g ^ (((n >> 1) & 1) | ((((n >> 2) ^ (n >> 3)) & 1) << 1))) << 4);
        LAS char* trp[2];
#pragma unroll
        for (int t = 0; t < 2; t++)
          trp[t] = lds + tb + (8 * g + (n >> 2)) * 64 + (((((n >> 1) ^ (n >> 3)) & 1) | (((g ^ t) & 1) << 1)) << 4) + (n & 1) * 8;
        unsigned vso[8];
#pragma unroll
        for (int k = 0; k < 8; k++) { vso[k] = (rt * 16 + wr * 8 + k) * HM + (56 + (ct * 2 + wc) * 2) * 1024 + lane * 16; PIN(vso[k]); }
        PIN(veo); PIN(vei); PIN(nval); PIN(twp); PIN(trp[0]); PIN(trp[1]);
        f32x4 acc[8][4];
        main_loop<false, 4>(lds, wr, wc, lane, acc);
        tag = *(LAS u64*)(lds + C_TAG) + TAG_STEP;
        f32x2 er[2], ei[2];
#pragma unroll
        for (int u = 0; u < 2; u++) { er[u] = lo2(acc[0][u]); ei[u] = lo2(acc[0][2 + u]); }
#pragma unroll
        for (int st = 1; st < 16; st++)
#pragma unroll
          for (int u = 0; u < 2; u++) {
            const f32x2 l2 = {lr[u], lr[u]}, i2 = {li[u], li[u]};
            const f32x2 vr = (st & 1) ? hi2(acc[st >> 1][u]) : lo2(acc[st >> 1][u]);
            const f32x2 vi = (st & 1) ? hi2(acc[st >> 1][2 + u]) : lo2(acc[st >> 1][2 + u]);
            const f32x2 t = fma2(-i2, ei[u], fma2(l2, er[u], vr));
            ei[u] = fma2(i2, er[u], fma2(l2, ei[u], vi));
            er[u] = t;
          }
        float xs[2][4], ys[2][4];
#pragma unroll
        for (int u = 0; u < 2; u++) {
          const float a = fmaf(-si[u], ei[u].x, fmaf(sr[u], er[u].x, er[u].y)), b = fmaf(si[u], er[u].x, fmaf(sr[u], ei[u].x, ei[u].y));
          float a2[2], b2[2];
          share2(a, a2); share2(b, b2);
#pragma unroll
          for (int k = 0; k < 2; k++) {
            const auto ta = __builtin_amdgcn_permlane32_swap(__float_as_uint(a2[k]), __float_as_uint(a2[k]), false, false);
            const auto tb_ = __builtin_amdgcn_permlane32_swap(__float_as_uint(b2[k]), __float_as_uint(b2[k]), false, false);
            xs[u][k] = __uint_as_float(ta[0]); xs[u][2 + k] = __uint_as_float(ta[1]);
            ys[u][k] = __uint_as_float(tb_[0]); ys[u][2 + k] = __uint_as_float(tb_[1]);
          }
#pragma unroll
          for (int k = 1; k < 4; k++) {
            xs[u][k] = fmaf(-mi[u], ys[u][k - 1], fmaf(mr[u], xs[u][k - 1], xs[u][k]));
            ys[u][k] = fmaf(mi[u], xs[u][k - 1], fmaf(mr[u], ys[u][k - 1], ys[u][k]));
            PIN(xs[u][k]); PIN(ys[u][k]);
          }
        }
        const unsigned tg = (unsigned)tag | 1u;
        __builtin_amdgcn_raw_buffer_store_b128((u32x4){__float_as_uint(g ? xs[1][3] : xs[0][3]), tg, __float_as_uint(g ? ys[1][3] : ys[0][3]), tg},
                                               wsr, veo, 0, SC1);
        SCHED();
        float pr[2], pi[2];
#pragma unroll
        for (int u = 0; u < 2; u++) {
          pr[u] = g == 0 ? 0.f : (g == 1 ? xs[u][0] : (g == 2 ? xs[u][1] : xs[u][2]));
          pi[u] = g == 0 ? 0.f : (g == 1 ? ys[u][0] : (g == 2 ? ys[u][1] : ys[u][2]));
          PIN(pr[u]); PIN(pi[u]);
        }
        const rsrc_t erc = mkrsrc(p.ws + E_OFF, (unsigned)E_BYTES);
        u32x4 v[8];
        auto ask = [&]() {
#pragma unroll
          for (int d = 0; d < 8; d++) v[d] = __builtin_amdgcn_raw_buffer_load_b128(erc, vei - d * 8192, 0, SC1);
        };
#pragma unroll
        for (int u = 0; u < 2; u++) {
          if (u == 1) { SCHED(); ask(); SCHED(); }
          PIN(cr[u]);
#pragma unroll
          for (int m = 0; m < 8; m++) {
            const f32x4 a = acc[m][u], b = acc[m][2 + u];
            acc[m][u] = cr[u] * a - ci[u] * b;
            acc[m][2 + u] = cr[u] * b + ci[u] * a;
            PIN(acc[m][u]); PIN(acc[m][2 + u]);
          }
        }
#pragma unroll 1
        for (unsigned spins = 0; spins < SPIN_CAP; spins++) {
          int cnt = 0;
#pragma unroll
          for (int d = 0; d < 8; d++) cnt += ((v[d][1] ^ tg) | (v[d][3] ^ tg)) == 0u;
          if (__all(cnt >= nval)) break;
          asm volatile("" ::: "memory");
          __builtin_amdgcn_s_sleep(4);
          ask();
        }
        float kr[2], ki[2];
        {
          float xr_ = __uint_as_float(v[7][0]), xi_ = __uint_as_float(v[7][2]);
#pragma unroll
          for (int d = 6; d >= 0; d--) {
            const float t = fmaf(-qi, xi_, fmaf(qr, xr_, __uint_as_float(v[d][0])));
            xi_ = fmaf(qi, xr_, fmaf(qr, xi_, __uint_as_float(v[d][2])));
            xr_ = t;
            PIN(xr_); PIN(xi_);
          }
          const auto ta = __builtin_amdgcn_permlane32_swap(__float_as_uint(xr_), __float_as_uint(xr_), false, false);
          const auto tb_ = __builtin_amdgcn_permlane32_swap(__float_as_uint(xi_), __float_as_uint(xi_), false, false);
          const float fr = __uint_as_float(ta[1]), fi = __uint_as_float(tb_[1]);
          share2(fmaf(-zi, fi, fmaf(zr, fr, __uint_as_float(ta[0]))), kr);
          share2(fmaf(zi, fr, fmaf(zr, fi, __uint_as_float(tb_[0]))), ki);
        }
        f32x2 hr_[2], hi_[2];
#pragma unroll
        for (int u = 0; u < 2; u++) {
          f32x2 a, b;
          a.x = fmaf(-wi_[u], ki[u], fmaf(wr_[u], kr[u], pr[u])); b.x = fmaf(wi_[u], kr[u], fmaf(wr_[u], ki[u], pi[u]));
          a.y = fmaf(-si[u], b.x, fmaf(sr[u], a.x, er[u].x)); b.y = fmaf(si[u], a.x, fmaf(sr[u], b.x, ei[u].x));
          PIN(a); PIN(b);
          hr_[u] = cr[u] * a - ci[u] * b; hi_[u] = cr[u] * b + ci[u] * a;
        }
        bf16x8 tv[4];
#pragma unroll
        for (int qt = 0; qt < 5; qt++) {
          u32x4 o[2][2];
          if (qt < 4) {
#pragma unroll
            for (int mm = 0; mm < 2; mm++)
#pragma unroll
              for (int u = 0; u < 2; u++) {
                const int m = qt * 2 + mm;
                const f32x2 l2 = {lr[u], lr[u]}, i2 = {li[u], li[u]};
                const f32x2 r0_ = fma2(-i2, hi_[u], fma2(l2, hr_[u], lo2(acc[m][u])));
                const f32x2 i0_ = fma2(i2, hr_[u], fma2(l2, hi_[u], lo2(acc[m][2 + u])));
                hr_[u] = fma2(-i2, i0_, fma2(l2, r0_, hi2(acc[m][u])));
                hi_[u] = fma2(i2, r0_, fma2(l2, i0_, hi2(acc[m][2 + u])));
                o[0][u][mm] = pack2(r0_.x, hr_[u].x); o[0][u][2 + mm] = pack2(r0_.y, hr_[u].y);
                o[1][u][mm] = pack2(-i0_.x, -hi_[u].x); o[1][u][2 + mm] = pack2(-i0_.y, -hi_[u].y);
              }
          }
          SCHED();
          if (qt > 0) {
#pragma unroll
            for (int k = 0; k < 4; k++) __builtin_amdgcn_raw_buffer_store_b128(__builtin_bit_cast(u32x4, tv[k]), hr, vso[(k & 1) * 4 + qt - 1] + (k >> 1) * 1024, 0, SC1);
          }
          if (qt < 4) {
#pragma unroll
            for (int part = 0; part < 2; part++) {
              asm volatile("" ::: "memory");
              *(LAS u32x4*)twp = o[part][0];
              *(LAS u32x4*)(twp + 1024) = o[part][1];
              asm volatile("" ::: "memory");
#pragma unroll
              for (int rho = 0; rho < 2; rho++) tv[part * 2 + rho] = ldtr(trp[rho], trp[rho ^ 1] + 256);
            }
          }
          SCHED();
        }
      } else {
        WAITV(0);
        BARRIER();
        tag = *(LAS u64*)(lds + C_TAG) + TAG_STEP;
        if (role == 2) {
          const u32x4 gwv = *(const u32x4*)(p.gw + lane * 8);
          f32x2 gwf[4];
#pragma unroll
          for (int e = 0; e < 4; e++) gwf[e] = (f32x2){__uint_as_float(gwv[e] << 16), __uint_as_float(gwv[e] & 0xffff0000u)};
          const bf16x2 ones = {(__bf16)1.0f, (__bf16)1.0f};
#pragma unroll 1
          for (int it = 0; it < 4; it++) {
            const int r0 = rt * 256 + wid * 64 + it * 16;
            u32x4 xw[16];
#pragma unroll
            for (int r = 0; r < 16; r++) xw[r] = *(const u32x4*)(p.x + (size_t)(r0 + r) * D + lane * 8);
            float s[16];
#pragma unroll
            for (int r = 0; r < 16; r++) {
              s[r] = 0.f;
#pragma unroll
              for (int e = 0; e < 4; e++) {
                const unsigned w = xw[r][e];
                const f32x2 pr = (f32x2){__uint_as_float(w << 16), __uint_as_float(w & 0xffff0000u)} * gwf[e];
                const bf16x2 pb = {(__bf16)pr.x, (__bf16)pr.y};
                s[r] = __builtin_amdgcn_fdot2_f32_bf16(pb, ones, s[r], false);
              }
            }
#pragma unroll
            for (int r = 0; r < 8; r++) {
              const auto t = __builtin_amdgcn_permlane32_swap(__float_as_uint(s[r]), __float_as_uint(s[r + 8]), false, false);
              s[r] = __uint_as_float(t[0]) + __uint_as_float(t[1]);
            }
#pragma unroll
            for (int r = 0; r < 4; r++) {
              const auto t = __builtin_amdgcn_permlane16_swap(__float_as_uint(s[r]), __float_as_uint(s[r + 4]), false, false);
              s[r] = __uint_as_float(t[0]) + __uint_as_float(t[1]);
            }
#pragma unroll
            for (int r = 0; r < 4; r++) {
              s[r] += __uint_as_float(__builtin_amdgcn_update_dpp(0, __float_as_uint(s[r]), 0x128, 0xf, 0xf, false));
              s[r] += __uint_as_float(__builtin_amdgcn_update_dpp(0, __float_as_uint(s[r]), 0x124, 0xf, 0xf, false));
              s[r] += __uint_as_float(__builtin_amdgcn_update_dpp(0, __float_as_uint(s[r]), 0x122, 0xf, 0xf, false));
              s[r] += __uint_as_float(__builtin_amdgcn_update_dpp(0, __float_as_uint(s[r]), 0x121, 0xf, 0xf, false));
            }
            const float sv = (n & 2) ? ((n & 1) ? s[3] : s[2]) : ((n & 1) ? s[1] : s[0]);
            __builtin_amdgcn_raw_buffer_store_b64((u32x2){__float_as_uint(rbf(sigmoidf_(rbf(sv)))), (unsigned)tag}, wsr,
                                                  n < 4 ? (r0 + n + 4 * (g & 1) + 8 * (g >> 1)) * 8 : OOR, (unsigned)G_OFF, SC1);
          }
        }
      }
    }
    lds_set(lds + C_FA + wid * 4);
    {
      asm volatile("" ::: "memory");
      SCHED();
      const u64 fv = ld_flag(wsr, vfi, FA_OFF);
      ld_final();
      set_vh();
      SCHED(); grab_w(); SCHED();
      WAITV(6);
      st_flag(wsr, vfo, FB_OFF, tag);
      if (!__all(fv == tag)) poll_flags(wsr, vfi, FA_OFF, tag);
    }
    asm volatile("" ::: "memory");
    bf16x8 hf[56];
#pragma unroll
    for (int q = 0; q < DE; q++) hf[q] = ldh(gstep(q >> 2), q & 3);
    SCHED(); prep_final();
#pragma unroll
    for (int c = 0; c < 4; c++)
#pragma unroll
      for (int m = 0; m < 4; m++) acc2[c][m] = (f32x4){0.f, 0.f, 0.f, 0.f};
    LAS char* pw[4];
    SCHED();
    u64 fbv = 0;
#pragma unroll
    for (int k = 0; k < 14; k++) {
      if (k == CHKB - 2) fbv = ld_flag(wsr, vfi, FB_OFF);
      if (k == CHKB) {
        if (!__all(fbv == tag)) poll_flags(wsr, vfi, FB_OFF, tag);
        asm volatile("" ::: "memory");
      }
      if (k == 5) lds_wait(lds + C_F2 + wid * 4);
      if (k == 8) lds_wait(lds + C_F2 + wid * 4, 2u);
      if (k == 9) lds_set(lds + C_F3 + wid * 4);
      if (k == 12) {
        int ln = lane;
        PIN(ln);
#pragma unroll
        for (int c = 0; c < 4; c++) { pw[c] = lds + wid * 12288 + c * 1024 + (ln >> 4) * 256 + (((ln & 15) ^ (4 * c + (ln >> 4))) << 4); PIN(pw[c]); }
      }
#pragma unroll
      for (int m = 0; m < 4; m++) {
        if (k + 1 >= 5 && k + 1 < 14) wf[(k + 1) % 6][m] = ldw(k + 1, m);
#pragma unroll
        for (int c = 0; c < 4; c++) acc2[c][m] = __builtin_amdgcn_mfma_f32_16x16x32_bf16(wf[k % 6][c], hf[k * 4 + m], acc2[c][m], 0, 0, 0);
        if (k * 4 + m + DE < 28) hf[k * 4 + m + DE] = ldh(gstep((k * 4 + m + DE) >> 2), (m + DE) & 3);
        if (k * 4 + m + DO >= 28 && k * 4 + m + DO < 56) hf[k * 4 + m + DO] = ldh(gstep((k * 4 + m + DO) >> 2), (m + DO) & 3);
        if (k == 13 && m > 0) {
#pragma unroll
          for (int c = 0; c < 4; c++) *(LAS f32x4*)(pw[c] + wslot(m - 1)) = acc2[c][m - 1];
        }
        SCHED();
      }
    }
#pragma unroll
    for (int c = 0; c < 4; c++) *(LAS f32x4*)(pw[c] + wslot(3)) = acc2[c][3];
  }
  BARRIER();
  {
    const rsrc_t orc = mkrsrc(p.out, L * D * 4);
#pragma unroll
    for (int u = 0; u < 2; u++) {
      f32x4 b = *(LAS f32x4*)sb[u], a = sk[u] + *(LAS f32x4*)sa[u];
#pragma unroll
      for (int w = 1; w < 4; w++) {
        b += *(LAS f32x4*)(sb[u] + w * 12288);
        a += *(LAS f32x4*)(sa[u] + w * 12288);
      }
      const f32x4 o = gv[u] * a + omg[u] * b;
      __builtin_amdgcn_raw_buffer_store_b128(__builtin_bit_cast(u32x4, o), orc, vo + u * 8192, 0, SC1 | NT);
    }
  }
  asm volatile(".Lgm_code_end:");
}

extern "C" void kernel_launch(void* const* d_in, const int* in_sizes, int n_in, void* d_out, int out_size, void* d_ws,
                              size_t ws_size, hipStream_t stream) {
  Params p;
  p.x = (const u16*)d_in[0]; p.lre = (const u16*)d_in[1]; p.lim = (const u16*)d_in[2];
  p.bre = (const u16*)d_in[3]; p.bim = (const u16*)d_in[4]; p.cre = (const u16*)d_in[5]; p.cim = (const u16*)d_in[6];
  p.ldt = (const u16*)d_in[7]; p.dsk = (const u16*)d_in[8];
  p.wg = (const u16*)d_in[10]; p.wu = (const u16*)d_in[11]; p.wd = (const u16*)d_in[12]; p.gw = (const u16*)d_in[13];
  p.ws = (char*)d_ws; p.out = (float*)d_out;
  (void)hipFuncSetAttribute((const void*)k_fused, hipFuncAttributeMaxDynamicSharedMemorySize, LDS_BYTES);
  k_fused<<<256, 512, LDS_BYTES, stream>>>(p);
}
```

```cpp
#include <hip/hip_runtime.h>
#include <stdint.h>

typedef unsigned short u16;
typedef unsigned long long u64;
typedef __bf16 bf16x8 __attribute__((ext_vector_type(8)));
typedef __bf16 bf16x4 __attribute__((ext_vector_type(4)));
typedef __bf16 bf16x2 __attribute__((ext_vector_type(2)));
typedef float f32x4 __attribute__((ext_vector_type(4)));
typedef float f32x2 __attribute__((ext_vector_type(2)));
typedef unsigned u32x4 __attribute__((ext_vector_type(4)));
typedef unsigned u32x2 __attribute__((ext_vector_type(2)));
#define LAS __attribute__((address_space(3)))
typedef __amdgpu_buffer_rsrc_t rsrc_t;

constexpr int L = 2048, D = 512, P = 512, F = 1792;
constexpr int KS2 = 88;
constexpr int SLOT = 53248;
constexpr int LDS_CTL = 3 * SLOT;
constexpr int LDS_BYTES = 163840;
constexpr int SC1 = 16, NT = 2;
constexpr size_t EPOCH_OFF = 0;
constexpr size_t G_OFF = 65536;
constexpr size_t E_OFF = 131072, E_BYTES = 16 * 512 * 16;
constexpr int FS = 128;
constexpr unsigned FA_OFF = 262144, FB_OFF = FA_OFF + 1024 * FS, FC_OFF = FB_OFF + 1024 * FS;
constexpr size_t CT_OFF = 1u << 20, CT_BYTES = 1u << 20;
constexpr int HM = KS2 * 1024;
constexpr size_t H_OFF = CT_OFF + CT_BYTES, H_BYTES = (size_t)128 * HM;
constexpr u64 TAG_STEP = 0x9E3779B97F4A7C15ull;
constexpr int NA = 5, NB = 4;
constexpr int S1A = 2, S2A = 6, S3A = 9;
constexpr int PUBA = 2;
constexpr int ASKD = 8;
constexpr int DE = 12, DO = 6, DH = 6;
constexpr int CHKB = (28 - DO) / 4;
constexpr unsigned OOR = 0x40000000u;
constexpr unsigned SPIN_CAP = 1u << 18;

#define BARRIER() asm volatile("s_waitcnt lgkmcnt(0)\n\ts_barrier" ::: "memory")
#define WAITV(n) asm volatile("s_waitcnt vmcnt(%0)" ::"i"(n) : "memory")
#define SCHED() __builtin_amdgcn_sched_barrier(0)
#define PIN(x) asm volatile("" : "+v"(x))

__device__ __forceinline__ float bf2f(u16 v) { return __uint_as_float(((unsigned)v) << 16); }
__device__ __forceinline__ float rbf(float f) { return (float)(__bf16)f; }
__device__ __forceinline__ unsigned pack2(float a, float b) {
  bf16x2 v = {(__bf16)a, (__bf16)b};
  return __builtin_bit_cast(unsigned, v);
}
__device__ __forceinline__ rsrc_t mkrsrc(const void* p, unsigned bytes) {
  return __builtin_amdgcn_make_buffer_rsrc((void*)p, 0, bytes, 0x00020000);
}
__device__ __forceinline__ u32x4 mkdesc(const void* p, unsigned bytes) {
  const u64 a = (u64)p;
  return (u32x4){(unsigned)a, (unsigned)(a >> 32) & 0xffffu, bytes, 0x00020000u};
}
__device__ __forceinline__ void dma16(u32x4 d, unsigned lds_dst, unsigned voff, unsigned soff) {
  asm volatile("s_mov_b32 m0, %2\n\ts_nop 3\n\tbuffer_load_dwordx4 %0, %1, %3 offen lds" ::"v"(voff), "s"(d), "s"(lds_dst), "s"(soff) : "memory");
}
__device__ __forceinline__ u32x4 ld16_async(u32x4 d, unsigned voff, unsigned soff) {
  u32x4 r;
  asm volatile("s_nop 4\n\tbuffer_load_dwordx4 %0, %1, %2, %3 offen sc1" : "=v"(r) : "v"(voff), "s"(d), "s"(soff) : "memory");
  return r;
}
__device__ __forceinline__ u64 ld8_async(u32x4 d, unsigned voff) {
  u64 r;
  asm volatile("s_nop 4\n\tbuffer_load_dwordx2 %0, %1, %2, 0 offen sc1" : "=v"(r) : "v"(voff), "s"(d) : "memory");
  return r;
}
__device__ __forceinline__ unsigned ld2_async(const void* base, unsigned voff) {
  unsigned r;
  asm volatile("s_nop 4\n\tglobal_load_ushort %0, %1, %2" : "=v"(r) : "v"(voff), "s"(base) : "memory");
  return r;
}
__device__ __forceinline__ void st16_async(u32x4 d, unsigned voff, u32x4 v) {
  asm volatile("s_nop 4\n\tbuffer_store_dwordx4 %0, %1, %2, 0 offen sc1\n\ts_nop 1" ::"v"(v), "v"(voff), "s"(d) : "memory");
}
__device__ __forceinline__ void lds_set(LAS char* p, unsigned v = 1u) { __hip_atomic_store((LAS unsigned*)p, v, __ATOMIC_RELAXED, __HIP_MEMORY_SCOPE_WORKGROUP); }
__device__ __forceinline__ void touch(u32x4 d, unsigned lds_dst, unsigned voff, unsigned soff) {
  asm volatile("s_mov_b32 m0, %2\n\ts_nop 3\n\tbuffer_load_dword %0, %1, %3 offen lds" ::"v"(voff), "s"(d), "s"(lds_dst), "s"(soff) : "memory");
}
__device__ __forceinline__ void lds_wait(LAS char* p, unsigned want = 1u) {
#pragma unroll 1
  for (unsigned spins = 0; spins < SPIN_CAP * 8; spins++) {
    if ((unsigned)__builtin_amdgcn_readfirstlane(__hip_atomic_load((LAS unsigned*)p, __ATOMIC_RELAXED, __HIP_MEMORY_SCOPE_WORKGROUP)) >= want) break;
    __builtin_amdgcn_s_sleep(1);
  }
  asm volatile("" ::: "memory");
}
__device__ __forceinline__ bf16x8 ld128(LAS char* p) { return *(LAS bf16x8*)p; }
__device__ __forceinline__ bf16x8 ldtr(LAS char* p0, LAS char* p1) {
  bf16x4 a = __builtin_amdgcn_ds_read_tr16_b64_v4bf16((LAS bf16x4*)p0);
  bf16x4 b = __builtin_amdgcn_ds_read_tr16_b64_v4bf16((LAS bf16x4*)p1);
  return __builtin_shufflevector(a, b, 0, 1, 2, 3, 4, 5, 6, 7);
}
__device__ __forceinline__ float sigmoidf_(float g) {
  return __builtin_amdgcn_rcpf(1.0f + __builtin_amdgcn_exp2f(-1.4426950408889634f * g));
}
__device__ __forceinline__ f32x2 fma2(f32x2 a, f32x2 b, f32x2 c) { return __builtin_elementwise_fma(a, b, c); }
__device__ __forceinline__ f32x2 lo2(f32x4 v) { return __builtin_shufflevector(v, v, 0, 1); }
__device__ __forceinline__ f32x2 hi2(f32x4 v) { return __builtin_shufflevector(v, v, 2, 3); }
__device__ __forceinline__ void csq(float& r, float& i) { const float t = r * r - i * i; i = 2.f * r * i; r = t; }

struct Params {
  const u16 *x, *wg, *wu, *bre, *bim, *ldt, *lre, *lim, *cre, *cim, *dsk, *wd, *gw;
  char* ws;
  float* out;
};

__device__ __forceinline__ void ssm_consts(u16 ldt, u16 lre, u16 lim, float& lr, float& li, float& cr, float& ci) {
  const float dt = rbf(expf(bf2f(ldt)));
  const float a = bf2f(lre), b = bf2f(lim);
  const float m = expf(dt * a);
  float s, c;
  sincosf(dt * b, &s, &c);
  lr = m * c;
  li = m * s;
  const float nr = lr - 1.0f, ni = li, rden = 1.0f / (a * a + b * b);
  cr = (nr * a + ni * b) * rden;
  ci = (ni * a - nr * b) * rden;
}
__device__ __forceinline__ void share2(float v, float (&o)[2]) {
  const auto t = __builtin_amdgcn_permlane16_swap(__float_as_uint(v), __float_as_uint(v), false, false);
  o[0] = __uint_as_float(t[0]); o[1] = __uint_as_float(t[1]);
}

__device__ __forceinline__ u64 ld_flag(rsrc_t r, unsigned voff, unsigned soff) {
  return __builtin_bit_cast(u64, __builtin_amdgcn_raw_buffer_load_b64(r, voff, soff, SC1));
}
__device__ __forceinline__ void st_flag(rsrc_t r, unsigned voff, unsigned soff, u64 tag) {
  __builtin_amdgcn_raw_buffer_store_b64(__builtin_bit_cast(u32x2, tag), r, voff, soff, SC1);
}
__device__ __forceinline__ void poll_flags(rsrc_t r, unsigned voff, unsigned soff, u64 tag) {
#pragma unroll 1
  for (unsigned spins = 0; spins < SPIN_CAP; spins++) {
    asm volatile("" ::: "memory");
    if (__all(ld_flag(r, voff, soff) == tag)) break;
    __builtin_amdgcn_s_sleep(2);
  }
  asm volatile("" ::: "memory");
}

template <bool GLU, int NR>
__device__ __forceinline__ void main_loop(LAS char* lds, int wr, int wc, int lane, f32x4 (&acc)[8][NR]) {
  const int i = lane & 15, g = lane >> 4;
  const int sw = (i >> 1) & 7;
  LAS char *resA[2], *rolA[4];
  if constexpr (GLU) {
    resA[0] = lds + (wr * 80 + i) * 128 + ((g ^ sw) << 4);
    resA[1] = lds + (wr * 80 + i) * 128 + ((g ^ sw ^ 4) << 4);
    const int q = (lane & 15) >> 2, pp = lane & 3;
    const int swz = q | ((g & 1) << 2);
#pragma unroll
    for (int c = 0; c < 4; c++) rolA[c] = lds + 20480 + (8 * g + q) * 256 + (((wc * 4 + c) ^ swz) << 5) + pp * 8;
  } else {
    resA[0] = lds + 32768 + (wc * 32 + i) * 128 + ((g ^ sw) << 4);
    resA[1] = lds + 32768 + (wc * 32 + i) * 128 + ((g ^ sw ^ 4) << 4);
    rolA[0] = lds + (wr * 128 + i) * 128 + ((g ^ sw) << 4);
    rolA[1] = lds + (wr * 128 + i) * 128 + ((g ^ sw ^ 4) << 4);
    rolA[2] = rolA[3] = lds;
  }
  auto ld_res = [&](int ks, int r) -> bf16x8 {
    if constexpr (GLU) return ld128(resA[ks] + r * 2048);
    else return ld128(resA[ks] + (r & 1) * 2048 + (r >> 1) * 8192);
  };
  auto ld_rol = [&](int j) -> bf16x8 {
    const int ks = j >> 3, c = j & 7;
    if constexpr (GLU) {
      LAS char* b = rolA[c & 3] + (c >> 2) * 16384 + ks * 8192;
      return ldtr(b, b + 1024);
    } else {
      return ld128(rolA[ks] + c * 2048);
    }
  };
#pragma unroll
  for (int c = 0; c < 8; c++)
#pragma unroll
    for (int r = 0; r < NR; r++) { acc[c][r] = (f32x4){0.f, 0.f, 0.f, 0.f}; PIN(acc[c][r]); }
  bf16x8 res[2][NR], rol[5];
  WAITV(0);
  BARRIER();
  rol[0] = ld_rol(0); SCHED(); res[0][0] = ld_res(0, 0); SCHED(); res[0][1] = ld_res(0, 1); SCHED();
  rol[1] = ld_rol(1); SCHED(); res[0][2] = ld_res(0, 2); SCHED(); res[0][3] = ld_res(0, 3); SCHED();
  rol[2] = ld_rol(2); SCHED();
  if constexpr (NR > 4) res[0][NR - 1] = ld_res(0, NR - 1);
  SCHED();
  int slot = 0;
#pragma unroll 1
  for (unsigned s = 0; s < 8; s++) {
#pragma unroll
    for (int j = 0; j < 16; j++) {
      int nds = 0;
      constexpr int RD = GLU ? 2 : 1;
      if (j == 12) {
        const int delta = (slot == 2) ? -2 * SLOT : SLOT;
        slot = (slot == 2) ? 0 : slot + 1;
        resA[0] += delta; resA[1] += delta;
        rolA[0] += delta; rolA[1] += delta;
        PIN(resA[0]); PIN(resA[1]); PIN(rolA[0]); PIN(rolA[1]);
        if constexpr (GLU) { rolA[2] += delta; rolA[3] += delta; PIN(rolA[2]); PIN(rolA[3]); }
      }
      if (j == 13) {
        BARRIER();
        SCHED();
      }
      if (j != 12) { rol[(j + 3) & 3] = ld_rol((j + 3) & 15); nds += RD; }
      if (j == 10) { rol[4] = ld_rol(15); nds += RD; }
      if (j >= 2 && j < 2 + NR) { res[1][j - 2] = ld_res(1, j - 2); nds++; }
      if (j == 13) { res[0][0] = ld_res(0, 0); res[0][1] = ld_res(0, 1); nds += 2; }
      if (j == 14) { res[0][2] = ld_res(0, 2); res[0][3] = ld_res(0, 3); nds += 2; }
      if (j == 15 && NR > 4) { res[0][NR - 1] = ld_res(0, NR - 1); nds++; }
#pragma unroll
      for (int r_ = 0; r_ < NR; r_++) {
        const int r = (j & 1) ? NR - 1 - r_ : r_;
        acc[j & 7][r] = __builtin_amdgcn_mfma_f32_16x16x32_bf16(rol[j == 15 ? 4 : (j & 3)], res[j >> 3][r], acc[j & 7][r], 0, 0, 0);
      }
#pragma unroll
      for (int r = 0; r < NR; r++) {
        __builtin_amdgcn_sched_group_barrier(0x8, 1, 0);
        if (j == 12) __builtin_amdgcn_sched_group_barrier(0x2, 2, 0);
        if (r < nds) __builtin_amdgcn_sched_group_barrier(0x100, 1, 0);
      }
      SCHED();
    }
  }
}

__device__ __forceinline__ constexpr int wslot(int i) { return (i / 3 == 0 ? 2 : (i / 3 == 1 ? 0 : 1)) * SLOT + (i % 3) * 4096; }
__device__ __forceinline__ constexpr int gstep(int i) { return i < 7 ? 2 * i : 2 * i - 13; }
constexpr int C_TAG = LDS_CTL, C_F0 = LDS_CTL + 16, C_F1 = LDS_CTL + 32, C_F2 = LDS_CTL + 48, C_F3 = LDS_CTL + 64, C_FA = LDS_CTL + 80;
constexpr int C_DUMP = LDS_CTL + 1024;

__global__ __launch_bounds__(512, 2) void k_fused(Params p) {
  extern __shared__ __attribute__((aligned(16))) char smem[];
  LAS char* lds = (LAS char*)smem;
  const int tid = threadIdx.x, lane = tid & 63;
  u64 ka0, ka1;
  asm volatile("s_load_dwordx2 %0, %2, 0x0\n\ts_load_dwordx2 %1, %2, 0x40" : "=&s"(ka0), "=&s"(ka1) : "s"(__builtin_amdgcn_kernarg_segment_ptr()));
  const int wid = __builtin_amdgcn_readfirstlane(tid >> 6);
  const int xg = blockIdx.x & 7, idx = blockIdx.x >> 3;
  u64 pc;
  unsigned clen;
  asm volatile("s_getpc_b64 %0\n.Lgm_code_begin:\n\ts_mov_b32 %1, .Lgm_code_end-.Lgm_code_begin" : "=s"(pc), "=s"(clen));
  touch(mkdesc((const void*)pc, clen), C_DUMP, lane < 2 ? (wid * 32 + idx) * 256 + lane * 128 : OOR, 0);
  const int hh = xg & 1, cj = xg >> 1, odd = cj & 1;
  const int ncol = 4 - odd, col0 = (cj >> 1) * 7 + odd * 4;
  const int e_ = idx - 4 - odd * 8, ec = (e_ * 43) >> 8, em = e_ - ncol * 6, es = em - (hh ? ncol - 2 : 2);
  const bool ssm = e_ < 0;
  const int role = ssm ? 1 : (es < 0 ? 0 : 2 + odd);
  const int rt = ssm ? hh * 4 + (idx & 3) : (em < 0 ? hh * 7 + e_ - ec * 6 : (es < 0 ? 6 : (cj + hh) * 2 + es));
  const int ct = ssm ? (cj >> 1) * 4 + odd + (idx >> 2) : col0 + (em < 0 ? ec : hh * 2 + em);
  const int w4 = wid & 3;
  const int n = lane & 15, g = lane >> 4;
  const int k0 = (2 * cj + 4 * hh) & 7, gam = (rt + ct) & 1;
  const u32x4 xr = mkdesc(p.x, L * D * 2);
  const u32x4 w0r = mkdesc(ssm ? p.bre : p.wg, ssm ? P * D * 2 : D * F * 2), w1r = mkdesc(ssm ? p.bim : p.wu, ssm ? P * D * 2 : D * F * 2);
  unsigned vx, vw;
  {
    const int r8 = 8 * w4 + (lane >> 3), ii = r8 & 15;
    const unsigned csw = ((lane & 7) ^ (ii >> 1)) << 4;
    const int xrow = ssm ? rt * 256 + 32 * (ii >> 2) + 16 * (ii & 1) + ((ii >> 1) & 1) + (w4 & 2) : min(rt * 160, L - 160) + r8;
    vx = xrow * 1024 + csw;
    vw = ssm ? (ct * 64 + r8) * 1024 + csw : (4 * w4 + g) * (F * 2) + ct * 256 + ((n ^ ((g | ((w4 & 2) << 1)) << 1)) << 4);
  }
  const unsigned stx = ssm ? 4096 : 32768, stw = ssm ? 32768 : 16 * F * 2, sgw = ssm ? 128 : 64 * F * 2;
  const unsigned lw0 = ssm ? 32768 : 20480, lw1 = ssm ? 40960 : 36864;
  auto pieces = [&](int s, int a, int b) {
    const unsigned sg = ((s ^ gam) + k0) & 7, lb = (s % 3) * SLOT + w4 * 1024;
    auto px = [&](int i) { dma16(xr, lb + i * 4096, vx, sg * 128 + (i >> 2) * 131072 + (i & 3) * stx); };
    auto pw = [&](int k) { dma16((k & 1) ? w1r : w0r, lb + ((k & 1) ? lw1 : lw0) + (k >> 1) * 4096, vw, sg * sgw + (k >> 1) * stw); };
#pragma unroll
    for (int k = a; k < (b < 9 ? b : 9); k++) { if (k < 5) px(k); else pw(k - 5); }
    if (b > 9) {
      if (ssm) { px(5); px(6); px(7); }
      else { pw(4); pw(5); pw(6); pw(7); }
    }
  };
  unsigned c_ldt, c_lre, c_lim;
  if (wid == 0)
    asm volatile("s_mov_b32 m0, %2\n\ts_nop 3\n\tbuffer_load_dword %0, %1, 0 offen sc1 lds" ::"v"(lane * 4), "s"(mkdesc(p.ws + EPOCH_OFF, 8)), "s"(C_TAG) : "memory");
  const int chg = (ct * 64 + (wid & 1) * 32 + (lane & 31)) & (P - 1);
  if (role < 2) {
    if (wid < 4) {
      if (ssm) { c_ldt = ld2_async(p.ldt, chg * 2); c_lre = ld2_async(p.lre, chg * 2); c_lim = ld2_async(p.lim, chg * 2); }
      pieces(0, 0, 6);
    } else {
      pieces(0, 6, 10);
    }
  }
  asm volatile("s_waitcnt lgkmcnt(0)" ::"s"(ka0), "s"(ka1));
  const int myid = role == 0 ? rt * 14 + ct : (ssm ? 182 + rt * 8 + ct : (role == 2 ? 246 + rt : 254 + (cj >> 1)));
  const int rb = (xg >> 1) * 8 + (idx >> 2), cb = (xg & 1) * 4 + (idx & 3);
  const rsrc_t hr = mkrsrc(p.ws + H_OFF, (unsigned)H_BYTES), wsr = mkrsrc(p.ws, (unsigned)H_OFF);
  f32x4 acc2[4][4];
  u64 tag;
  unsigned vh[4];
  auto ldh = [&](int k, int m) -> bf16x8 {
    return __builtin_bit_cast(bf16x8, __builtin_amdgcn_raw_buffer_load_b128(hr, vh[m] + (k & 3) * 1024, (k & ~3) * 1024, SC1));
  };
  auto set_vh = [&]() { vh[1] = vh[0] + HM; vh[2] = vh[0] + 2 * HM; vh[3] = vh[0] + 3 * HM; PIN(vh[1]); PIN(vh[2]); PIN(vh[3]); };
  auto glu_flags = [&]() -> unsigned {
    const int l14 = lane < 14 ? lane : 0, j = l14 >= 7 ? l14 - 7 : l14, pr = 7 * w4 + j;
    const int mt = rb * 4 + (l14 >= 7 ? 3 : 0), om = mt < 120 ? mt / 5 : (mt < 123 ? 24 : 25);
    return (((om >> 1) * 14 + (pr >> 1)) * 4 + (om & 1) * 2 + (pr & 1)) * FS;
  };
  float gv[2];
  u32x2 gg[2], xv[2], dv;
  unsigned vg;
  auto ld_final = [&]() {
    int ln = lane;
    PIN(ln);
    const int t = rb * 64 + wid * 8 + (ln >> 4), dcol = cb * 64 + (ln & 15) * 4;
    const rsrc_t xr_ = mkrsrc(p.x, L * D * 2);
    vg = t * 8;
#pragma unroll
    for (int u = 0; u < 2; u++) {
      gg[u] = __builtin_amdgcn_raw_buffer_load_b64(wsr, vg + u * 32, (unsigned)G_OFF, SC1);
      xv[u] = __builtin_amdgcn_raw_buffer_load_b64(xr_, t * 1024 + dcol * 2, u * 4096, 0);
    }
    dv = *(const u32x2*)(p.dsk + dcol);
  };

  LAS char *sb[2], *sa[2];
  f32x4 sk[2];
  float omg[2];
  unsigned vo;
  auto prep_final = [&]() {
#pragma unroll 1
    for (unsigned spins = 0; spins < SPIN_CAP && !__all(gg[0][1] == (unsigned)tag && gg[1][1] == (unsigned)tag); spins++) {
      asm volatile("" ::: "memory");
      __builtin_amdgcn_s_sleep(2);
#pragma unroll
      for (int u = 0; u < 2; u++) gg[u] = __builtin_amdgcn_raw_buffer_load_b64(wsr, vg + u * 32, (unsigned)G_OFF, SC1);
    }
    gv[0] = __uint_as_float(gg[0][0]); gv[1] = __uint_as_float(gg[1][0]);
    int ln = lane;
    PIN(ln);
    const int nn = ln & 15, mt = wid >> 1;
    const int ob = mt < 3 ? wslot(0) + mt * 4096 : wslot(3), oa = mt == 0 ? wslot(5) : wslot(6) + (mt - 1) * 4096;
    vo = ((rb * 64 + wid * 8 + (ln >> 4)) * D + cb * 64 + nn * 4) * 4;
#pragma unroll
    for (int u = 0; u < 2; u++) {
      const int sl = ((wid & 1) * 8 + u * 4 + (ln >> 4)) ^ nn;
      sb[u] = lds + ob + nn * 256 + (sl << 4);
      sa[u] = lds + oa + nn * 256 + ((sl ^ (4 * mt)) << 4);
      omg[u] = rbf(1.0f - gv[u]);
#pragma unroll
      for (int e = 0; e < 4; e++) {
        const unsigned xw = xv[u][e >> 1], dw = dv[e >> 1];
        const float xf = __uint_as_float((e & 1) ? (xw & 0xffff0000u) : (xw << 16));
        const float df = __uint_as_float((e & 1) ? (dw & 0xffff0000u) : (dw << 16));
        sk[u][e] = rbf(xf * df);
      }
      PIN(sb[u]); PIN(sa[u]); PIN(omg[u]); PIN(sk[u]);
    }
    PIN(vo);
  };

  if (wid >= 4) {
    const int lw = w4;
    const u32x4 wsd = mkdesc(p.ws, (unsigned)H_OFF);
    const u32x4 wdr = mkdesc(p.wd, F * D * 2);
    const unsigned vwd = (lw * 14 * 32 + (lane >> 3)) * 1024 + cb * 128 + (((lane & 7) ^ (((lane >> 4) & 3) << 1)) << 4);
    u32x4 ctv;
    u64 cfl;
    auto ct_load = [&]() {
      const int b = blockIdx.x;
      ctv = ld16_async(mkdesc((b & 1) ? p.cim : p.cre, D * P * 2),
                       ((b >> 5) * 64 + ((b >> 1) & 3) * 16 + n) * 1024 + ((b >> 3) & 3) * 256 + lw * 64 + g * 16, 0);
    };
    auto ct_store = [&]() { st16_async(wsd, (unsigned)CT_OFF + (blockIdx.x * 4 + lw) * 1024 + lane * 16, ctv); };
    auto ct_flag = [&]() { st_flag(wsr, lane == 0 ? (blockIdx.x * 4 + lw) * FS : OOR, FC_OFF, tag); };
    const unsigned cfo = FC_OFF + ((cb * 4 + lw) * 32 + (lane & 31)) * FS;
    auto ct_ask = [&]() { cfl = ld8_async(wsd, cfo); };
    auto ct_check = [&]() { PIN(cfl); if (!__all(cfl == tag)) poll_flags(wsr, cfo, 0, tag); };
    u32x4 cf[32];
    const unsigned vcf = (cb * 4 + lw) * 32768 + lane * 16;
    auto ldc = [&](int a, int b) {
#pragma unroll
      for (int f = a; f < b; f++) cf[f] = ld16_async(wsd, vcf, (unsigned)CT_OFF + f * 1024);
    };
    auto wpiece = [&](int i, int k) { dma16(wdr, wslot(i < 9 ? i : i - 9) + lw * 12288 + k * 1024, vwd, (gstep(i) * 32 + k * 8) * 1024); };
    auto wround = [&](int r) {
#pragma unroll
      for (int q = 0; q < 12; q++) wpiece(r * 3 + (q >> 2), q & 3);
    };
    const u32x4 gr = mkdesc(p.wg, D * F * 2), ur = mkdesc(p.wu, D * F * 2);
    auto pre_1 = [&]() {
      const unsigned v = (((k0 + 2) & 7) * 64 + idx * 4 + lw) * (F * 2) + col0 * 256 + (lane < ncol * 16 ? lane : 0) * 16;
      dma16(gr, C_DUMP, v, 0);
      dma16(ur, C_DUMP, v, 0);
    };
    auto pre_w = [&]() { touch(wdr, C_DUMP, lane < 19 ? (((role ? myid - 64 : myid) * 4 + lw) * 19 + lane) * 128 : OOR, 0); };
    constexpr int NP = 12, ALL = 10;
    if (role < 2) {
      if (ssm) {
        const unsigned v = (ct * 64 + (idx & 3) * 16 + lw * 4 + g) * 1024 + ((k0 + 2) & 7) * 128 + n * 16;
        dma16(w0r, C_DUMP, v, 0);
        dma16(w1r, C_DUMP, v, 0);
      } else {
        pre_1();
      }
      pieces(1, 0, S1A); WAITV(2 + S1A);
      BARRIER();
      tag = *(LAS u64*)(lds + C_TAG) + TAG_STEP;
      pieces(1, S1A, ALL); ct_load(); pieces(2, 0, S2A); WAITV(1 + S2A);
      BARRIER();
      WAITV(S2A);
      ct_store(); pieces(2, S2A, ALL); pieces(3, 0, S3A); WAITV(S3A); ct_flag();
      BARRIER();
      pieces(3, S3A, ALL); pieces(4, 0, ALL); WAITV(NP);
      BARRIER();
      pieces(5, 0, ALL); WAITV(NP);
      if (!ssm) { ct_ask(); pre_w(); }
      BARRIER();
      pieces(6, 0, ALL);
      if (!ssm) { WAITV(NP + 2); ct_check(); ldc(0, NA); } else WAITV(NP);
      BARRIER();
      pieces(7, 0, ALL);
      if (!ssm) { ldc(NA, NA + NB); WAITV(NP + 1 + NA + NB); } else WAITV(NP);
      BARRIER();
      wround(0);
      if (!ssm) { ldc(NA + NB, NA + 2 * NB); WAITV(12 + NB); } else { WAITV(12); ct_ask(); }
      BARRIER();
      wround(1);
      if (!ssm) ldc(NA + 2 * NB, NA + 3 * NB);
      BARRIER();
      if (ssm) { WAITV(12); ct_check(); ldc(0, NA + 3 * NB); }
      WAITV(0);
    } else {
      ct_load();
      BARRIER();
      tag = *(LAS u64*)(lds + C_TAG) + TAG_STEP;
      WAITV(0); ct_store(); WAITV(0); ct_flag();
      pre_1(); pre_w();
      wround(0); wround(1);
      poll_flags(wsr, cfo, 0, tag);
      ldc(0, NA + 3 * NB);
      WAITV(0);
      if (myid == 255 && lw == 3) {
#pragma unroll 1
        for (int k = 0; k < 4; k++) poll_flags(wsr, (k * 64 + lane) * 4 * FS, FC_OFF, tag);
        st_flag(wsr, lane == 0 ? (unsigned)EPOCH_OFF : OOR, 0, tag);
      }
    }
    lds_set(lds + C_F0 + lw * 4);
    if (role == 0) lds_wait(lds + C_FA + lw * 4);
    wround(2);
    ldc(NA + 3 * NB, 32);
    const unsigned fs = FB_OFF + ((182 + (rb >> 2) * 8 + lw * 2 + ((lane >> 1) & 1)) * 4 + (rb & 2) + (lane & 1)) * FS;
    vh[0] = rb * 4 * HM + (56 + lw * 8) * 1024 + lane * 16;
    set_vh();
    LAS char* pb[4];
#pragma unroll
    for (int k = 0; k < 4; k++) {
      const int mt = n >> 2;
      pb[k] = lds + (mt == 0 ? wslot(5) : wslot(6) + (mt - 1) * 4096) + lw * 12288 + g * 256 + ((((n & 3) ^ g) | ((k ^ mt) << 2)) << 4);
      PIN(pb[k]);
    }
    ld_final();
    lds_wait(lds + C_F1 + lw * 4);
#pragma unroll
    for (int q = 0; q < 20; q++) {
      if (q == 20 - ASKD) cfl = ld8_async(wsd, fs);
      wpiece(9 + (q >> 2), q & 3);
    }
    WAITV(ASKD);
    lds_set(lds + C_F2 + lw * 4);
    PIN(cfl);
    if (!__all(cfl == tag)) { WAITV(0); lds_set(lds + C_F2 + lw * 4, 2u); poll_flags(wsr, fs, 0, tag); }
    asm volatile("" ::: "memory");
    bf16x8 hf[32];
#pragma unroll
    for (int q = 0; q < DH; q++) hf[q] = ldh(q >> 2, q & 3);
    WAITV(DH);
    lds_set(lds + C_F2 + lw * 4, 2u);
    SCHED();
    prep_final();
#pragma unroll
    for (int f = 0; f < 32; f++) PIN(cf[f]);
#pragma unroll
    for (int c = 0; c < 4; c++)
#pragma unroll
      for (int m = 0; m < 4; m++) acc2[c][m] = (f32x4){0.f, 0.f, 0.f, 0.f};
    SCHED();
#pragma unroll
    for (int k = 0; k < 8; k++) {
      if (k == 7) lds_wait(lds + C_F3 + lw * 4);
#pragma unroll
      for (int m = 0; m < 4; m++) {
#pragma unroll
        for (int c_ = 0; c_ < 4; c_++) {
          const int c = (m & 1) ? 3 - c_ : c_;
          acc2[c][m] = __builtin_amdgcn_mfma_f32_16x16x32_bf16(__builtin_bit_cast(bf16x8, cf[c * 8 + (k & 1) * 4 + (k >> 1)]), hf[k * 4 + m], acc2[c][m], 0, 0, 0);
        }
        if (k * 4 + m + DH < 32) hf[k * 4 + m + DH] = ldh((k * 4 + m + DH) >> 2, (m + DH) & 3);
        if (k == 7 && m > 0) {
#pragma unroll
          for (int c = 0; c < 4; c++) *(LAS f32x4*)(pb[(m - 1) ^ c] + c * 1024) = acc2[c][m - 1];
        }
        SCHED();
      }
    }
#pragma unroll
    for (int c = 0; c < 4; c++) *(LAS f32x4*)(pb[3 ^ c] + c * 1024) = acc2[c][3];
  } else {
    const int wr = wid >> 1, wc = wid & 1;
    unsigned vfo = lane == 0 && role < 2 ? (myid * 4 + wid) * FS : OOR;
    unsigned vfi = glu_flags();
    vh[0] = rb * 4 * HM + wid * 14 * 1024 + lane * 16;
    LAS char* ra0 = lds + wid * 12288 + (4 * g + (n >> 2)) * 128 + (((2 * g + (n >> 3)) & 3) << 5) + (n & 3) * 8;
    PIN(vfo); PIN(vfi); PIN(vh[0]); PIN(ra0);
    bf16x8 wf[6][4];
    LAS char* ra[3][4];
    auto ldw = [&](int i, int c) -> bf16x8 {
      const int k = i < 9 ? i : i - 9;
      LAS char* a = ra[k / 3][c] + (k % 3) * 4096;
      return ldtr(a, a + 2048);
    };
    auto grab_w = [&]() {
#pragma unroll
      for (int c = 0; c < 4; c++) {
        ra[1][c] = (LAS char*)(size_t)((unsigned)(size_t)ra0 ^ (c << 5));
        ra[0][c] = ra[1][c] + 2 * SLOT;
        ra[2][c] = ra[1][c] + SLOT;
      }
      lds_wait(lds + C_F0 + wid * 4);
#pragma unroll
      for (int i = 0; i < 5; i++)
#pragma unroll
        for (int c = 0; c < 4; c++) wf[i][c] = ldw(i, c);
      asm volatile("" ::: "memory");
      lds_set(lds + C_F1 + wid * 4);
    };
    if (role == 0) {
      const int mt0 = (min(rt * 160, L - 160) >> 4) + wr * 5;
      unsigned vs[5];
#pragma unroll
      for (int m = 0; m < 5; m++) { vs[m] = mt0 + m < rt * 10 ? OOR : (mt0 + m) * HM + (ct * 4 + wc * 2) * 1024 + lane * 16; PIN(vs[m]); }
      f32x4 acc[8][5];
      main_loop<true, 5>(lds, wr, wc, lane, acc);
      tag = *(LAS u64*)(lds + C_TAG) + TAG_STEP;
#pragma unroll
      for (int h = 0; h < 2; h++) {
#pragma unroll
        for (int mi = 0; mi < 5; mi++) {
          const int m = (mi + 2) % 5;
          if (h == 1 && mi == PUBA) { WAITV(PUBA); st_flag(wsr, vfo, FA_OFF, tag); }
          u32x4 o;
#pragma unroll
          for (int e = 0; e < 4; e++) {
            const int c = 2 * h + (e >> 1);
            const f32x2 gv_ = (e & 1) ? hi2(acc[c][m]) : lo2(acc[c][m]);
            const f32x2 uv = (e & 1) ? hi2(acc[4 + c][m]) : lo2(acc[4 + c][m]);
            f32x2 t = gv_ * -1.4426950408889634f;
            t = (f32x2){__builtin_amdgcn_exp2f(t.x), __builtin_amdgcn_exp2f(t.y)} + 1.0f;
            t = (f32x2){__builtin_amdgcn_rcpf(t.x), __builtin_amdgcn_rcpf(t.y)} * uv;
            o[e] = pack2(t.x, t.y);
          }
          __builtin_amdgcn_raw_buffer_store_b128(o, hr, vs[m] + h * 1024, 0, SC1);
          SCHED();
        }
      }
    } else {
      if (role == 1) {
        float lr[2], li[2], cr[2], ci[2], sr[2], si[2], mr[2], mi[2], wr_[2], wi_[2], qr, qi, zr, zi;
        {
          float a2, a3;
          WAITV(6);
          PIN(c_ldt); PIN(c_lre); PIN(c_lim);
          ssm_consts(c_ldt, c_lre, c_lim, qr, qi, a2, a3);
          share2(qr, lr); share2(qi, li); share2(2.f * a2, cr); share2(2.f * a3, ci);
          csq(qr, qi); csq(qr, qi); csq(qr, qi); csq(qr, qi);
          share2(qr, sr); share2(qi, si);
          csq(qr, qi);
          share2(qr, mr); share2(qi, mi);
          csq(qr, qi); csq(qr, qi);
          zr = qr; zi = qi;
          csq(zr, zi); csq(zr, zi); csq(zr, zi);
          if (rt * 2 + wr <= 8) { zr = 0.f; zi = 0.f; }
#pragma unroll
          for (int u = 0; u < 2; u++) {
            float ar = mr[u], ai = mi[u];
            csq(ar, ai);
            const float br_ = (g & 1) ? mr[u] : 1.f, bi_ = (g & 1) ? mi[u] : 0.f;
            wr_[u] = (g & 2) ? ar * br_ - ai * bi_ : br_;
            wi_[u] = (g & 2) ? ar * bi_ + ai * br_ : bi_;
            PIN(mr[u]); PIN(mi[u]); PIN(lr[u]); PIN(li[u]); PIN(cr[u]); PIN(ci[u]); PIN(sr[u]); PIN(si[u]); PIN(wr_[u]); PIN(wi_[u]);
          }
          PIN(qr); PIN(qi); PIN(zr); PIN(zi);
        }
        const int ck = rt * 2 + wr;
        unsigned veo = lane < 32 ? (unsigned)E_OFF + (ck * 512 + chg) * 16 : OOR;
        const int c0 = ck - 1 - 8 * (lane >> 5);
        int vei = (c0 * 512 + chg) * 16, nval = min(max(c0 + 1, 0), 8);
        const unsigned tb = (wid >> 1) * 2 * SLOT + 49152 + (wid & 1) * 2048;
        LAS char* twp = lds + tb + n * 64 + ((g ^ (((n >> 1) & 1) | ((((n >> 2) ^ (n >> 3)) & 1) << 1))) << 4);
        LAS char* trp[2];
#pragma unroll
        for (int t = 0; t < 2; t++)
          trp[t] = lds + tb + (8 * g + (n >> 2)) * 64 + (((((n >> 1) ^ (n >> 3)) & 1) | (((g ^ t) & 1) << 1)) << 4) + (n & 1) * 8;
        unsigned vso[8];
#pragma unroll
        for (int k = 0; k < 8; k++) { vso[k] = (rt * 16 + wr * 8 + k) * HM + (56 + (ct * 2 + wc) * 2) * 1024 + lane * 16; PIN(vso[k]); }
        PIN(veo); PIN(vei); PIN(nval); PIN(twp); PIN(trp[0]); PIN(trp[1]);
        f32x4 acc[8][4];
        main_loop<false, 4>(lds, wr, wc, lane, acc);
        tag = *(LAS u64*)(lds + C_TAG) + TAG_STEP;
        f32x2 er[2], ei[2];
#pragma unroll
        for (int u = 0; u < 2; u++) { er[u] = lo2(acc[0][u]); ei[u] = lo2(acc[0][2 + u]); }
#pragma unroll
        for (int st = 1; st < 16; st++)
#pragma unroll
          for (int u = 0; u < 2; u++) {
            const f32x2 l2 = {lr[u], lr[u]}, i2 = {li[u], li[u]};
            const f32x2 vr = (st & 1) ? hi2(acc[st >> 1][u]) : lo2(acc[st >> 1][u]);
            const f32x2 vi = (st & 1) ? hi2(acc[st >> 1][2 + u]) : lo2(acc[st >> 1][2 + u]);
            const f32x2 t = fma2(-i2, ei[u], fma2(l2, er[u], vr));
            ei[u] = fma2(i2, er[u], fma2(l2, ei[u], vi));
            er[u] = t;
          }
        float xs[2][4], ys[2][4];
#pragma unroll
        for (int u = 0; u < 2; u++) {
          const float a = fmaf(-si[u], ei[u].x, fmaf(sr[u], er[u].x, er[u].y)), b = fmaf(si[u], er[u].x, fmaf(sr[u], ei[u].x, ei[u].y));
          float a2[2], b2[2];
          share2(a, a2); share2(b, b2);
#pragma unroll
          for (int k = 0; k < 2; k++) {
            const auto ta = __builtin_amdgcn_permlane32_swap(__float_as_uint(a2[k]), __float_as_uint(a2[k]), false, false);
            const auto tb_ = __builtin_amdgcn_permlane32_swap(__float_as_uint(b2[k]), __float_as_uint(b2[k]), false, false);
            xs[u][k] = __uint_as_float(ta[0]); xs[u][2 + k] = __uint_as_float(ta[1]);
            ys[u][k] = __uint_as_float(tb_[0]); ys[u][2 + k] = __uint_as_float(tb_[1]);
          }
#pragma unroll
          for (int k = 1; k < 4; k++) {
            xs[u][k] = fmaf(-mi[u], ys[u][k - 1], fmaf(mr[u], xs[u][k - 1], xs[u][k]));
            ys[u][k] = fmaf(mi[u], xs[u][k - 1], fmaf(mr[u], ys[u][k - 1], ys[u][k]));
            PIN(xs[u][k]); PIN(ys[u][k]);
          }
        }
        const unsigned tg = (unsigned)tag | 1u;
        __builtin_amdgcn_raw_buffer_store_b128((u32x4){__float_as_uint(g ? xs[1][3] : xs[0][3]), tg, __float_as_uint(g ? ys[1][3] : ys[0][3]), tg},
                                               wsr, veo, 0, SC1);
        SCHED();
        float pr[2], pi[2];
#pragma unroll
        for (int u = 0; u < 2; u++) {
          pr[u] = g == 0 ? 0.f : (g == 1 ? xs[u][0] : (g == 2 ? xs[u][1] : xs[u][2]));
          pi[u] = g == 0 ? 0.f : (g == 1 ? ys[u][0] : (g == 2 ? ys[u][1] : ys[u][2]));
          PIN(pr[u]); PIN(pi[u]);
        }
        const rsrc_t erc = mkrsrc(p.ws + E_OFF, (unsigned)E_BYTES);
        u32x4 v[8];
        auto ask = [&]() {
#pragma unroll
          for (int d = 0; d < 8; d++) v[d] = __builtin_amdgcn_raw_buffer_load_b128(erc, vei - d * 8192, 0, SC1);
        };
#pragma unroll
        for (int u = 0; u < 2; u++) {
          if (u == 1) { SCHED(); ask(); SCHED(); }
          PIN(cr[u]);
#pragma unroll
          for (int m = 0; m < 8; m++) {
            const f32x4 a = acc[m][u], b = acc[m][2 + u];
            acc[m][u] = cr[u] * a - ci[u] * b;
            acc[m][2 + u] = cr[u] * b + ci[u] * a;
            PIN(acc[m][u]); PIN(acc[m][2 + u]);
          }
        }
#pragma unroll 1
        for (unsigned spins = 0; spins < SPIN_CAP; spins++) {
          int cnt = 0;
#pragma unroll
          for (int d = 0; d < 8; d++) cnt += ((v[d][1] ^ tg) | (v[d][3] ^ tg)) == 0u;
          if (__all(cnt >= nval)) break;
          asm volatile("" ::: "memory");
          __builtin_amdgcn_s_sleep(4);
          ask();
        }
        float kr[2], ki[2];
        {
          float xr_ = __uint_as_float(v[7][0]), xi_ = __uint_as_float(v[7][2]);
#pragma unroll
          for (int d = 6; d >= 0; d--) {
            const float t = fmaf(-qi, xi_, fmaf(qr, xr_, __uint_as_float(v[d][0])));
            xi_ = fmaf(qi, xr_, fmaf(qr, xi_, __uint_as_float(v[d][2])));
            xr_ = t;
            PIN(xr_); PIN(xi_);
          }
          const auto ta = __builtin_amdgcn_permlane32_swap(__float_as_uint(xr_), __float_as_uint(xr_), false, false);
          const auto tb_ = __builtin_amdgcn_permlane32_swap(__float_as_uint(xi_), __float_as_uint(xi_), false, false);
          const float fr = __uint_as_float(ta[1]), fi = __uint_as_float(tb_[1]);
          share2(fmaf(-zi, fi, fmaf(zr, fr, __uint_as_float(ta[0]))), kr);
          share2(fmaf(zi, fr, fmaf(zr, fi, __uint_as_float(tb_[0]))), ki);
        }
        f32x2 hr_[2], hi_[2];
#pragma unroll
        for (int u = 0; u < 2; u++) {
          f32x2 a, b;
          a.x = fmaf(-wi_[u], ki[u], fmaf(wr_[u], kr[u], pr[u])); b.x = fmaf(wi_[u], kr[u], fmaf(wr_[u], ki[u], pi[u]));
          a.y = fmaf(-si[u], b.x, fmaf(sr[u], a.x, er[u].x)); b.y = fmaf(si[u], a.x, fmaf(sr[u], b.x, ei[u].x));
          PIN(a); PIN(b);
          hr_[u] = cr[u] * a - ci[u] * b; hi_[u] = cr[u] * b + ci[u] * a;
        }
        bf16x8 tv[4];
#pragma unroll
        for (int qt = 0; qt < 5; qt++) {
          u32x4 o[2][2];
          if (qt < 4) {
#pragma unroll
            for (int mm = 0; mm < 2; mm++)
#pragma unroll
              for (int u = 0; u < 2; u++) {
                const int m = qt * 2 + mm;
                const f32x2 l2 = {lr[u], lr[u]}, i2 = {li[u], li[u]};
                const f32x2 r0_ = fma2(-i2, hi_[u], fma2(l2, hr_[u], lo2(acc[m][u])));
                const f32x2 i0_ = fma2(i2, hr_[u], fma2(l2, hi_[u], lo2(acc[m][2 + u])));
                hr_[u] = fma2(-i2, i0_, fma2(l2, r0_, hi2(acc[m][u])));
                hi_[u] = fma2(i2, r0_, fma2(l2, i0_, hi2(acc[m][2 + u])));
                o[0][u][mm] = pack2(r0_.x, hr_[u].x); o[0][u][2 + mm] = pack2(r0_.y, hr_[u].y);
                o[1][u][mm] = pack2(-i0_.x, -hi_[u].x); o[1][u][2 + mm] = pack2(-i0_.y, -hi_[u].y);
              }
          }
          SCHED();
          if (qt > 0) {
#pragma unroll
            for (int k = 0; k < 4; k++) __builtin_amdgcn_raw_buffer_store_b128(__builtin_bit_cast(u32x4, tv[k]), hr, vso[(k & 1) * 4 + qt - 1] + (k >> 1) * 1024, 0, SC1);
          }
          if (qt < 4) {
#pragma unroll
            for (int part = 0; part < 2; part++) {
              asm volatile("" ::: "memory");
              *(LAS u32x4*)twp = o[part][0];
              *(LAS u32x4*)(twp + 1024) = o[part][1];
              asm volatile("" ::: "memory");
#pragma unroll
              for (int rho = 0; rho < 2; rho++) tv[part * 2 + rho] = ldtr(trp[rho], trp[rho ^ 1] + 256);
            }
          }
          SCHED();
        }
      } else {
        WAITV(0);
        BARRIER();
        tag = *(LAS u64*)(lds + C_TAG) + TAG_STEP;
        if (role == 2) {
          const u32x4 gwv = *(const u32x4*)(p.gw + lane * 8);
          f32x2 gwf[4];
#pragma unroll
          for (int e = 0; e < 4; e++) gwf[e] = (f32x2){__uint_as_float(gwv[e] << 16), __uint_as_float(gwv[e] & 0xffff0000u)};
          const bf16x2 ones = {(__bf16)1.0f, (__bf16)1.0f};
#pragma unroll 1
          for (int it = 0; it < 4; it++) {
            const int r0 = rt * 256 + wid * 64 + it * 16;
            u32x4 xw[16];
#pragma unroll
            for (int r = 0; r < 16; r++) xw[r] = *(const u32x4*)(p.x + (size_t)(r0 + r) * D + lane * 8);
            float s[16];
#pragma unroll
            for (int r = 0; r < 16; r++) {
              s[r] = 0.f;
#pragma unroll
              for (int e = 0; e < 4; e++) {
                const unsigned w = xw[r][e];
                const f32x2 pr = (f32x2){__uint_as_float(w << 16), __uint_as_float(w & 0xffff0000u)} * gwf[e];
                const bf16x2 pb = {(__bf16)pr.x, (__bf16)pr.y};
                s[r] = __builtin_amdgcn_fdot2_f32_bf16(pb, ones, s[r], false);
              }
            }
#pragma unroll
            for (int r = 0; r < 8; r++) {
              const auto t = __builtin_amdgcn_permlane32_swap(__float_as_uint(s[r]), __float_as_uint(s[r + 8]), false, false);
              s[r] = __uint_as_float(t[0]) + __uint_as_float(t[1]);
            }
#pragma unroll
            for (int r = 0; r < 4; r++) {
              const auto t = __builtin_amdgcn_permlane16_swap(__float_as_uint(s[r]), __float_as_uint(s[r + 4]), false, false);
              s[r] = __uint_as_float(t[0]) + __uint_as_float(t[1]);
            }
#pragma unroll
            for (int r = 0; r < 4; r++) {
              s[r] += __uint_as_float(__builtin_amdgcn_update_dpp(0, __float_as_uint(s[r]), 0x128, 0xf, 0xf, false));
              s[r] += __uint_as_float(__builtin_amdgcn_update_dpp(0, __float_as_uint(s[r]), 0x124, 0xf, 0xf, false));
              s[r] += __uint_as_float(__builtin_amdgcn_update_dpp(0, __float_as_uint(s[r]), 0x122, 0xf, 0xf, false));
              s[r] += __uint_as_float(__builtin_amdgcn_update_dpp(0, __float_as_uint(s[r]), 0x121, 0xf, 0xf, false));
            }
            const float sv = (n & 2) ? ((n & 1) ? s[3] : s[2]) : ((n & 1) ? s[1] : s[0]);
            __builtin_amdgcn_raw_buffer_store_b64((u32x2){__float_as_uint(rbf(sigmoidf_(rbf(sv)))), (unsigned)tag}, wsr,
                                                  n < 4 ? (r0 + n + 4 * (g & 1) + 8 * (g >> 1)) * 8 : OOR, (unsigned)G_OFF, SC1);
          }
        }
      }
    }
    lds_set(lds + C_FA + wid * 4);
    {
      asm volatile("" ::: "memory");
      SCHED();
      const u64 fv = ld_flag(wsr, vfi, FA_OFF);
      ld_final();
      set_vh();
      SCHED(); grab_w(); SCHED();
      WAITV(6);
      st_flag(wsr, vfo, FB_OFF, tag);
      if (!__all(fv == tag)) poll_flags(wsr, vfi, FA_OFF, tag);
    }
    asm volatile("" ::: "memory");
    bf16x8 hf[56];
#pragma unroll
    for (int q = 0; q < DE; q++) hf[q] = ldh(gstep(q >> 2), q & 3);
    SCHED(); prep_final();
#pragma unroll
    for (int c = 0; c < 4; c++)
#pragma unroll
      for (int m = 0; m < 4; m++) acc2[c][m] = (f32x4){0.f, 0.f, 0.f, 0.f};
    LAS char* pw[4];
    SCHED();
    u64 fbv = 0;
#pragma unroll
    for (int k = 0; k < 14; k++) {
      if (k == CHKB - 2) fbv = ld_flag(wsr, vfi, FB_OFF);
      if (k == CHKB) {
        if (!__all(fbv == tag)) poll_flags(wsr, vfi, FB_OFF, tag);
        asm volatile("" ::: "memory");
      }
      if (k == 5) lds_wait(lds + C_F2 + wid * 4);
      if (k == 8) lds_wait(lds + C_F2 + wid * 4, 2u);
      if (k == 9) lds_set(lds + C_F3 + wid * 4);
      if (k == 12) {
        int ln = lane;
        PIN(ln);
#pragma unroll
        for (int c = 0; c < 4; c++) { pw[c] = lds + wid * 12288 + c * 1024 + (ln >> 4) * 256 + (((ln & 15) ^ (4 * c + (ln >> 4))) << 4); PIN(pw[c]); }
      }
#pragma unroll
      for (int m = 0; m < 4; m++) {
        if (k + 1 >= 5 && k + 1 < 14) wf[(k + 1) % 6][m] = ldw(k + 1, m);
#pragma unroll
        for (int c_ = 0; c_ < 4; c_++) {
          const int c = (m & 1) ? 3 - c_ : c_;
          acc2[c][m] = __builtin_amdgcn_mfma_f32_16x16x32_bf16(wf[k % 6][c], hf[k * 4 + m], acc2[c][m], 0, 0, 0);
        }
        if (k * 4 + m + DE < 28) hf[k * 4 + m + DE] = ldh(gstep((k * 4 + m + DE) >> 2), (m + DE) & 3);
        if (k * 4 + m + DO >= 28 && k * 4 + m + DO < 56) hf[k * 4 + m + DO] = ldh(gstep((k * 4 + m + DO) >> 2), (m + DO) & 3);
        if (k == 13 && m > 0) {
#pragma unroll
          for (int c = 0; c < 4; c++) *(LAS f32x4*)(pw[c] + wslot(m - 1)) = acc2[c][m - 1];
        }
        SCHED();
      }
    }
#pragma unroll
    for (int c = 0; c < 4; c++) *(LAS f32x4*)(pw[c] + wslot(3)) = acc2[c][3];
  }
  BARRIER();
  {
    const rsrc_t orc = mkrsrc(p.out, L * D * 4);
#pragma unroll
    for (int u = 0; u < 2; u++) {
      f32x4 b = *(LAS f32x4*)sb[u], a = sk[u] + *(LAS f32x4*)sa[u];
#pragma unroll
      for (int w = 1; w < 4; w++) {
        b += *(LAS f32x4*)(sb[u] + w * 12288);
        a += *(LAS f32x4*)(sa[u] + w * 12288);
      }
      const f32x4 o = gv[u] * a + omg[u] * b;
      __builtin_amdgcn_raw_buffer_store_b128(__builtin_bit_cast(u32x4, o), orc, vo + u * 8192, 0, SC1 | NT);
    }
  }
  asm volatile(".Lgm_code_end:");
}

extern "C" void kernel_launch(void* const* d_in, const int* in_sizes, int n_in, void* d_out, int out_size, void* d_ws,
                              size_t ws_size, hipStream_t stream) {
  Params p;
  p.x = (const u16*)d_in[0]; p.lre = (const u16*)d_in[1]; p.lim = (const u16*)d_in[2];
  p.bre = (const u16*)d_in[3]; p.bim = (const u16*)d_in[4]; p.cre = (const u16*)d_in[5]; p.cim = (const u16*)d_in[6];
  p.ldt = (const u16*)d_in[7]; p.dsk = (const u16*)d_in[8];
  p.wg = (const u16*)d_in[10]; p.wu = (const u16*)d_in[11]; p.wd = (const u16*)d_in[12]; p.gw = (const u16*)d_in[13];
  p.ws = (char*)d_ws; p.out = (float*)d_out;
  (void)hipFuncSetAttribute((const void*)k_fused, hipFuncAttributeMaxDynamicSharedMemorySize, LDS_BYTES);
  k_fused<<<256, 512, LDS_BYTES, stream>>>(p);
}
```
